```python
import torch

def _softmax(x, axis=-1):
    return torch.softmax(x, dim=axis)

def _gelu(x):
    return torch.nn.functional.gelu(x, approximate='tanh')

def _silu(x):
    return torch.nn.functional.silu(x)

def _sigmoid(x):
    return torch.sigmoid(x)

def _layernorm(x, axis=-1, eps=1e-05):
    m = x.mean(dim=axis, keepdim=True)
    v = x.var(dim=axis, keepdim=True, unbiased=False)
    return (x - m) / torch.sqrt(v + eps)

def _scatter_add(tgt, idx, src, axis=0):
    return tgt.clone().index_add_(axis, idx, src)

def _segment_sum(src, seg, n):
    out = torch.zeros((n,) + src.shape[1:], dtype=src.dtype)
    return out.index_add_(0, seg, src)

def _pad(x, pw):
    flat = tuple((v for pair in reversed(pw) for v in pair))
    return torch.nn.functional.pad(x, flat)
kernel_name = 'interleave_layers__sliding_block__memory_aug_recurrent_mem_tr__prompt'

def setup_inputs():

    _w = lambda a: (lambda t: t.to(torch.bfloat16) if t.is_floating_point() else t)(torch.as_tensor(a))

    def _A_setup():
        B, S, D = (1, 2048, 448)
        return {'x': _w((lambda *s: torch.randn(*s, dtype=torch.bfloat16) * 0.05)(B, S, D)), 'wqkv': _w((lambda *s: torch.randn(*s, dtype=torch.bfloat16) * 0.05)(D, 3 * D)), 'wo': _w((lambda *s: torch.randn(*s, dtype=torch.bfloat16) * 0.05)(D, D))}
    _A_in = _A_setup()

    def _B_setup():
        B, S, Dm = (1, 2048, 448)
        H, Dh = (16, 256)
        M, seg = (8, 32)
        seg = min(seg, S)
        S = S // seg * seg
        return {'x': _w((lambda *s: torch.randn(*s, dtype=torch.bfloat16) * 0.05)(B, S, Dm)), 'mem0': _w((lambda *s: torch.randn(*s, dtype=torch.bfloat16) * 0.8)(B, M, Dm)), 'Wq': _w((lambda *s: torch.randn(*s, dtype=torch.bfloat16) * 1.6)(Dm, H, Dh) * Dm ** (-0.5)), 'Wk': _w((lambda *s: torch.randn(*s, dtype=torch.bfloat16) * 1.6)(Dm, H, Dh) * Dm ** (-0.5)), 'Wv': _w((lambda *s: torch.randn(*s, dtype=torch.bfloat16) * 0.8)(Dm, H, Dh) * Dm ** (-0.5)), 'Wo': _w((lambda *s: torch.randn(*s, dtype=torch.bfloat16) * 0.8)(H, Dh, Dm) * (H * Dh) ** (-0.5)), 'seg_len': seg}
    _B_in = _B_setup()
    return {'A_x': _A_in['x'], 'A_wqkv': _A_in['wqkv'], 'A_wo': _A_in['wo'], 'B_x': _B_in['x'], 'B_mem0': _B_in['mem0'], 'B_Wq': _B_in['Wq'], 'B_Wk': _B_in['Wk'], 'B_Wv': _B_in['Wv'], 'B_Wo': _B_in['Wo'], 'B_seg_len': _B_in['seg_len']}

def reference(A_x, A_wqkv, A_wo, B_x, B_mem0, B_Wq, B_Wk, B_Wv, B_Wo, B_seg_len):

    def _A_ref(x, wqkv, wo):
        B, S, D = x.shape
        H = 16
        Dh = D // H
        W = 256
        qkv = (lambda x, *d: x.permute(*d))((x @ wqkv).reshape(B, S, 3, H, Dh), 2, 0, 3, 1, 4)
        q, k, v = (qkv[0], qkv[1], qkv[2])
        scores = torch.einsum('bhqd,bhkd->bhqk', q, k) * Dh ** (-0.5)
        idx = torch.arange(S)
        mask = (idx[:, None] - idx[None, :] >= 0) & (idx[:, None] - idx[None, :] < W)
        scores = torch.where(mask[None, None], scores, -1000000000.0)
        p = _softmax(scores, axis=-1)
        o = torch.einsum('bhqk,bhkd->bhqd', p, v)
        return (lambda x, *d: x.permute(*d))(o, 0, 2, 1, 3).reshape(B, S, D) @ wo

    def _B_ref(x, mem0, Wq, Wk, Wv, Wo, seg_len):
        B, S, Dm = x.shape
        H, Dh = (Wq.shape[1], Wq.shape[2])
        M = mem0.shape[1]
        scale = Dh ** (-0.5)
        n_seg = S // seg_len
        L = M + seg_len
        idx = torch.arange(L)
        mask = (idx[:, None] < M) | (idx[:, None] >= idx[None, :])
        mem = mem0
        outs = []
        for s in range(n_seg):
            xs = x[:, s * seg_len:(s + 1) * seg_len, :].clone()
            h = torch.concatenate([mem, xs], axis=1)
            q = torch.einsum('btd,dhe->bhte', h, Wq)
            k = torch.einsum('btd,dhe->bhte', h, Wk)
            v = torch.einsum('btd,dhe->bhte', h, Wv)
            scores = torch.einsum('bhqe,bhke->bhqk', q, k) * scale
            scores = torch.where(mask[None, None], scores, -1000000000.0)
            p = _softmax(scores, axis=-1)
            o = torch.einsum('bhqk,bhke->bhqe', p, v)
            o = torch.einsum('bhte,hed->btd', o, Wo)
            mem = o[:, :M, :].clone()
            outs.append(o[:, M:, :])
        return torch.concatenate(outs, axis=1)
    x = A_x
    x = x + _A_ref(_layernorm(x, axis=-1), A_wqkv, A_wo)
    x = x + _B_ref(_layernorm(x, axis=-1), B_mem0, B_Wq, B_Wk, B_Wv, B_Wo, B_seg_len)
    x = x + _A_ref(_layernorm(x, axis=-1), A_wqkv, A_wo)
    x = x + _B_ref(_layernorm(x, axis=-1), B_mem0, B_Wq, B_Wk, B_Wv, B_Wo, B_seg_len)
    return x
```

```cpp
#include <hip/hip_runtime.h>
#include <hip/hip_bf16.h>

typedef unsigned v4u __attribute__((ext_vector_type(4)));
typedef unsigned v2u __attribute__((ext_vector_type(2)));
typedef float f4 __attribute__((ext_vector_type(4)));
typedef float f2 __attribute__((ext_vector_type(2)));
typedef short s8v __attribute__((ext_vector_type(8)));
typedef short s4v __attribute__((ext_vector_type(4)));
typedef __bf16 b2v __attribute__((ext_vector_type(2)));
typedef __amdgpu_buffer_rsrc_t rsrc_t;
#define DEVI __device__ __forceinline__
#define RLX_AGENT __ATOMIC_RELAXED, __HIP_MEMORY_SCOPE_AGENT

#define LAG 6
constexpr int NSEG = 64, KS = 14, NT = NSEG + LAG;
constexpr unsigned SENT = 0xFFFFFFFFu;
constexpr unsigned SPINCAP = 1u << 20;
constexpr float LOG2E = 1.4426950408889634f;

constexpr unsigned KB = 1024u;
constexpr unsigned FLAG_OFF = 0;
constexpr unsigned FLAG_BYTES = 256 * KB;
constexpr unsigned CH_OFF = FLAG_OFF + FLAG_BYTES;
constexpr unsigned MEM_T = 14 * KB, PM_H = 384, PM_T = 16 * PM_H, EX_T = 16 * KB, XS_H = 128, XS_T = 16 * XS_H;
constexpr unsigned TICK_BYTES = MEM_T + PM_T + EX_T + XS_T;
constexpr unsigned CH_BYTES = (NT + 1) * TICK_BYTES;
constexpr unsigned WG_OFF = (CH_OFF + CH_BYTES + 4095u) & ~4095u;
constexpr unsigned WG_BYTES = 16 * 14 * 2 * 14 * KB;
constexpr unsigned WU_OFF = WG_OFF + WG_BYTES;
constexpr unsigned WQ_OFF = WU_OFF + WG_BYTES;
constexpr unsigned WO_OFF = WQ_OFF + 16 * 6 * 14 * KB;
constexpr unsigned SEGX = 28 * KB;
constexpr unsigned XN_OFF = WO_OFF + 392 * KB;
constexpr unsigned XR_OFF = XN_OFF + 2 * NSEG * SEGX;
constexpr unsigned X2_OFF = XR_OFF + 2 * NSEG * SEGX;
constexpr unsigned OA_OFF = X2_OFF + NSEG * SEGX;
constexpr unsigned G_OFF = OA_OFF + 2 * NSEG * SEGX;
constexpr unsigned SEGG = 448 * KB;
constexpr unsigned U_OFF = G_OFF + 2 * NSEG * SEGG;
constexpr unsigned ETT_OFF = U_OFF + 2 * NSEG * SEGG;
constexpr unsigned ETS_OFF = ETT_OFF + 2 * NSEG * 32 * KB;
constexpr unsigned STM_OFF = ETS_OFF + 2 * NSEG * 4 * KB;
constexpr unsigned ZB_OFF = STM_OFF + 2 * NSEG * 16 * KB;
constexpr unsigned WS_END = ZB_OFF + 2 * NSEG * 112 * KB;
static_assert(WS_END < 256u * 1024u * 1024u, "workspace");

constexpr unsigned F_ABORT = 0;
constexpr unsigned F_XN = 8;
constexpr unsigned F_G = F_XN + 128;
constexpr unsigned F_ETT = F_G + 128;
constexpr unsigned F_STM = F_ETT + 128;
constexpr unsigned F_OA = F_STM + 128;
constexpr unsigned F_X2 = F_OA + 128;
constexpr unsigned F_U = F_X2 + 64;
constexpr unsigned F_Z = F_U + 128;
static_assert((F_Z + 128) * 128 <= FLAG_BYTES, "flags");

DEVI f4 mfma32(v4u a, v4u b, f4 c) {
  return __builtin_amdgcn_mfma_f32_16x16x32_bf16(__builtin_bit_cast(s8v, a), __builtin_bit_cast(s8v, b), c, 0, 0, 0);
}
DEVI f4 mfma16(v2u a, v2u b, f4 c) {
  return __builtin_amdgcn_mfma_f32_16x16x16bf16_1k(__builtin_bit_cast(s4v, a), __builtin_bit_cast(s4v, b), c, 0, 0, 0);
}
DEVI unsigned pk2(float a, float b) {
  f2 v = {a, b};
  return __builtin_bit_cast(unsigned, __builtin_convertvector(v, b2v));
}
DEVI float bflo(unsigned u) { return __builtin_bit_cast(float, u << 16); }
DEVI float bfhi(unsigned u) { return __builtin_bit_cast(float, u & 0xFFFF0000u); }
DEVI float rbf(float a) { return bflo(pk2(a, 0.f)); }
DEVI v4u pk8(f4 a, f4 b) { return v4u{pk2(a[0], a[1]), pk2(a[2], a[3]), pk2(b[0], b[1]), pk2(b[2], b[3])}; }
DEVI v2u pk4(f4 a) { return v2u{pk2(a[0], a[1]), pk2(a[2], a[3])}; }
DEVI v4u resid8(v4u x, f4 a, f4 b) {
  return v4u{pk2(bflo(x[0]) + rbf(a[0]), bfhi(x[0]) + rbf(a[1])), pk2(bflo(x[1]) + rbf(a[2]), bfhi(x[1]) + rbf(a[3])),
             pk2(bflo(x[2]) + rbf(b[0]), bfhi(x[2]) + rbf(b[1])), pk2(bflo(x[3]) + rbf(b[2]), bfhi(x[3]) + rbf(b[3]))};
}
DEVI unsigned nosent(unsigned x) { return x == SENT ? 0x7FC07FC0u : x; }
DEVI v4u nosent4(v4u x) { return v4u{nosent(x[0]), nosent(x[1]), nosent(x[2]), nosent(x[3])}; }
DEVI v2u nosent2(v2u x) { return v2u{nosent(x[0]), nosent(x[1])}; }
DEVI bool ok4(v4u v) { return (v[0] != SENT) & (v[1] != SENT) & (v[2] != SENT) & (v[3] != SENT); }
DEVI bool ok2(v2u v) { return (v[0] != SENT) & (v[1] != SENT); }
DEVI float ex2(float x) { return __builtin_amdgcn_exp2f(x); }
DEVI float rcp(float x) { return __builtin_amdgcn_rcpf(x); }
DEVI float shx(float v, int m) { return __shfl_xor(v, m, 64); }

DEVI v4u ld16(rsrc_t r, unsigned off) { return __builtin_amdgcn_raw_buffer_load_b128(r, off, 0, 0); }
DEVI v4u ld16c(rsrc_t r, unsigned off) { return __builtin_amdgcn_raw_buffer_load_b128(r, off, 0, 16); }
DEVI v2u ld8c(rsrc_t r, unsigned off) { return __builtin_amdgcn_raw_buffer_load_b64(r, off, 0, 16); }
DEVI unsigned ld4c(rsrc_t r, unsigned off) { return __builtin_amdgcn_raw_buffer_load_b32(r, off, 0, 16); }
DEVI void st16c(rsrc_t r, unsigned off, v4u v) { __builtin_amdgcn_raw_buffer_store_b128(v, r, off, 0, 16); }
DEVI void st8c(rsrc_t r, unsigned off, v2u v) { __builtin_amdgcn_raw_buffer_store_b64(v, r, off, 0, 16); }
DEVI void st4c(rsrc_t r, unsigned off, unsigned v) { __builtin_amdgcn_raw_buffer_store_b32(v, r, off, 0, 16); }
#define PIN_A(x) asm volatile("" : "+a"(x))
#define DRAIN() asm volatile("s_waitcnt vmcnt(0)" ::: "memory")
#define CBAR() asm volatile("" ::: "memory")

struct Ctx {
  unsigned char* ws;
  rsrc_t rs;
  int tid, lane, wave;
};
DEVI unsigned* flagp(const Ctx& c, unsigned idx) { return (unsigned*)(c.ws + FLAG_OFF + idx * 128u); }
DEVI void set_abort(const Ctx& c) { __hip_atomic_store(flagp(c, F_ABORT), 1u, RLX_AGENT); }
DEVI bool aborted(const Ctx& c) { return __builtin_amdgcn_readfirstlane(__hip_atomic_load(flagp(c, F_ABORT), RLX_AGENT)) != 0u; }
DEVI bool spin_fail(const Ctx& c, unsigned& spins) {
  ++spins;
  if (spins > SPINCAP) { set_abort(c); return true; }
  if ((spins & 1023u) == 0u && aborted(c)) return true;
  return false;
}
DEVI bool wait_ge(const Ctx& c, unsigned* word, unsigned want) {
  for (unsigned spins = 0;;) {
    CBAR();
    unsigned v = __builtin_amdgcn_readfirstlane(__hip_atomic_load(word, RLX_AGENT));
    if (v >= want) return true;
    __builtin_amdgcn_s_sleep(1);
    if (spin_fail(c, spins)) return false;
  }
}
DEVI bool wait_words(const Ctx& c, unsigned* line, int first, int n) {
  const int idx = first + (c.lane < n ? c.lane : 0);
  for (unsigned spins = 0;;) {
    CBAR();
    unsigned v = __hip_atomic_load(line + idx, RLX_AGENT);
    if (__all(v != 0u)) return true;
    __builtin_amdgcn_s_sleep(1);
    if (spin_fail(c, spins)) return false;
  }
}
DEVI void flag_add(unsigned* word) { __hip_atomic_fetch_add(word, 1u, RLX_AGENT); }
DEVI void flag_set(unsigned* word) { __hip_atomic_store(word, 1u, RLX_AGENT); }

DEVI unsigned mem_off(int t) { return CH_OFF + (unsigned)t * TICK_BYTES; }
DEVI unsigned pm_off(int t, int h) { return mem_off(t) + MEM_T + (unsigned)h * PM_H; }
DEVI unsigned ex_off(int t, int h) { return mem_off(t) + MEM_T + PM_T + (unsigned)h * KB; }
DEVI unsigned xs_off(int t, int h) { return mem_off(t) + MEM_T + PM_T + EX_T + (unsigned)h * XS_H; }
DEVI int tok_of(int tau, int m) { return 8 * (m >> 2) + 4 * tau + (m & 3); }

DEVI bool gather_mem(const Ctx& c, int t, unsigned char* lds_mem, volatile unsigned* lds_fail) {
  const unsigned base = mem_off(t) + c.tid * 16;
  const unsigned o3 = mem_off(t) + 12288 + (c.tid & 127) * 16;
  v4u v0, v1, v2, v3;
  bool good = true;
  for (unsigned spins = 0;;) {
    CBAR();
    v0 = ld16c(c.rs, base);
    v1 = ld16c(c.rs, base + 4096);
    v2 = ld16c(c.rs, base + 8192);
    v3 = ld16c(c.rs, o3);
    const bool ok = ok4(v0) & ok4(v1) & ok4(v2) & ok4(v3);
    if (__all(ok)) break;
    if (spin_fail(c, spins)) { good = false; break; }
  }
  *(v4u*)(lds_mem + c.tid * 16) = v0;
  *(v4u*)(lds_mem + 4096 + c.tid * 16) = v1;
  *(v4u*)(lds_mem + 8192 + c.tid * 16) = v2;
  if (c.tid < 128) *(v4u*)(lds_mem + 12288 + c.tid * 16) = v3;
  if (!good) *lds_fail = 1u;
  __syncthreads();
  return *lds_fail == 0u;
}

DEVI void role_H(const Ctx& c, int h, unsigned char* lds) {
  const int lane = c.lane, w = c.wave;
  const int i16 = lane & 15, q = lane >> 4;
  unsigned char* lmem = lds;
  f4* red = (f4*)(lds + 14 * KB);
  volatile unsigned* lfail = (volatile unsigned*)(lds + 30 * KB);
  v4u W[98];
  {
    const unsigned b0 = WG_OFF + (unsigned)(h * 14 + 3 * w) * 28 * KB + lane * 16;
#pragma unroll
    for (int f = 0; f < 84; ++f) W[f] = ld16(c.rs, b0 + f * KB);
    const unsigned b1 = WG_OFF + (unsigned)(h * 14 + 12 + (w >> 1)) * 28 * KB + (w & 1) * 7 * KB + lane * 16;
#pragma unroll
    for (int k = 0; k < 7; ++k) { W[84 + k] = ld16(c.rs, b1 + k * KB); W[91 + k] = ld16(c.rs, b1 + (14 + k) * KB); }
#pragma unroll
    for (int f = 0; f < 58; ++f) PIN_A(W[f]);
  }
  const int ux = 12 + (w >> 1), kx = 7 * (w & 1);
  if (c.tid == 0) *lfail = 0u;
  __syncthreads();
  for (int t = 0; t < NT; ++t) {
    if (!gather_mem(c, t, lmem, lfail)) return;
    v4u tb[4];
    f4 sacc = {0.f, 0.f, 0.f, 0.f};
#pragma unroll
    for (int p = 0; p < 3; ++p) {
      f4 a0 = {0.f, 0.f, 0.f, 0.f}, a1 = {0.f, 0.f, 0.f, 0.f};
#pragma unroll
      for (int ks = 0; ks < 14; ++ks) {
        const v4u m = *(const v4u*)(lmem + ks * KB + lane * 16);
        a0 = mfma32(W[p * 28 + ks], m, a0);
        a1 = mfma32(W[p * 28 + 14 + ks], m, a1);
      }
      tb[p] = pk8(a0, a1);
      const v4u mu = *(const v4u*)(lmem + (3 * w + p) * KB + lane * 16);
      sacc = mfma32(tb[p], mu, sacc);
    }
    {
      f4 a0 = {0.f, 0.f, 0.f, 0.f}, a1 = {0.f, 0.f, 0.f, 0.f};
#pragma unroll
      for (int k = 0; k < 7; ++k) {
        const v4u m = *(const v4u*)(lmem + (kx + k) * KB + lane * 16);
        a0 = mfma32(W[84 + k], m, a0);
        a1 = mfma32(W[91 + k], m, a1);
      }
      tb[3] = pk8(a0, a1);
      const v4u mu = *(const v4u*)(lmem + ux * KB + lane * 16);
      sacc = mfma32(tb[3], mu, sacc);
    }
    red[w * 64 + lane] = sacc;
    __syncthreads();
    if (w == 0) {
      f4 s = red[lane] + red[64 + lane] + red[128 + lane] + red[192 + lane];
      v2u st;
      bool good = true;
      for (unsigned spins = 0;;) {
        CBAR();
        st = ld8c(c.rs, xs_off(t, h) + i16 * 8);
        if (__all(ok2(st))) break;
        if (spin_fail(c, spins)) { good = false; break; }
      }
      if (!good) *lfail = 1u;
      const float mx = __uint_as_float(st.x), lx = __uint_as_float(st.y);
      float mm = fmaxf(fmaxf(s[0], s[1]), fmaxf(s[2], s[3]));
      mm = fmaxf(mm, shx(mm, 16));
      const float m = fmaxf(mm, mx);
      f4 e;
#pragma unroll
      for (int r = 0; r < 4; ++r) e[r] = ex2((s[r] - m) * LOG2E);
      float lm = (e[0] + e[1]) + (e[2] + e[3]);
      lm += shx(lm, 16);
      const float ax = ex2((mx - m) * LOG2E);
      const float inv = 1.0f / (lm + ax * lx);
      const bool valid = (q >> 1) == (i16 >> 3);
      if (valid) {
        st8c(c.rs, pm_off(t, h) + i16 * 16 + (q & 1) * 8, nosent2(pk4(e * inv)));
        if (!(q & 1)) st4c(c.rs, pm_off(t, h) + 256 + i16 * 4, nosent(__builtin_bit_cast(unsigned, ax * inv)));
      }
    }
    const int us[4] = {3 * w, 3 * w + 1, 3 * w + 2, ux};
    __syncthreads();
#pragma unroll
    for (int l = 0; l < 2; ++l) {
      const int s = t - l * LAG;
      f4 a[2] = {{0.f, 0.f, 0.f, 0.f}, {0.f, 0.f, 0.f, 0.f}};
      if (s >= 0 && s < NSEG) {
        const unsigned xb = XN_OFF + (unsigned)(l * NSEG + s) * SEGX + lane * 16;
        v4u x[8];
#pragma unroll
        for (int tau = 0; tau < 2; ++tau)
#pragma unroll
          for (int p = 0; p < 4; ++p) x[tau * 4 + p] = ld16c(c.rs, xb + (tau * 14 + us[p]) * KB);
#pragma unroll
        for (int tau = 0; tau < 2; ++tau)
#pragma unroll
          for (int p = 0; p < 4; ++p) a[tau] = mfma32(tb[p], x[tau * 4 + p], a[tau]);
      }
      red[((l * 2 + 0) * 4 + w) * 64 + lane] = a[0];
      red[((l * 2 + 1) * 4 + w) * 64 + lane] = a[1];
    }
    __syncthreads();
    {
      const int l = w >> 1, tau = w & 1, s = t - l * LAG;
      if (s >= 0 && s < NSEG) {
        const f4 v = red[(w * 4 + 0) * 64 + lane] + red[(w * 4 + 1) * 64 + lane] + red[(w * 4 + 2) * 64 + lane] + red[(w * 4 + 3) * 64 + lane];
        if ((q >> 1) == l)
          st16c(c.rs, STM_OFF + (unsigned)((l * NSEG + s) * 16 + h) * KB + (tau * 16 + i16) * 32 + (q & 1) * 16, __builtin_bit_cast(v4u, v));
        DRAIN();
        if (lane == 0) flag_add(flagp(c, F_STM + l * NSEG + s));
      }
    }
    if (*lfail) return;
    __syncthreads();
  }
}

DEVI void role_X(const Ctx& c, int h) {
  const int lane = c.lane, w = c.wave;
  const int i16 = lane & 15, q = lane >> 4;
  const int l = w >> 1, tq = w & 1;
  for (int t = 0; t < NT; ++t) {
    const int s = t - l * LAG;
    const bool act = (s >= 0 && s < NSEG);
    if (!act && tq == 1) continue;
    v4u G[28];
    if (act) {
      if (!wait_ge(c, flagp(c, F_G + l * NSEG + s), 28)) return;
      const unsigned gb = G_OFF + (unsigned)(l * NSEG + s) * SEGG + (unsigned)h * 28 * KB + lane * 16;
      const unsigned xb = XN_OFF + (unsigned)(l * NSEG + s) * SEGX + (unsigned)tq * 14 * KB + lane * 16;
      v4u X[14];
#pragma unroll
      for (int f = 0; f < 28; ++f) G[f] = ld16c(c.rs, gb + f * KB);
#pragma unroll
      for (int f = 0; f < 14; ++f) X[f] = ld16c(c.rs, xb + f * KB);
      f4 a0 = {0.f, 0.f, 0.f, 0.f}, a1 = {0.f, 0.f, 0.f, 0.f};
#pragma unroll
      for (int ks = 0; ks < 14; ++ks) {
        a0 = mfma32(G[ks], X[ks], a0);
        a1 = mfma32(G[14 + ks], X[ks], a1);
      }
      const int tqk = tok_of(tq, i16);
      float m = -3.0e38f;
#pragma unroll
      for (int r = 0; r < 4; ++r) {
        if (8 * q + r > tqk) a0[r] = -3.0e38f;
        if (8 * q + 4 + r > tqk) a1[r] = -3.0e38f;
        m = fmaxf(m, fmaxf(a0[r], a1[r]));
      }
      m = fmaxf(m, shx(m, 16));
      m = fmaxf(m, shx(m, 32));
      float lsum = 0.f;
#pragma unroll
      for (int r = 0; r < 4; ++r) {
        a0[r] = (8 * q + r > tqk) ? 0.f : ex2((a0[r] - m) * LOG2E);
        a1[r] = (8 * q + 4 + r > tqk) ? 0.f : ex2((a1[r] - m) * LOG2E);
        lsum += a0[r] + a1[r];
      }
      lsum += shx(lsum, 16);
      lsum += shx(lsum, 32);
      st16c(c.rs, ETT_OFF + (unsigned)(((l * NSEG + s) * 16 + h) * 2 + tq) * KB + lane * 16, pk8(a0, a1));
      if (q == 0)
        st8c(c.rs, ETS_OFF + (unsigned)((l * NSEG + s) * 16 + h) * 256 + (tq * 16 + i16) * 8,
             v2u{__builtin_bit_cast(unsigned, m), __builtin_bit_cast(unsigned, lsum)});
      DRAIN();
      if (lane == 0) flag_add(flagp(c, F_ETT + l * NSEG + s));
    }
    if (tq == 1) continue;
    const bool mine = (i16 >> 3) == l;
    if (act) {
      v4u M[14];
      const unsigned mb = mem_off(t) + (unsigned)(q * 16 + (l * 8 + (i16 & 7))) * 16;
      for (unsigned spins = 0;;) {
        CBAR();
        bool ok = true;
#pragma unroll
        for (int ks = 0; ks < 14; ++ks) M[ks] = ld16c(c.rs, mb + ks * KB);
#pragma unroll
        for (int ks = 0; ks < 14; ++ks) ok &= ok4(M[ks]);
        if (__all(ok)) break;
        if (spin_fail(c, spins)) return;
      }
      f4 a0 = {0.f, 0.f, 0.f, 0.f}, a1 = {0.f, 0.f, 0.f, 0.f};
#pragma unroll
      for (int ks = 0; ks < 14; ++ks) {
        a0 = mfma32(G[ks], M[ks], a0);
        a1 = mfma32(G[14 + ks], M[ks], a1);
      }
      float m = fmaxf(fmaxf(fmaxf(a0[0], a0[1]), fmaxf(a0[2], a0[3])), fmaxf(fmaxf(a1[0], a1[1]), fmaxf(a1[2], a1[3])));
      m = fmaxf(m, shx(m, 16));
      m = fmaxf(m, shx(m, 32));
      float lsum = 0.f;
#pragma unroll
      for (int r = 0; r < 4; ++r) {
        a0[r] = ex2((a0[r] - m) * LOG2E);
        a1[r] = ex2((a1[r] - m) * LOG2E);
        lsum += a0[r] + a1[r];
      }
      lsum += shx(lsum, 16);
      lsum += shx(lsum, 32);
      if (mine) {
        if (q == 0) st8c(c.rs, xs_off(t, h) + i16 * 8, nosent2(v2u{__builtin_bit_cast(unsigned, m), __builtin_bit_cast(unsigned, lsum)}));
        st16c(c.rs, ex_off(t, h) + lane * 16, nosent4(pk8(a0, a1)));
      }
    } else if (mine) {
      if (q == 0) st8c(c.rs, xs_off(t, h) + i16 * 8, v2u{__builtin_bit_cast(unsigned, -1.0e30f), 0u});
      st16c(c.rs, ex_off(t, h) + lane * 16, v4u{0u, 0u, 0u, 0u});
    }
  }
}

DEVI void role_C(const Ctx& c, int j, unsigned char* lds) {
  const int lane = c.lane, w = c.wave;
  const int i16 = lane & 15, q = lane >> 4;
  unsigned char* lmem = lds;
  f4* red = (f4*)(lds + 14 * KB);
  volatile unsigned* lfail = (volatile unsigned*)(lds + 30 * KB);
  v4u W[56];
  {
    const unsigned b0 = WU_OFF + (unsigned)(j * 16 + 4 * w) * 14 * KB + lane * 16;
#pragma unroll
    for (int f = 0; f < 56; ++f) W[f] = ld16(c.rs, b0 + f * KB);
#pragma unroll
    for (int f = 0; f < 56; ++f) PIN_A(W[f]);
  }
  const unsigned slot = (unsigned)(j >> 1) * KB + (unsigned)((2 * (j & 1) + ((c.tid >> 4) & 1)) * 16 + i16) * 16;
  const v4u m0 = ld16(c.rs, mem_off(0) + slot);
  if (c.tid == 0) *lfail = 0u;
  __syncthreads();
  const bool pvalid = (q >> 1) == (i16 >> 3);
  for (int t = 0; t < NT; ++t) {
    v4u U[8];
    bool good = true;
#pragma unroll
    for (int l = 0; l < 2; ++l) {
      const int s = t - l * LAG;
      if (s >= 0 && s < NSEG) {
        if (!wait_words(c, flagp(c, F_U + l * NSEG + s), j, 1)) good = false;
        const unsigned ub = U_OFF + (unsigned)(l * NSEG + s) * SEGG + (unsigned)(j * 16 + 4 * w) * KB + lane * 16;
#pragma unroll
        for (int hh = 0; hh < 4; ++hh) U[l * 4 + hh] = ld16c(c.rs, ub + hh * KB);
      } else {
#pragma unroll
        for (int hh = 0; hh < 4; ++hh) U[l * 4 + hh] = v4u{0u, 0u, 0u, 0u};
      }
    }
    if (!good) *lfail = 1u;
    if (!gather_mem(c, t, lmem, lfail)) return;
    v2u zb[4];
    {
      f4 z[4];
#pragma unroll
      for (int hh = 0; hh < 4; ++hh) z[hh] = f4{0.f, 0.f, 0.f, 0.f};
#pragma unroll
      for (int ks = 0; ks < 14; ++ks) {
        const v4u m = *(const v4u*)(lmem + ks * KB + lane * 16);
#pragma unroll
        for (int hh = 0; hh < 4; ++hh) z[hh] = mfma32(m, W[hh * 14 + ks], z[hh]);
      }
      const int l = q >> 1, s = t - l * LAG;
#pragma unroll
      for (int hh = 0; hh < 4; ++hh) {
        zb[hh] = pk4(z[hh]);
        if (s >= 0 && s < NSEG)
          st8c(c.rs, ZB_OFF + (unsigned)(((l * NSEG + s) * 28 + j) * 4 + w) * KB + (hh * 16 + i16) * 16 + (q & 1) * 8, zb[hh]);
      }
    }
    f4 ox[4];
    {
      v4u E[4];
      for (unsigned spins = 0;;) {
        CBAR();
        bool ok = true;
#pragma unroll
        for (int hh = 0; hh < 4; ++hh) E[hh] = ld16c(c.rs, ex_off(t, 4 * w + hh) + lane * 16);
#pragma unroll
        for (int hh = 0; hh < 4; ++hh) ok &= ok4(E[hh]);
        if (__all(ok)) break;
        if (spin_fail(c, spins)) { *lfail = 1u; break; }
      }
      const v4u zero = {0u, 0u, 0u, 0u};
#pragma unroll
      for (int hh = 0; hh < 4; ++hh) {
        const v4u e0 = (i16 < 8) ? E[hh] : zero, e1 = (i16 < 8) ? zero : E[hh];
        ox[hh] = mfma32(U[hh], e0, f4{0.f, 0.f, 0.f, 0.f});
        ox[hh] = mfma32(U[4 + hh], e1, ox[hh]);
      }
    }
    {
      v2u P[4];
      unsigned wx[4];
      for (unsigned spins = 0;;) {
        CBAR();
        bool ok = true;
#pragma unroll
        for (int hh = 0; hh < 4; ++hh) {
          P[hh] = ld8c(c.rs, pm_off(t, 4 * w + hh) + i16 * 16 + (q & 1) * 8);
          wx[hh] = ld4c(c.rs, pm_off(t, 4 * w + hh) + 256 + i16 * 4);
        }
#pragma unroll
        for (int hh = 0; hh < 4; ++hh) ok &= ok2(P[hh]) & (wx[hh] != SENT);
        if (__all(ok)) break;
        if (spin_fail(c, spins)) { *lfail = 1u; break; }
      }
      f4 acc = {0.f, 0.f, 0.f, 0.f};
#pragma unroll
      for (int hh = 0; hh < 4; ++hh) {
        const v2u p = pvalid ? P[hh] : v2u{0u, 0u};
        acc = mfma16(zb[hh], p, acc);
      }
#pragma unroll
      for (int hh = 0; hh < 4; ++hh) acc += ox[hh] * __builtin_bit_cast(float, wx[hh]);
      red[w * 64 + lane] = acc;
    }
    __syncthreads();
    if (c.tid < 32) {
      const int hq = c.tid >> 4;
      f4 a = red[(2 * hq) * 16 + i16], b = red[(2 * hq + 1) * 16 + i16];
#pragma unroll
      for (int ww = 1; ww < 4; ++ww) { a += red[ww * 64 + (2 * hq) * 16 + i16]; b += red[ww * 64 + (2 * hq + 1) * 16 + i16]; }
      v4u o = nosent4(pk8(a, b));
      if (i16 >= 8 && t + 1 <= LAG) o = m0;
      st16c(c.rs, mem_off(t + 1) + slot, o);
    }
    DRAIN();
    __syncthreads();
    if (c.tid < 2) {
      const int l = c.tid, s = t - l * LAG;
      if (s >= 0 && s < NSEG) flag_set(flagp(c, F_Z + l * NSEG + s) + j);
    }
    if (*lfail) return;
  }
}

DEVI void role_K(const Ctx& c, int l, int k, unsigned char* lds) {
  const int lane = c.lane, w = c.wave;
  volatile unsigned* lfail = (volatile unsigned*)(lds + 30 * KB);
  const bool isG = k < 28;
  v4u W[56];
  {
    const unsigned b0 = isG ? WG_OFF + (unsigned)(8 * k + 2 * w) * 28 * KB + lane * 16
                            : WU_OFF + (unsigned)((k - 28) * 16 + 4 * w) * 14 * KB + lane * 16;
#pragma unroll
    for (int f = 0; f < 56; ++f) W[f] = ld16(c.rs, b0 + f * KB);
#pragma unroll
    for (int f = 0; f < 56; ++f) PIN_A(W[f]);
  }
  if (c.tid == 0) *lfail = 0u;
  __syncthreads();
  for (int s = 0; s < NSEG; ++s) {
    if (w == 0) { if (!wait_ge(c, flagp(c, F_XN + l * NSEG + s), 2)) *lfail = 1u; }
    __syncthreads();
    if (*lfail) return;
    {
      const unsigned xb = XN_OFF + (unsigned)(l * NSEG + s) * SEGX + c.tid * 16;
      v4u x[7];
#pragma unroll
      for (int f = 0; f < 7; ++f) x[f] = ld16c(c.rs, xb + f * 4096);
#pragma unroll
      for (int f = 0; f < 7; ++f) *(v4u*)(lds + f * 4096 + c.tid * 16) = x[f];
    }
    __syncthreads();
    v4u X[28];
#pragma unroll
    for (int f = 0; f < 28; ++f) X[f] = *(const v4u*)(lds + f * KB + lane * 16);
    if (isG) {
#pragma unroll
      for (int p = 0; p < 2; ++p) {
        const int P = 8 * k + 2 * w + p, h = P / 14, u = P - h * 14;
#pragma unroll
        for (int tau = 0; tau < 2; ++tau) {
          f4 a0 = {0.f, 0.f, 0.f, 0.f}, a1 = {0.f, 0.f, 0.f, 0.f};
#pragma unroll
          for (int ks = 0; ks < 14; ++ks) {
            a0 = mfma32(W[p * 28 + ks], X[tau * 14 + ks], a0);
            a1 = mfma32(W[p * 28 + 14 + ks], X[tau * 14 + ks], a1);
          }
          st16c(c.rs, G_OFF + (unsigned)(l * NSEG + s) * SEGG + (unsigned)((h * 2 + tau) * 14 + u) * KB + lane * 16, pk8(a0, a1));
        }
      }
    } else {
#pragma unroll
      for (int hh = 0; hh < 4; ++hh) {
        f4 a0 = {0.f, 0.f, 0.f, 0.f}, a1 = {0.f, 0.f, 0.f, 0.f};
#pragma unroll
        for (int ks = 0; ks < 14; ++ks) {
          a0 = mfma32(X[ks], W[hh * 14 + ks], a0);
          a1 = mfma32(X[14 + ks], W[hh * 14 + ks], a1);
        }
        st16c(c.rs, U_OFF + (unsigned)(l * NSEG + s) * SEGG + (unsigned)((k - 28) * 16 + 4 * w + hh) * KB + lane * 16, pk8(a0, a1));
      }
    }
    DRAIN();
    __syncthreads();
    if (c.tid == 0) {
      if (isG) flag_add(flagp(c, F_G + l * NSEG + s));
      else flag_set(flagp(c, F_U + l * NSEG + s) + (k - 28));
    }
  }
}

DEVI void role_O(const Ctx& c, int l, int cg, unsigned char* lds, __hip_bfloat16* out) {
  const int lane = c.lane, w = c.wave;
  const int i16 = lane & 15, q = lane >> 4;
  const int ct = 4 * cg + w;
  unsigned char* lE = lds;
  f2* lS = (f2*)(lds + 32 * KB);
  unsigned char* lP = lds + 36 * KB;
  float* lW = (float*)(lds + 44 * KB);
  volatile unsigned* lfail = (volatile unsigned*)(lds + 48 * KB);
  if (c.tid == 0) *lfail = 0u;
  __syncthreads();
  for (int s = 0; s < NSEG; ++s) {
    const unsigned ls = (unsigned)(l * NSEG + s);
    if (w == 0) { if (!wait_ge(c, flagp(c, F_ETT + ls), 32)) *lfail = 1u; }
    if (w == 1) { if (!wait_words(c, flagp(c, F_U + ls), 4 * cg, 4)) *lfail = 1u; }
    __syncthreads();
    if (*lfail) return;
    v4u U[16];
    {
      v4u e[8];
#pragma unroll
      for (int f = 0; f < 8; ++f) e[f] = ld16c(c.rs, ETT_OFF + ls * 32 * KB + f * 4096 + c.tid * 16);
      const v4u st = ld16c(c.rs, ETS_OFF + ls * 4 * KB + c.tid * 16);
#pragma unroll
      for (int h = 0; h < 16; ++h) U[h] = ld16c(c.rs, U_OFF + ls * SEGG + (unsigned)(ct * 16 + h) * KB + lane * 16);
#pragma unroll
      for (int f = 0; f < 8; ++f) *(v4u*)(lE + f * 4096 + c.tid * 16) = e[f];
      *(v4u*)((unsigned char*)lS + c.tid * 16) = st;
    }
    v2u xo[2];
#pragma unroll
    for (int tq = 0; tq < 2; ++tq)
      xo[tq] = ld8c(c.rs, XR_OFF + ls * SEGX + (unsigned)(tq * 14 + (ct >> 1)) * KB + (unsigned)((2 * (ct & 1) + (q >> 1)) * 16 + i16) * 16 + (q & 1) * 8);
    __syncthreads();
    f4 ott[32];
#pragma unroll
    for (int h = 0; h < 16; ++h)
#pragma unroll
      for (int tq = 0; tq < 2; ++tq)
        ott[h * 2 + tq] = mfma32(U[h], *(const v4u*)(lE + (h * 2 + tq) * KB + lane * 16), f4{0.f, 0.f, 0.f, 0.f});
    if (w == 0) { if (!wait_ge(c, flagp(c, F_STM + ls), 32)) *lfail = 1u; }
    if (w == 1) { if (!wait_words(c, flagp(c, F_Z + ls), 4 * cg, 4)) *lfail = 1u; }
    __syncthreads();
    if (*lfail) return;
    v4u Z[4];
    {
      v4u sv[4];
#pragma unroll
      for (int r = 0; r < 2; ++r) {
        const int row = c.tid + 256 * r;
        sv[2 * r] = ld16c(c.rs, STM_OFF + ls * 16 * KB + row * 32);
        sv[2 * r + 1] = ld16c(c.rs, STM_OFF + ls * 16 * KB + row * 32 + 16);
      }
#pragma unroll
      for (int g = 0; g < 4; ++g) Z[g] = ld16c(c.rs, ZB_OFF + ((ls * 28 + ct) * 4 + g) * KB + lane * 16);
#pragma unroll
      for (int r = 0; r < 2; ++r) {
        const int row = c.tid + 256 * r, h = row >> 5, tr = row & 31;
        f4 s0 = __builtin_bit_cast(f4, sv[2 * r]), s1 = __builtin_bit_cast(f4, sv[2 * r + 1]);
        const f2 st = lS[row];
        float m = fmaxf(fmaxf(fmaxf(s0[0], s0[1]), fmaxf(s0[2], s0[3])), fmaxf(fmaxf(s1[0], s1[1]), fmaxf(s1[2], s1[3])));
        m = fmaxf(m, st[0]);
        float lsum = 0.f;
#pragma unroll
        for (int e = 0; e < 4; ++e) {
          s0[e] = ex2((s0[e] - m) * LOG2E);
          s1[e] = ex2((s1[e] - m) * LOG2E);
          lsum += s0[e] + s1[e];
        }
        const float at = ex2((st[0] - m) * LOG2E);
        const float inv = 1.0f / (lsum + at * st[1]);
        *(v4u*)(lP + ((h >> 2) * 2 + (tr >> 4)) * KB + ((h & 3) * 16 + (tr & 15)) * 16) = pk8(s0 * inv, s1 * inv);
        lW[row] = at * inv;
      }
    }
    __syncthreads();
#pragma unroll
    for (int tq = 0; tq < 2; ++tq) {
      f4 acc = {0.f, 0.f, 0.f, 0.f};
#pragma unroll
      for (int g = 0; g < 4; ++g) acc = mfma32(Z[g], *(const v4u*)(lP + (g * 2 + tq) * KB + lane * 16), acc);
#pragma unroll
      for (int h = 0; h < 16; ++h) acc += ott[h * 2 + tq] * lW[h * 32 + tq * 16 + i16];
      f4 xn;
      xn[0] = bflo(xo[tq][0]) + rbf(acc[0]);
      xn[1] = bfhi(xo[tq][0]) + rbf(acc[1]);
      xn[2] = bflo(xo[tq][1]) + rbf(acc[2]);
      xn[3] = bfhi(xo[tq][1]) + rbf(acc[3]);
      const v2u o = pk4(xn);
      if (l == 0) {
        st8c(c.rs, X2_OFF + (unsigned)s * SEGX + (unsigned)(tq * 14 + (ct >> 1)) * KB + (unsigned)((2 * (ct & 1) + (q >> 1)) * 16 + i16) * 16 + (q & 1) * 8, o);
      } else {
        *(v2u*)((unsigned char*)out + (size_t)(s * 32 + tok_of(tq, i16)) * 896 + (ct * 16 + 4 * q) * 2) = o;
      }
    }
    if (l == 0) {
      DRAIN();
      __syncthreads();
      if (c.tid == 0) flag_add(flagp(c, F_X2 + s));
    } else {
      __syncthreads();
    }
  }
}

DEVI void role_AH(const Ctx& c, int l, int a, unsigned char* lds, const unsigned char* x0) {
  const int lane = c.lane, w = c.wave;
  const int i16 = lane & 15, q = lane >> 4;
  unsigned char* lX = lds;
  unsigned char* lK = lds + 28 * KB;
  unsigned char* lV = lds + 46 * KB;
  unsigned char* lQ = lds + 64 * KB;
  float* lst = (float*)(lds + 66 * KB);
  f4* lmg = (f4*)(lds + 67 * KB);
  f2* lms = (f2*)(lds + 71 * KB);
  volatile unsigned* lfail = (volatile unsigned*)(lds + 72 * KB);
  v4u W[28];
  {
    const unsigned b0 = WQ_OFF + (unsigned)(a * 6 + (w < 2 ? 2 * w : 2 + w)) * 14 * KB + lane * 16;
#pragma unroll
    for (int f = 0; f < 14; ++f) W[f] = ld16(c.rs, b0 + f * KB);
#pragma unroll
    for (int f = 14; f < 28; ++f) W[f] = (w < 2) ? ld16(c.rs, b0 + f * KB) : v4u{0u, 0u, 0u, 0u};
  }
  if (c.tid == 0) *lfail = 0u;
  __syncthreads();
  const int tau = w & 1, kh = w >> 1;
  const float cs = 0.18898223650461362f * LOG2E;
  for (int s = 0; s < NSEG; ++s) {
    if (l == 1) {
      if (w == 0) { if (!wait_ge(c, flagp(c, F_X2 + s), 7)) *lfail = 1u; }
      __syncthreads();
      if (*lfail) return;
    }
    {
      v4u x[7];
      if (l == 0) {
        const unsigned char* p = x0 + (size_t)(s * 32 + tok_of(tau, i16)) * 896 + (kh * 7 * 32 + 8 * q) * 2;
#pragma unroll
        for (int f = 0; f < 7; ++f) x[f] = *(const v4u*)(p + f * 64);
      } else {
        const unsigned xb = X2_OFF + (unsigned)s * SEGX + (unsigned)(tau * 14 + kh * 7) * KB + lane * 16;
#pragma unroll
        for (int f = 0; f < 7; ++f) x[f] = ld16c(c.rs, xb + f * KB);
      }
      float sum = 0.f;
#pragma unroll
      for (int f = 0; f < 7; ++f)
#pragma unroll
        for (int e = 0; e < 4; ++e) sum += bflo(x[f][e]) + bfhi(x[f][e]);
      sum += shx(sum, 16);
      sum += shx(sum, 32);
      if (q == 0) lst[w * 16 + i16] = sum;
      __syncthreads();
      const float mean = (lst[tau * 16 + i16] + lst[(2 + tau) * 16 + i16]) * (1.0f / 448.0f);
      float sq = 0.f;
#pragma unroll
      for (int f = 0; f < 7; ++f)
#pragma unroll
        for (int e = 0; e < 4; ++e) {
          const float d0 = bflo(x[f][e]) - mean, d1 = bfhi(x[f][e]) - mean;
          sq += d0 * d0 + d1 * d1;
        }
      sq += shx(sq, 16);
      sq += shx(sq, 32);
      if (q == 0) lst[64 + w * 16 + i16] = sq;
      __syncthreads();
      const float var = (lst[64 + tau * 16 + i16] + lst[64 + (2 + tau) * 16 + i16]) * (1.0f / 448.0f);
      const float rstd = 1.0f / sqrtf(var + 1e-5f);
#pragma unroll
      for (int f = 0; f < 7; ++f) {
        v4u o;
#pragma unroll
        for (int e = 0; e < 4; ++e) o[e] = pk2((bflo(x[f][e]) - mean) * rstd, (bfhi(x[f][e]) - mean) * rstd);
        *(v4u*)(lX + (tau * 14 + kh * 7 + f) * KB + lane * 16) = o;
      }
    }
    __syncthreads();
    const int slot = s % 9;
    if (w < 2) {
#pragma unroll
      for (int tt = 0; tt < 2; ++tt) {
        f4 a0 = {0.f, 0.f, 0.f, 0.f}, a1 = {0.f, 0.f, 0.f, 0.f};
#pragma unroll
        for (int ks = 0; ks < 14; ++ks) {
          const v4u x = *(const v4u*)(lX + (tt * 14 + ks) * KB + lane * 16);
          a0 = mfma32(W[ks], x, a0);
          a1 = mfma32(W[14 + ks], x, a1);
        }
        unsigned char* dst = (w == 0) ? lQ + tt * KB : lK + (slot * 2 + tt) * KB;
        *(v4u*)(dst + lane * 16) = pk8(a0, a1);
      }
    } else {
      f4 a0 = {0.f, 0.f, 0.f, 0.f}, a1 = {0.f, 0.f, 0.f, 0.f};
#pragma unroll
      for (int ks = 0; ks < 14; ++ks) {
        a0 = mfma32(*(const v4u*)(lX + ks * KB + lane * 16), W[ks], a0);
        a1 = mfma32(*(const v4u*)(lX + (14 + ks) * KB + lane * 16), W[ks], a1);
      }
      *(v4u*)(lV + (slot * 2 + (w - 2)) * KB + lane * 16) = pk8(a0, a1);
    }
    __syncthreads();
    {
      const v4u qf = *(const v4u*)(lQ + tau * KB + lane * 16);
      const int iq = tok_of(tau, i16);
      f4 sc[5][2];
      float m = -3.0e38f;
#pragma unroll
      for (int dd = 0; dd < 5; ++dd) {
        const int d = kh * 5 + dd;
        const bool on = (d <= 8) && (s - d >= 0);
        if (on) {
          const int sl = (s - d) % 9;
          sc[dd][0] = mfma32(*(const v4u*)(lK + (sl * 2 + 0) * KB + lane * 16), qf, f4{0.f, 0.f, 0.f, 0.f});
          sc[dd][1] = mfma32(*(const v4u*)(lK + (sl * 2 + 1) * KB + lane * 16), qf, f4{0.f, 0.f, 0.f, 0.f});
        }
#pragma unroll
        for (int hf = 0; hf < 2; ++hf)
#pragma unroll
          for (int r = 0; r < 4; ++r) {
            const int ik = 8 * q + 4 * hf + r;
            bool v = on;
            if (d == 0) v = v && (ik <= iq);
            if (d == 8) v = v && (ik > iq);
            sc[dd][hf][r] = v ? sc[dd][hf][r] : -3.0e38f;
            m = fmaxf(m, sc[dd][hf][r]);
          }
      }
      m = fmaxf(m, shx(m, 16));
      m = fmaxf(m, shx(m, 32));
      float lsum = 0.f;
      f4 o0 = {0.f, 0.f, 0.f, 0.f}, o1 = {0.f, 0.f, 0.f, 0.f};
#pragma unroll
      for (int dd = 0; dd < 5; ++dd) {
        const int d = kh * 5 + dd;
        const bool on = (d <= 8) && (s - d >= 0);
        if (on) {
#pragma unroll
          for (int hf = 0; hf < 2; ++hf)
#pragma unroll
            for (int r = 0; r < 4; ++r) {
              const float e = (sc[dd][hf][r] < -1.0e38f) ? 0.f : ex2((sc[dd][hf][r] - m) * cs);
              sc[dd][hf][r] = e;
              lsum += e;
            }
          const v4u p = pk8(sc[dd][0], sc[dd][1]);
          const int sl = (s - d) % 9;
          o0 = mfma32(*(const v4u*)(lV + (sl * 2 + 0) * KB + lane * 16), p, o0);
          o1 = mfma32(*(const v4u*)(lV + (sl * 2 + 1) * KB + lane * 16), p, o1);
        }
      }
      lsum += shx(lsum, 16);
      lsum += shx(lsum, 32);
      if (kh == 1) {
        lmg[(tau * 2 + 0) * 64 + lane] = o0;
        lmg[(tau * 2 + 1) * 64 + lane] = o1;
        lms[tau * 64 + lane] = f2{m, lsum};
      }
      __syncthreads();
      if (kh == 0) {
        const f2 ot = lms[tau * 64 + lane];
        const float mt = fmaxf(m, ot[0]);
        const float fa = ex2((m - mt) * cs), fb = (ot[1] > 0.f) ? ex2((ot[0] - mt) * cs) : 0.f;
        const float inv = 1.0f / (lsum * fa + ot[1] * fb);
        const f4 r0 = (o0 * fa + lmg[(tau * 2 + 0) * 64 + lane] * fb) * inv;
        const f4 r1 = (o1 * fa + lmg[(tau * 2 + 1) * 64 + lane] * fb) * inv;
        const unsigned ob = OA_OFF + (unsigned)(l * NSEG + s) * SEGX + (unsigned)tau * 14 * KB;
        {
          const int col = 28 * a + 4 * q;
          st8c(c.rs, ob + (col >> 5) * KB + (((col >> 3) & 3) * 16 + i16) * 16 + (col & 4) * 2, pk4(r0));
        }
        if (q < 3) {
          const int col = 28 * a + 16 + 4 * q;
          st8c(c.rs, ob + (col >> 5) * KB + (((col >> 3) & 3) * 16 + i16) * 16 + (col & 4) * 2, pk4(r1));
        }
        DRAIN();
        if (lane == 0) flag_add(flagp(c, F_OA + l * NSEG + s));
      }
    }
  }
}

DEVI void role_AO(const Ctx& c, int l, int tau, unsigned char* lds, const unsigned char* x0) {
  const int lane = c.lane, w = c.wave;
  const int i16 = lane & 15, q = lane >> 4;
  unsigned char* lO = lds;
  f4* lx = (f4*)(lds + 14 * KB);
  float* lst = (float*)(lds + 18 * KB);
  volatile unsigned* lfail = (volatile unsigned*)(lds + 30 * KB);
  unsigned char* lWx = lds + 32 * KB + w * 14 * KB;
  v4u W[84];
  {
    const unsigned b0 = WO_OFF + (unsigned)(3 * w) * 28 * KB + lane * 16;
#pragma unroll
    for (int f = 0; f < 84; ++f) W[f] = ld16(c.rs, b0 + f * KB);
    const unsigned b1 = WO_OFF + (unsigned)(12 + (w >> 1)) * 28 * KB + (w & 1) * 7 * KB + lane * 16;
#pragma unroll
    for (int k = 0; k < 7; ++k) {
      *(v4u*)(lWx + k * KB + lane * 16) = ld16(c.rs, b1 + k * KB);
      *(v4u*)(lWx + (7 + k) * KB + lane * 16) = ld16(c.rs, b1 + (14 + k) * KB);
    }
#pragma unroll
    for (int f = 0; f < 56; ++f) PIN_A(W[f]);
  }
  const int ux = 12 + (w >> 1), kx = 7 * (w & 1);
  const bool own4 = !(w & 1);
  if (c.tid == 0) *lfail = 0u;
  __syncthreads();
  for (int s = 0; s < NSEG; ++s) {
    const unsigned ls = (unsigned)(l * NSEG + s);
    if (w == 0) { if (!wait_ge(c, flagp(c, F_OA + ls), 32)) *lfail = 1u; }
    __syncthreads();
    if (*lfail) return;
    {
      const unsigned ob = OA_OFF + ls * SEGX + (unsigned)tau * 14 * KB;
      const v4u v0 = ld16c(c.rs, ob + c.tid * 16), v1 = ld16c(c.rs, ob + 4096 + c.tid * 16), v2 = ld16c(c.rs, ob + 8192 + c.tid * 16);
      const v4u v3 = ld16c(c.rs, ob + 12288 + (c.tid & 127) * 16);
      *(v4u*)(lO + c.tid * 16) = v0;
      *(v4u*)(lO + 4096 + c.tid * 16) = v1;
      *(v4u*)(lO + 8192 + c.tid * 16) = v2;
      if (c.tid < 128) *(v4u*)(lO + 12288 + c.tid * 16) = v3;
    }
    v4u xr[4];
#pragma unroll
    for (int p = 0; p < 4; ++p) {
      const int u = (p < 3) ? 3 * w + p : ux;
      if (l == 0) xr[p] = *(const v4u*)(x0 + (size_t)(s * 32 + tok_of(tau, i16)) * 896 + (u * 32 + 8 * q) * 2);
      else xr[p] = ld16c(c.rs, X2_OFF + (unsigned)s * SEGX + (unsigned)(tau * 14 + u) * KB + lane * 16);
    }
    __syncthreads();
    v4u xb[4];
    float sum = 0.f;
    f4 s0 = {0.f, 0.f, 0.f, 0.f}, s1 = {0.f, 0.f, 0.f, 0.f};
#pragma unroll
    for (int k = 0; k < 7; ++k) {
      const v4u m = *(const v4u*)(lO + (kx + k) * KB + lane * 16);
      s0 = mfma32(*(const v4u*)(lWx + k * KB + lane * 16), m, s0);
      s1 = mfma32(*(const v4u*)(lWx + (7 + k) * KB + lane * 16), m, s1);
    }
    if (!own4) { lx[((w >> 1) * 2 + 0) * 64 + lane] = s0; lx[((w >> 1) * 2 + 1) * 64 + lane] = s1; }
#pragma unroll
    for (int p = 0; p < 3; ++p) {
      f4 a0 = {0.f, 0.f, 0.f, 0.f}, a1 = {0.f, 0.f, 0.f, 0.f};
#pragma unroll
      for (int ks = 0; ks < 14; ++ks) {
        const v4u m = *(const v4u*)(lO + ks * KB + lane * 16);
        a0 = mfma32(W[p * 28 + ks], m, a0);
        a1 = mfma32(W[p * 28 + 14 + ks], m, a1);
      }
      xb[p] = resid8(xr[p], a0, a1);
#pragma unroll
      for (int e = 0; e < 4; ++e) sum += bflo(xb[p][e]) + bfhi(xb[p][e]);
    }
    __syncthreads();
    if (own4) {
      s0 += lx[((w >> 1) * 2 + 0) * 64 + lane];
      s1 += lx[((w >> 1) * 2 + 1) * 64 + lane];
      xb[3] = resid8(xr[3], s0, s1);
#pragma unroll
      for (int e = 0; e < 4; ++e) sum += bflo(xb[3][e]) + bfhi(xb[3][e]);
    } else {
      xb[3] = v4u{0u, 0u, 0u, 0u};
    }
    sum += shx(sum, 16);
    sum += shx(sum, 32);
    if (q == 0) lst[w * 16 + i16] = sum;
    __syncthreads();
    const float mean = (lst[i16] + lst[16 + i16] + lst[32 + i16] + lst[48 + i16]) * (1.0f / 448.0f);
    float sq = 0.f;
#pragma unroll
    for (int p = 0; p < 4; ++p)
      if (p < 3 || own4)
#pragma unroll
        for (int e = 0; e < 4; ++e) {
          const float d0 = bflo(xb[p][e]) - mean, d1 = bfhi(xb[p][e]) - mean;
          sq += d0 * d0 + d1 * d1;
        }
    sq += shx(sq, 16);
    sq += shx(sq, 32);
    if (q == 0) lst[64 + w * 16 + i16] = sq;
    __syncthreads();
    const float var = (lst[64 + i16] + lst[80 + i16] + lst[96 + i16] + lst[112 + i16]) * (1.0f / 448.0f);
    const float rstd = 1.0f / sqrtf(var + 1e-5f);
#pragma unroll
    for (int p = 0; p < 4; ++p)
      if (p < 3 || own4) {
        const int u = (p < 3) ? 3 * w + p : ux;
        v4u o;
#pragma unroll
        for (int e = 0; e < 4; ++e) o[e] = pk2((bflo(xb[p][e]) - mean) * rstd, (bfhi(xb[p][e]) - mean) * rstd);
        st16c(c.rs, XN_OFF + ls * SEGX + (unsigned)(tau * 14 + u) * KB + lane * 16, o);
        st16c(c.rs, XR_OFF + ls * SEGX + (unsigned)(tau * 14 + u) * KB + lane * 16, xb[p]);
      }
    DRAIN();
    __syncthreads();
    if (c.tid == 0) flag_add(flagp(c, F_XN + ls));
  }
}

__global__ void __launch_bounds__(256, 1) mega_kernel(unsigned char* ws, const unsigned char* x0, __hip_bfloat16* out) {
  extern __shared__ __attribute__((aligned(16))) unsigned char lds[];
  Ctx c;
  c.ws = ws;
  c.rs = __builtin_amdgcn_make_buffer_rsrc(ws, 0, WS_END, 0x00020000);
  c.tid = threadIdx.x;
  c.lane = threadIdx.x & 63;
  c.wave = __builtin_amdgcn_readfirstlane(threadIdx.x >> 6);
  const int b = blockIdx.x, x = b & 7, r = b >> 3;
  if (x == 0) {
    if (r < 16) role_H(c, r, lds); else role_X(c, r - 16);
  } else if (x == 1) {
    if (r < 28) role_C(c, r, lds); else role_AO(c, (r - 28) >> 1, (r - 28) & 1, lds, x0);
  } else if (x < 6) {
    const int l = (x - 2) >> 1, i = ((x - 2) & 1) * 32 + r;
    if (i < 56) role_K(c, l, i, lds); else if (i < 63) role_O(c, l, i - 56, lds, out);
  } else if (x == 6) {
    role_AH(c, r >> 4, r & 15, lds, x0);
  }
}

__global__ void __launch_bounds__(256) prologue_kernel(unsigned char* ws, const unsigned short* wqkv, const unsigned short* wo,
                                                       const unsigned short* mem0, const unsigned short* Wq, const unsigned short* Wk,
                                                       const unsigned short* Wv, const unsigned short* Wo) {
  __shared__ __attribute__((aligned(16))) unsigned short lt[64 * 264];
  const int b = blockIdx.x, tid = threadIdx.x, lane = tid & 63, w = tid >> 6;
  const int i16 = lane & 15, q = lane >> 4;
  if (b < 224) {
    const bool isG = b < 112;
    const int bb = isG ? b : b - 112, h = bb / 7, cg = bb % 7;
    v4u B[4][8];
    if (isG) {
#pragma unroll
      for (int nt = 0; nt < 4; ++nt) {
        const int cc = 64 * cg + 32 * (nt >> 1) + 8 * (i16 >> 2) + 4 * (nt & 1) + (i16 & 3);
#pragma unroll
        for (int es = 0; es < 8; ++es) B[nt][es] = *(const v4u*)(Wq + ((size_t)cc * 16 + h) * 256 + es * 32 + 8 * q);
      }
    } else {
      for (int idx = tid; idx < 256 * 8; idx += 256) {
        const int e = idx >> 3, c8 = idx & 7;
        const v4u v = *(const v4u*)(Wo + ((size_t)h * 256 + e) * 448 + 64 * cg + 8 * c8);
#pragma unroll
        for (int k = 0; k < 4; ++k) {
          lt[(8 * c8 + 2 * k) * 264 + e] = (unsigned short)(v[k] & 0xFFFFu);
          lt[(8 * c8 + 2 * k + 1) * 264 + e] = (unsigned short)(v[k] >> 16);
        }
      }
      __syncthreads();
#pragma unroll
      for (int nt = 0; nt < 4; ++nt)
#pragma unroll
        for (int es = 0; es < 8; ++es) B[nt][es] = *(const v4u*)(lt + (16 * nt + i16) * 264 + es * 32 + 8 * q);
    }
    const unsigned short* Wa = isG ? Wk : Wv;
    const float sc = isG ? 0.0625f : 1.0f;
    for (int ks = w; ks < 14; ks += 4) {
      v4u A[2][8];
#pragma unroll
      for (int mt = 0; mt < 2; ++mt) {
        const int d = 32 * ks + 8 * (i16 >> 2) + 4 * mt + (i16 & 3);
#pragma unroll
        for (int es = 0; es < 8; ++es) A[mt][es] = *(const v4u*)(Wa + ((size_t)d * 16 + h) * 256 + es * 32 + 8 * q);
      }
#pragma unroll
      for (int nt = 0; nt < 4; ++nt) {
        f4 a0 = {0.f, 0.f, 0.f, 0.f}, a1 = {0.f, 0.f, 0.f, 0.f};
#pragma unroll
        for (int es = 0; es < 8; ++es) {
          a0 = mfma32(A[0][es], B[nt][es], a0);
          a1 = mfma32(A[1][es], B[nt][es], a1);
        }
        const v4u o = pk8(a0 * sc, a1 * sc);
        size_t off;
        if (isG) off = WG_OFF + (size_t)(((h * 14 + 2 * cg + (nt >> 1)) * 2 + (nt & 1)) * 14 + ks) * KB;
        else off = WU_OFF + (size_t)(((4 * cg + nt) * 16 + h) * 14 + ks) * KB;
        *(v4u*)(ws + off + lane * 16) = o;
      }
    }
  } else {
    const int nb = gridDim.x - 224, bb = b - 224;
    const int gtid = bb * 256 + tid, gstride = nb * 256;
    for (int idx = gtid; idx < 16 * 6 * 14 * 64; idx += gstride) {
      const int ln = idx & 63, f = idx >> 6, ks = f % 14, tl = (f / 14) % 6, a = f / 84;
      const int n = ln & 15, qq = ln >> 4;
      int dh, col;
      if (tl < 4) { dh = 8 * (n >> 2) + 4 * (tl & 1) + (n & 3); col = (tl >> 1) * 448 + 28 * a + dh; }
      else { dh = 16 * (tl - 4) + n; col = 896 + 28 * a + dh; }
      v4u o = {0u, 0u, 0u, 0u};
      if (dh < 28) {
#pragma unroll
        for (int jj = 0; jj < 4; ++jj) {
          const int d = 32 * ks + 8 * qq + 2 * jj;
          o[jj] = (unsigned)wqkv[(size_t)d * 1344 + col] | ((unsigned)wqkv[(size_t)(d + 1) * 1344 + col] << 16);
        }
      }
      *(v4u*)(ws + WQ_OFF + (size_t)idx * 16) = o;
    }
    for (int idx = gtid; idx < 14 * 2 * 14 * 64; idx += gstride) {
      const int ln = idx & 63, f = idx >> 6, ks = f % 14, tt = (f / 14) & 1, u = f / 28;
      const int n = ln & 15, qq = ln >> 4;
      const int col = 32 * u + 8 * (n >> 2) + 4 * tt + (n & 3);
      v4u o;
#pragma unroll
      for (int jj = 0; jj < 4; ++jj) {
        const int d = 32 * ks + 8 * qq + 2 * jj;
        o[jj] = (unsigned)wo[(size_t)d * 448 + col] | ((unsigned)wo[(size_t)(d + 1) * 448 + col] << 16);
      }
      *(v4u*)(ws + WO_OFF + (size_t)idx * 16) = o;
    }
    for (unsigned idx = gtid; idx < FLAG_BYTES / 16; idx += gstride) *(v4u*)(ws + FLAG_OFF + (size_t)idx * 16) = v4u{0u, 0u, 0u, 0u};
    for (unsigned idx = gtid; idx < CH_BYTES / 16; idx += gstride) {
      v4u o = {SENT, SENT, SENT, SENT};
      if (idx < MEM_T / 16) {
        const int ln = idx & 63, ks = idx >> 6;
        o = nosent4(*(const v4u*)(mem0 + (size_t)(ln & 7) * 448 + 32 * ks + 8 * (ln >> 4)));
      }
      *(v4u*)(ws + CH_OFF + (size_t)idx * 16) = o;
    }
  }
}

extern "C" void kernel_launch(void* const* d_in, const int* in_sizes, int n_in, void* d_out, int out_size, void* d_ws, size_t ws_size,
                              hipStream_t stream) {
  constexpr int LDS_BYTES = 96 * 1024;
  hipFuncSetAttribute((const void*)mega_kernel, hipFuncAttributeMaxDynamicSharedMemorySize, LDS_BYTES);
  unsigned char* ws = (unsigned char*)d_ws;
  prologue_kernel<<<256 + 32, 256, 0, stream>>>(ws, (const unsigned short*)d_in[1], (const unsigned short*)d_in[2], (const unsigned short*)d_in[4],
                                          (const unsigned short*)d_in[5], (const unsigned short*)d_in[6], (const unsigned short*)d_in[7],
                                          (const unsigned short*)d_in[8]);
  mega_kernel<<<256, 256, LDS_BYTES, stream>>>(ws, (const unsigned char*)d_in[0], (__hip_bfloat16*)d_out);
}
```

```cpp
#include <hip/hip_runtime.h>
#include <hip/hip_bf16.h>

typedef unsigned v4u __attribute__((ext_vector_type(4)));
typedef unsigned v2u __attribute__((ext_vector_type(2)));
typedef float f4 __attribute__((ext_vector_type(4)));
typedef float f2 __attribute__((ext_vector_type(2)));
typedef short s8v __attribute__((ext_vector_type(8)));
typedef __bf16 b2v __attribute__((ext_vector_type(2)));
typedef __amdgpu_buffer_rsrc_t rsrc_t;
#define DEVI __device__ __forceinline__
#define RLX_AGENT __ATOMIC_RELAXED, __HIP_MEMORY_SCOPE_AGENT

#define LAG 6
constexpr int NSEG = 64, NT = NSEG + LAG, AD = 4;
constexpr unsigned SENT = 0xFFFFFFFFu;
constexpr unsigned SPINCAP = 1u << 20;
constexpr float LOG2E = 1.4426950408889634f;
constexpr float NEG = -3.0e38f;

constexpr unsigned KB = 1024u, MB = 1024u * 1024u;
constexpr unsigned FLAG_BYTES = 128 * KB;
constexpr unsigned F_ABORT = 0, F_SIDE = 128;
constexpr unsigned F_U = 4 * KB;
constexpr unsigned RING_BYTES = 1 * MB;
constexpr unsigned L_MEMH = 0, L_MEMC = 64 * KB, L_MEMG = 128 * KB, L_S = 192 * KB, L_E = 256 * KB, L_XS = 384 * KB, L_PROBE = 504 * KB;
constexpr unsigned WG_OFF = 4 * MB;
constexpr unsigned WG_BYTES = 16 * 14 * 2 * 14 * KB;
constexpr unsigned WU_OFF = WG_OFF + WG_BYTES;
constexpr unsigned WQ_OFF = WU_OFF + WG_BYTES;
constexpr unsigned WO_OFF = WQ_OFF + 16 * 6 * 14 * KB;
constexpr unsigned SEGX = 28 * KB;
constexpr unsigned R_XN = 0, R_XR = SEGX, R_OA = 2 * SEGX, R_X2 = 3 * SEGX, R_ETT = 4 * SEGX, R_ETS = R_ETT + 32 * KB, R_STM = R_ETS + 4 * KB,
                   R_ZB = R_STM + 32 * KB, REC = R_ZB + 112 * KB;
constexpr unsigned REC_OFF = WO_OFF + 392 * KB;
constexpr unsigned SEGG = 448 * KB;
constexpr unsigned U_OFF = REC_OFF + 2 * NSEG * REC;
constexpr unsigned WS_END = U_OFF + 2 * NSEG * SEGG;
static_assert(WS_END < 200u * MB, "workspace");

DEVI f4 mfma32(v4u a, v4u b, f4 c) {
  return __builtin_amdgcn_mfma_f32_16x16x32_bf16(__builtin_bit_cast(s8v, a), __builtin_bit_cast(s8v, b), c, 0, 0, 0);
}
DEVI unsigned pk2(float a, float b) {
  f2 v = {a, b};
  return __builtin_bit_cast(unsigned, __builtin_convertvector(v, b2v));
}
DEVI float bflo(unsigned u) { return __uint_as_float(u << 16); }
DEVI float bfhi(unsigned u) { return __uint_as_float(u & 0xFFFF0000u); }
DEVI float rbf(float a) { return bflo(pk2(a, 0.f)); }
DEVI v4u pk8(f4 a, f4 b) { return v4u{pk2(a[0], a[1]), pk2(a[2], a[3]), pk2(b[0], b[1]), pk2(b[2], b[3])}; }
DEVI v2u pk4(f4 a) { return v2u{pk2(a[0], a[1]), pk2(a[2], a[3])}; }
DEVI v4u resid8(v4u x, f4 a, f4 b) {
  const v4u y = pk8(a, b);
  return v4u{pk2(bflo(x.x) + bflo(y.x), bfhi(x.x) + bfhi(y.x)), pk2(bflo(x.y) + bflo(y.y), bfhi(x.y) + bfhi(y.y)),
             pk2(bflo(x.z) + bflo(y.z), bfhi(x.z) + bfhi(y.z)), pk2(bflo(x.w) + bflo(y.w), bfhi(x.w) + bfhi(y.w))};
}
DEVI unsigned ns(unsigned x) { return min(x, 0xFFFFFFFEu); }
DEVI v4u ns4(v4u x) { return v4u{ns(x.x), ns(x.y), ns(x.z), ns(x.w)}; }
DEVI v2u ns2(v2u x) { return v2u{ns(x.x), ns(x.y)}; }
DEVI v4u f2u(f4 x) { return v4u{__float_as_uint(x[0]), __float_as_uint(x[1]), __float_as_uint(x[2]), __float_as_uint(x[3])}; }
DEVI f4 u2f(v4u x) { return f4{__uint_as_float(x.x), __uint_as_float(x.y), __uint_as_float(x.z), __uint_as_float(x.w)}; }
DEVI unsigned mx4(unsigned m, v4u v) { return max(max(m, max(v.x, v.y)), max(v.z, v.w)); }
DEVI unsigned mx2(unsigned m, v2u v) { return max(m, max(v.x, v.y)); }
DEVI bool anysent(unsigned m) { return __builtin_amdgcn_ballot_w64(m == SENT) != 0ull; }
DEVI float ex2(float x) { return __builtin_amdgcn_exp2f(x); }
DEVI float dot2(unsigned a, unsigned b, float c) {
  return __builtin_amdgcn_fdot2_f32_bf16(__builtin_bit_cast(b2v, a), __builtin_bit_cast(b2v, b), c, false);
}
DEVI void swap16(unsigned& a, unsigned& b) { auto r = __builtin_amdgcn_permlane16_swap(a, b, false, false); a = r[0]; b = r[1]; }
DEVI void swap32(unsigned& a, unsigned& b) { auto r = __builtin_amdgcn_permlane32_swap(a, b, false, false); a = r[0]; b = r[1]; }
DEVI float rmax4(float x) {
  unsigned a = __float_as_uint(x), b = a;
  swap16(a, b);
  a = b = __float_as_uint(fmaxf(__uint_as_float(a), __uint_as_float(b)));
  swap32(a, b);
  return fmaxf(__uint_as_float(a), __uint_as_float(b));
}
DEVI float rsum4(float x) {
  unsigned a = __float_as_uint(x), b = a;
  swap16(a, b);
  a = b = __float_as_uint(__uint_as_float(a) + __uint_as_float(b));
  swap32(a, b);
  return __uint_as_float(a) + __uint_as_float(b);
}
DEVI float ror8(float x) { return __uint_as_float(__builtin_amdgcn_update_dpp(0u, __float_as_uint(x), 0x128, 0xF, 0xF, false)); }
DEVI void rbcast4(float x, float* o) {
  unsigned a = __float_as_uint(x), b = a;
  swap16(a, b);
  unsigned a2 = a, b2 = b;
  swap32(a, a2);
  swap32(b, b2);
  o[0] = __uint_as_float(a); o[1] = __uint_as_float(b); o[2] = __uint_as_float(a2); o[3] = __uint_as_float(b2);
}

DEVI v4u ld16(rsrc_t r, unsigned off) { return __builtin_amdgcn_raw_buffer_load_b128(r, off, 0, 0); }
DEVI v4u ld16c(rsrc_t r, unsigned off) { return __builtin_amdgcn_raw_buffer_load_b128(r, off, 0, 16); }
DEVI v2u ld8c(rsrc_t r, unsigned off) { return __builtin_amdgcn_raw_buffer_load_b64(r, off, 0, 16); }
DEVI unsigned ld4c(rsrc_t r, unsigned off) { return __builtin_amdgcn_raw_buffer_load_b32(r, off, 0, 16); }
DEVI void st16c(rsrc_t r, unsigned off, v4u v) { __builtin_amdgcn_raw_buffer_store_b128(v, r, off, 0, 16); }
DEVI void st8c(rsrc_t r, unsigned off, v2u v) { __builtin_amdgcn_raw_buffer_store_b64(v, r, off, 0, 16); }
DEVI void st4c(rsrc_t r, unsigned off, unsigned v) { __builtin_amdgcn_raw_buffer_store_b32(v, r, off, 0, 16); }
#define PIN_A(x) asm volatile("" : "+a"(x))
#define DRAIN() asm volatile("s_waitcnt vmcnt(0)" ::: "memory")
#define CBAR() asm volatile("" ::: "memory")
#define SB() __builtin_amdgcn_sched_barrier(0)
#define BAR() do { asm volatile("s_waitcnt lgkmcnt(0)" ::: "memory"); __builtin_amdgcn_s_barrier(); asm volatile("" ::: "memory"); } while (0)
#define LDS16(p) (*(v4u*)(p))
#define LDSF4(p) (*(f4*)(p))
#define S4 (v4u{SENT, SENT, SENT, SENT})
#define S2 (v2u{SENT, SENT})
#define Z4 (f4{0.f, 0.f, 0.f, 0.f})

#define STAMP(it, e) do { } while (0)
struct Ctx {
  unsigned char* ws;
  rsrc_t rs;
  unsigned ring;
  int tid, lane, wave;
  bool dead;
};
DEVI bool give_up(Ctx& c, unsigned& spins) {
  if (c.dead) return true;
  ++spins;
  bool d = false;
  if (spins > SPINCAP) { __hip_atomic_store((unsigned*)(c.ws + F_ABORT), 1u, RLX_AGENT); d = true; }
  else if ((spins & 255u) == 0u) d = __builtin_amdgcn_readfirstlane(__hip_atomic_load((unsigned*)(c.ws + F_ABORT), RLX_AGENT)) != 0u;
  c.dead = d;
  return d;
}
#define POLL(BADEXPR, RELOAD, SLEEP) do { unsigned spins_ = 0; while (BADEXPR) { if (give_up(c, spins_)) break; if (SLEEP) __builtin_amdgcn_s_sleep(SLEEP); CBAR(); RELOAD; } } while (0)

DEVI unsigned rmap(const Ctx& c, unsigned lo, unsigned side) { return c.ring + ((lo >> 13) << 14) + (side << 13) + (lo & 8191u); }
DEVI unsigned rec_off(int l, int s) { return REC_OFF + (unsigned)(l * NSEG + s) * REC; }
DEVI int tok_of(int tau, int m) { return 8 * (m >> 2) + 4 * tau + (m & 3); }

DEVI void probe_side(Ctx& c, int g) {
  if (c.wave != 0) return;
  unsigned best[2] = {0xFFFFFFFFu, 0xFFFFFFFFu};
#pragma unroll 1
  for (int k = 0; k < 4; ++k) {
#pragma unroll
    for (int sd = 0; sd < 2; ++sd) {
      DRAIN();
      const unsigned t0 = (unsigned)__builtin_amdgcn_s_memtime();
      if (c.lane < 8) st16c(c.rs, rmap(c, L_PROBE + g * 256 + c.lane * 16, sd), S4);
      DRAIN();
      const unsigned t1 = (unsigned)__builtin_amdgcn_s_memtime();
      best[sd] = min(best[sd], t1 - t0);
    }
  }
  if (c.lane == 0) st4c(c.rs, F_SIDE + g * 128, best[1] < best[0] ? 2u : 1u);
}
DEVI unsigned get_sides(Ctx& c) {
  unsigned v = ld4c(c.rs, F_SIDE + (c.lane & 3) * 128);
  POLL(__builtin_amdgcn_ballot_w64(v == 0u) != 0ull, v = ld4c(c.rs, F_SIDE + (c.lane & 3) * 128), 2);
  return (unsigned)__builtin_amdgcn_ballot_w64(v == 2u) & 15u;
}

DEVI void role_H(Ctx& c, int h, int a, unsigned char* lds) {
  const int lane = c.lane, w = c.wave;
  const int i16 = lane & 15, q = lane >> 4;
  unsigned char* lmem = lds;
  unsigned char* lred = lds + 8 * KB;
  unsigned char* ltb = lds + 24 * KB;
  const int odd = w & 1;
  const int ks0 = (w >> 1) * 7 + odd * 4, ksx = (w >> 1) * 7 + 3;
  if (h == 0 && a == 0) probe_side(c, 0);
  v4u W[42], Wx[8];
#pragma unroll
  for (int lp = 0; lp < 7; ++lp) {
    const int gp = odd ? 6 - lp : lp;
    const unsigned b0 = WG_OFF + (unsigned)(h * 14 + 7 * a + gp) * 28 * KB + lane * 16;
#pragma unroll
    for (int t = 0; t < 2; ++t)
#pragma unroll
      for (int k = 0; k < 3; ++k) W[(lp * 2 + t) * 3 + k] = ld16(c.rs, b0 + (t * 14 + ks0 + k) * KB);
    if (lp < 4) {
#pragma unroll
      for (int t = 0; t < 2; ++t) {
        Wx[lp * 2 + t] = ld16(c.rs, b0 + (t * 14 + ksx) * KB);
        if (lp == 3 && t != odd) Wx[lp * 2 + t] = v4u{0u, 0u, 0u, 0u};
      }
    }
  }
#pragma unroll
  for (int f = 0; f < 42; ++f) PIN_A(W[f]);
#pragma unroll
  for (int f = 0; f < 8; ++f) PIN_A(Wx[f]);
  const unsigned sides = get_sides(c);
  unsigned mo[4];
#pragma unroll
  for (int k = 0; k < 4; ++k) mo[k] = rmap(c, L_MEMH + (k < 3 ? ks0 + k : ksx) * KB + lane * 16, sides & 1);
  const unsigned so = rmap(c, L_S + ((a * 2 + (q & 1)) * 16 + h) * 256 + i16 * 16, (sides >> 1) & 1);
  const bool sval = (q >> 1) == (i16 >> 3);
  const bool stager = (w >> 1) == a;
  const int gp1 = (w < 3) ? w + 4 : 3;
  v4u X[8];
  auto load_x = [&](int t) {
#pragma unroll
    for (int l = 0; l < 2; ++l) {
      const int s = min(max(t - l * LAG, 0), NSEG - 1);
      const bool on = (t - l * LAG >= 0) && (t - l * LAG < NSEG);
      const unsigned xb = rec_off(l, s) + R_XN + (unsigned)(7 * a) * KB + lane * 16;
#pragma unroll
      for (int tau = 0; tau < 2; ++tau) {
        X[(l * 2 + tau) * 2 + 0] = on ? ld16c(c.rs, xb + (tau * 14 + w) * KB) : v4u{0u, 0u, 0u, 0u};
        X[(l * 2 + tau) * 2 + 1] = on ? ld16c(c.rs, xb + (tau * 14 + gp1) * KB) : v4u{0u, 0u, 0u, 0u};
      }
    }
  };
  for (int t = 0; t < NT; ++t) {
    const unsigned slot = (unsigned)(t & 3) * 32 * KB;
    STAMP(t, 0);
    v4u m[4];
    CBAR();
#pragma unroll
    for (int k = 0; k < 4; ++k) m[k] = ld16c(c.rs, mo[k] + slot);
    POLL(anysent(mx4(mx4(mx4(mx4(0u, m[0]), m[1]), m[2]), m[3])),
         { _Pragma("unroll") for (int k = 0; k < 4; ++k) m[k] = ld16c(c.rs, mo[k] + slot); }, 0);
    STAMP(t, 1);
    if (stager) {
#pragma unroll
      for (int k = 0; k < 4; ++k) LDS16(lmem + ((k < 3 ? ks0 + k : ksx) - 7 * a) * KB + lane * 16) = m[k];
    }
    SB();
    v4u tb[7], mu[7];
    f4 acc[7][2];
    f4 sacc = Z4;
#pragma unroll
    for (int lp = 0; lp < 7; ++lp) {
      acc[lp][0] = mfma32(W[(lp * 2 + 0) * 3 + 0], m[0], Z4);
      acc[lp][1] = mfma32(W[(lp * 2 + 1) * 3 + 0], m[0], Z4);
      acc[lp][0] = mfma32(W[(lp * 2 + 0) * 3 + 1], m[1], acc[lp][0]);
      acc[lp][1] = mfma32(W[(lp * 2 + 1) * 3 + 1], m[1], acc[lp][1]);
      if (lp >= 2) { SB(); tb[lp - 2] = pk8(acc[lp - 2][0], acc[lp - 2][1]); SB(); }
      acc[lp][0] = mfma32(W[(lp * 2 + 0) * 3 + 2], m[2], acc[lp][0]);
      acc[lp][1] = mfma32(W[(lp * 2 + 1) * 3 + 2], m[2], acc[lp][1]);
      if (lp < 4) {
        acc[lp][0] = mfma32(Wx[lp * 2 + 0], m[3], acc[lp][0]);
        acc[lp][1] = mfma32(Wx[lp * 2 + 1], m[3], acc[lp][1]);
      }
      if (lp >= 2) sacc = mfma32(tb[lp - 2], mu[lp - 2], sacc);
      SB();
      if (lp == 0) {
        BAR();
#pragma unroll
        for (int k = 0; k < 7; ++k) mu[k] = LDS16(lmem + (odd ? 6 - k : k) * KB + lane * 16);
        SB();
      }
    }
    tb[5] = pk8(acc[5][0], acc[5][1]);
    sacc = mfma32(tb[5], mu[5], sacc);
    SB();
    tb[6] = pk8(acc[6][0], acc[6][1]);
    sacc = mfma32(tb[6], mu[6], sacc);
    if (w != 0) LDSF4(lred + w * KB + lane * 16) = sacc;
    BAR();
    if (w == 0) {
      const f4 s = (sacc + LDSF4(lred + KB + lane * 16)) + (LDSF4(lred + 2 * KB + lane * 16) + LDSF4(lred + 3 * KB + lane * 16));
      if (sval) {
        st16c(c.rs, so + slot, ns4(f2u(s)));
        if (t >= 1) st16c(c.rs, so + (unsigned)((t - 1) & 3) * 32 * KB, S4);
      }
    }
    STAMP(t, 2);
    CBAR();
    load_x(t);
#pragma unroll
    for (int lp = 0; lp < 7; ++lp) LDS16(ltb + (w * 7 + (odd ? 6 - lp : lp)) * KB + lane * 16) = tb[lp];
    BAR();
    v4u tp[8];
#pragma unroll
    for (int ww = 0; ww < 4; ++ww) {
      tp[ww * 2 + 0] = LDS16(ltb + (ww * 7 + w) * KB + lane * 16);
      tp[ww * 2 + 1] = LDS16(ltb + (ww * 7 + gp1) * KB + lane * 16);
      if (w == 3) tp[ww * 2 + 1] = v4u{0u, 0u, 0u, 0u};
    }
    POLL(anysent(mx4(mx4(mx4(mx4(mx4(mx4(mx4(mx4(0u, X[0]), X[1]), X[2]), X[3]), X[4]), X[5]), X[6]), X[7])), load_x(t), 1);
    SB();
#pragma unroll
    for (int lt = 0; lt < 4; ++lt) {
      f4 s = Z4;
#pragma unroll
      for (int k = 0; k < 8; ++k) s = mfma32(tp[k], X[lt * 2 + (k & 1)], s);
      LDSF4(lred + (lt * 4 + w) * KB + lane * 16) = s;
    }
    BAR();
    {
      const int l = w >> 1, tau = w & 1, s = t - l * LAG;
      const f4 v = (LDSF4(lred + (w * 4 + 0) * KB + lane * 16) + LDSF4(lred + (w * 4 + 1) * KB + lane * 16)) +
                   (LDSF4(lred + (w * 4 + 2) * KB + lane * 16) + LDSF4(lred + (w * 4 + 3) * KB + lane * 16));
      if (s >= 0 && s < NSEG && (q >> 1) == l) {
        const unsigned o = R_STM + (unsigned)((((a * 2 + (q & 1)) * 2 + tau) * 16 + h) * 256 + i16 * 16);
        st16c(c.rs, rec_off(l, s) + o, ns4(f2u(v)));
        if (s + AD < NSEG) st16c(c.rs, rec_off(l, s + AD) + o, S4);
      }
    }
    STAMP(t, 3);
  }
}

DEVI void role_C(Ctx& c, int j, unsigned char* lds, const unsigned char* mem0) {
  const int lane = c.lane, w = c.wave;
  const int i16 = lane & 15, q = lane >> 4;
  unsigned char* lmem = lds;
  unsigned char* lred = lds + 16 * KB;
  if (j == 0) probe_side(c, 1);
  v4u W[56];
  {
    const unsigned b0 = WU_OFF + (unsigned)(j * 16 + 4 * w) * 14 * KB + lane * 16;
#pragma unroll
    for (int f = 0; f < 56; ++f) W[f] = ld16(c.rs, b0 + f * KB);
#pragma unroll
    for (int f = 0; f < 56; ++f) PIN_A(W[f]);
  }
  const v2u m0 = ns2(*(const v2u*)(mem0 + (size_t)(i16 & 7) * 896 + (j * 16 + 4 * q) * 2));
  const unsigned sides = get_sides(c);
  const unsigned sC = (sides >> 1) & 1;
  unsigned mo[4], so[4], eo[4];
#pragma unroll
  for (int k = 0; k < 4; ++k) {
    mo[k] = rmap(c, L_MEMC + (k < 3 ? k * 4096 + c.tid * 16 : 12288 + (c.tid & 127) * 16), sC);
    so[k] = rmap(c, L_S + (k * 16 + 4 * w) * 256 + lane * 16, sC);
    eo[k] = rmap(c, L_E + (4 * w + k) * KB + lane * 16, sC);
  }
  const unsigned xo = rmap(c, L_XS + ((4 * w + q) * 2 + (i16 >> 3)) * 128 + (i16 & 7) * 8, sC);
  const unsigned pf = (unsigned)(j >> 1) * KB + (unsigned)((2 * (j & 1) + (q >> 1)) * 16 + i16) * 16 + (q & 1) * 8;
  const unsigned pH = rmap(c, L_MEMH + pf, sides & 1), pC = rmap(c, L_MEMC + pf, sC);
  const unsigned pg = (unsigned)(i16 >> 3) * 8 * KB + (unsigned)(j >> 1) * 512 + (unsigned)(2 * (j & 1) + (q >> 1)) * 128 + (i16 & 7) * 16 + (q & 1) * 8;
  const unsigned pG0 = rmap(c, L_MEMG + pg, (sides >> 2) & 1), pG1 = rmap(c, L_MEMG + pg, (sides >> 3) & 1);
  const unsigned fo = F_U + (unsigned)(j * 4 + w) * 4;
  for (int t = 0; t < NT; ++t) {
    const unsigned slot = (unsigned)(t & 3) * 32 * KB, eslot = (unsigned)(t & 7) * 32 * KB, xslot = (unsigned)(t & 7) * 16 * KB;
    const int s0 = t, s1 = t - LAG;
    const bool on0 = s0 < NSEG, on1 = s1 >= 0;
    STAMP(t, 0);
    const unsigned fa = fo + (unsigned)((lane & 1) ? NSEG + max(s1, 0) : min(s0, NSEG - 1)) * 512;
    const bool fon = (lane & 1) ? on1 : on0;
    v4u m[4];
    CBAR();
#pragma unroll
    for (int k = 0; k < 4; ++k) m[k] = ld16c(c.rs, mo[k] + slot);
    unsigned fl = ld4c(c.rs, fa);
    POLL(anysent(mx4(mx4(mx4(mx4(0u, m[0]), m[1]), m[2]), m[3])),
         { _Pragma("unroll") for (int k = 0; k < 4; ++k) m[k] = ld16c(c.rs, mo[k] + slot); }, 0);
#pragma unroll
    for (int k = 0; k < 3; ++k) LDS16(lmem + k * 4096 + c.tid * 16) = m[k];
    LDS16(lmem + 12288 + (c.tid & 127) * 16) = m[3];
    STAMP(t, 1);
    POLL(__builtin_amdgcn_ballot_w64(fon && fl == 0u) != 0ull, fl = ld4c(c.rs, fa), 1);
    v4u U[8];
#pragma unroll
    for (int l = 0; l < 2; ++l) {
      const bool on = l ? on1 : on0;
      const unsigned ub = U_OFF + (unsigned)(l * NSEG + (l ? max(s1, 0) : min(s0, NSEG - 1))) * SEGG + (unsigned)(j * 16 + 4 * w) * KB + lane * 16;
#pragma unroll
      for (int hh = 0; hh < 4; ++hh) U[l * 4 + hh] = on ? ld16c(c.rs, ub + hh * KB) : v4u{0u, 0u, 0u, 0u};
    }
    BAR();
    STAMP(t, 2);
    v4u zA[2];
    v4u E[4];
    v2u xs;
    {
      v4u mf[14];
#pragma unroll
      for (int ks = 0; ks < 14; ++ks) mf[ks] = LDS16(lmem + ks * KB + lane * 16);
      SB();
      f4 z[4] = {Z4, Z4, Z4, Z4};
#pragma unroll
      for (int ks = 0; ks < 14; ++ks) {
#pragma unroll
        for (int hh = 0; hh < 4; ++hh) z[hh] = mfma32(mf[ks], W[hh * 14 + ks], z[hh]);
        SB();
      }
      CBAR();
#pragma unroll
      for (int hh = 0; hh < 4; ++hh) E[hh] = ld16c(c.rs, eo[hh] + eslot);
      xs = ld8c(c.rs, xo + xslot);
      unsigned zl[4], zh[4];
#pragma unroll
      for (int hh = 0; hh < 4; ++hh) { zl[hh] = pk2(z[hh][0], z[hh][1]); zh[hh] = pk2(z[hh][2], z[hh][3]); }
      swap16(zl[0], zl[1]); swap16(zl[2], zl[3]); swap32(zl[0], zl[2]); swap32(zl[1], zl[3]);
      swap16(zh[0], zh[1]); swap16(zh[2], zh[3]); swap32(zh[0], zh[2]); swap32(zh[1], zh[3]);
      zA[0] = v4u{zl[0], zh[0], zl[1], zh[1]};
      zA[1] = v4u{zl[2], zh[2], zl[3], zh[3]};
      const unsigned zo = R_ZB + (unsigned)(j * 4 + w) * KB + lane * 16;
      if (on0) { st16c(c.rs, rec_off(0, s0) + zo, ns4(zA[0])); if (s0 + AD < NSEG) st16c(c.rs, rec_off(0, s0 + AD) + zo, S4); }
      if (on1) { st16c(c.rs, rec_off(1, s1) + zo, ns4(zA[1])); if (s1 + AD < NSEG) st16c(c.rs, rec_off(1, s1 + AD) + zo, S4); }
    }
    STAMP(t, 3);
    POLL(anysent(mx2(mx4(mx4(mx4(mx4(0u, E[0]), E[1]), E[2]), E[3]), xs)),
         { _Pragma("unroll") for (int hh = 0; hh < 4; ++hh) E[hh] = ld16c(c.rs, eo[hh] + eslot); xs = ld8c(c.rs, xo + xslot); }, 0);
    v4u sv[4];
    CBAR();
#pragma unroll
    for (int k = 0; k < 4; ++k) sv[k] = ld16c(c.rs, so[k] + slot);
    SB();
    f4 ox[4];
#pragma unroll
    for (int hh = 0; hh < 4; ++hh) {
      const f4 o0 = mfma32(U[hh], E[hh], Z4), o1 = mfma32(U[4 + hh], E[hh], Z4);
      ox[hh] = (i16 < 8) ? o0 : o1;
    }
    STAMP(t, 4);
    SB();
    POLL(anysent(mx4(mx4(mx4(mx4(0u, sv[0]), sv[1]), sv[2]), sv[3])),
         { _Pragma("unroll") for (int k = 0; k < 4; ++k) sv[k] = ld16c(c.rs, so[k] + slot); }, 0);
    STAMP(t, 5);
    {
      f4 a = u2f(sv[0]) + u2f(sv[2]), b = u2f(sv[1]) + u2f(sv[3]);
      const float mxx = __uint_as_float(xs.x), lx = __uint_as_float(xs.y);
      const float mm = fmaxf(fmaxf(fmaxf(fmaxf(a[0], a[1]), fmaxf(a[2], a[3])), fmaxf(fmaxf(b[0], b[1]), fmaxf(b[2], b[3]))), mxx);
      const float ml = mm * LOG2E;
#pragma unroll
      for (int r = 0; r < 4; ++r) { a[r] = ex2(a[r] * LOG2E - ml); b[r] = ex2(b[r] * LOG2E - ml); }
      const float ax = ex2(mxx * LOG2E - ml);
      const float den = ((a[0] + a[1]) + (a[2] + a[3])) + ((b[0] + b[1]) + (b[2] + b[3])) + ax * lx;
      const float inv = 1.0f / den;
      const v4u P = pk8(a * inv, b * inv);
      const f4 d0 = mfma32(zA[0], P, Z4), d1 = mfma32(zA[1], P, Z4);
      float wx[4];
      rbcast4(ax * inv, wx);
      f4 acc = (ox[0] * wx[0] + ox[1] * wx[1]) + (ox[2] * wx[2] + ox[3] * wx[3]);
      acc += (i16 < 8) ? d0 : d1;
      if (w != 0) LDSF4(lred + w * KB + lane * 16) = acc;
      BAR();
      if (w == 0) {
        acc = (acc + LDSF4(lred + KB + lane * 16)) + (LDSF4(lred + 2 * KB + lane * 16) + LDSF4(lred + 3 * KB + lane * 16));
        v2u o = ns2(pk4(acc));
        if (i16 >= 8 && t + 1 <= LAG) o = m0;
        const unsigned nslot = (unsigned)((t + 1) & 3) * 32 * KB;
        st8c(c.rs, pH + nslot, o);
        st8c(c.rs, (((t + 1) & 1) ? pG1 : pG0) + nslot, o);
        st8c(c.rs, pC + nslot, o);
        st8c(c.rs, pH + slot, S2);
        st8c(c.rs, ((t & 1) ? pG1 : pG0) + slot, S2);
        if (t >= 1) st8c(c.rs, pC + (unsigned)((t - 1) & 3) * 32 * KB, S2);
      }
    }
    STAMP(t, 6);
  }
}

DEVI void role_GX(Ctx& c, int h, int l, int p, unsigned char* lds) {
  const int lane = c.lane, w = c.wave;
  const int i16 = lane & 15, q = lane >> 4;
  unsigned char* lX = lds;
  unsigned char* lG = lds + 28 * KB;
  unsigned char* lred = lds + 60 * KB;
  if (h == 0 && l == 0) probe_side(c, 2 + p);
  v4u W[98];
  {
    const unsigned b0 = WG_OFF + (unsigned)(h * 14 + 3 * w) * 28 * KB + lane * 16;
#pragma unroll
    for (int f = 0; f < 84; ++f) W[f] = ld16(c.rs, b0 + f * KB);
    const unsigned b1 = WG_OFF + (unsigned)(h * 14 + 12 + (w >> 1)) * 28 * KB + (w & 1) * 7 * KB + lane * 16;
#pragma unroll
    for (int k = 0; k < 7; ++k) { W[84 + k] = ld16(c.rs, b1 + k * KB); W[91 + k] = ld16(c.rs, b1 + (14 + k) * KB); }
#pragma unroll
    for (int f = 0; f < 62; ++f) PIN_A(W[f]);
  }
  const unsigned sides = get_sides(c);
  const unsigned sC = (sides >> 1) & 1;
  const int kx = 7 * (w & 1);
  unsigned mo[4];
#pragma unroll
  for (int k = 0; k < 4; ++k) {
    const int vu = 4 * w + k, ks = vu < 12 ? vu : 12 + ((vu - 12) >> 1);
    mo[k] = rmap(c, L_MEMG + l * 8 * KB + ks * 512 + q * 128 + (i16 & 7) * 16, (sides >> (2 + p)) & 1);
  }
  const unsigned eo = rmap(c, L_E + h * KB + (q * 16 + l * 8 + (i16 & 7)) * 16 + (i16 >> 3) * 8, sC);
  const unsigned xo = rmap(c, L_XS + (h * 2 + l) * 128 + (i16 & 7) * 8, sC);
  for (int t = p; t < NT; t += 2) {
    const int s = t - l * LAG;
    const bool act = (s >= 0 && s < NSEG);
    v4u g[8];
    STAMP(t, 0);
    if (act) {
      {
        const unsigned xb = rec_off(l, s) + R_XN + c.tid * 16;
        v4u x[7];
        CBAR();
#pragma unroll
        for (int f = 0; f < 7; ++f) x[f] = ld16c(c.rs, xb + f * 4096);
        POLL(anysent(mx4(mx4(mx4(mx4(mx4(mx4(mx4(0u, x[0]), x[1]), x[2]), x[3]), x[4]), x[5]), x[6])),
             { _Pragma("unroll") for (int f = 0; f < 7; ++f) x[f] = ld16c(c.rs, xb + f * 4096); }, 2);
#pragma unroll
        for (int f = 0; f < 7; ++f) LDS16(lX + f * 4096 + c.tid * 16) = x[f];
      }
      BAR();
      STAMP(t, 1);
#pragma unroll 1
      for (int tau = 0; tau < 2; ++tau) {
        const unsigned char* xp = lX + tau * 14 * KB + lane * 16;
        f4 acc[8] = {Z4, Z4, Z4, Z4, Z4, Z4, Z4, Z4};
        v4u xs[7];
#pragma unroll
        for (int k = 0; k < 7; ++k) xs[k] = LDS16(xp + (kx + k) * KB);
        v4u x0 = LDS16(xp), x1 = LDS16(xp + KB);
        SB();
#pragma unroll
        for (int k = 0; k < 7; ++k) {
          acc[6] = mfma32(W[84 + k], xs[k], acc[6]);
          acc[7] = mfma32(W[91 + k], xs[k], acc[7]);
        }
        SB();
#pragma unroll
        for (int ks = 0; ks < 14; ++ks) {
          const v4u x = x0;
          x0 = x1;
          if (ks + 2 < 14) x1 = LDS16(xp + (ks + 2) * KB);
#pragma unroll
          for (int pp = 0; pp < 3; ++pp) {
            acc[pp * 2 + 0] = mfma32(W[pp * 28 + ks], x, acc[pp * 2 + 0]);
            acc[pp * 2 + 1] = mfma32(W[pp * 28 + 14 + ks], x, acc[pp * 2 + 1]);
          }
          SB();
        }
        unsigned char* gp = lG + tau * 16 * KB + lane * 16;
        LDS16(gp + (12 + w) * KB) = pk8(acc[6], acc[7]);
#pragma unroll
        for (int pp = 0; pp < 3; ++pp) LDS16(gp + (3 * w + pp) * KB) = pk8(acc[pp * 2], acc[pp * 2 + 1]);
      }
      BAR();
      STAMP(t, 2);
      {
        const int tq = w & 1, kh = w >> 1;
        f4 a0 = Z4, a1 = Z4;
#pragma unroll
        for (int hf = 0; hf < 2; ++hf) {
          v4u ga[4], gb[4], xq[4];
#pragma unroll
          for (int k = 0; k < 4; ++k) {
            const int vu = kh * 8 + hf * 4 + k, ks = vu < 12 ? vu : 12 + ((vu - 12) >> 1);
            ga[k] = LDS16(lG + vu * KB + lane * 16);
            gb[k] = LDS16(lG + (16 + vu) * KB + lane * 16);
            xq[k] = LDS16(lX + (tq * 14 + ks) * KB + lane * 16);
          }
          SB();
#pragma unroll
          for (int k = 0; k < 4; ++k) { a0 = mfma32(ga[k], xq[k], a0); a1 = mfma32(gb[k], xq[k], a1); }
          SB();
        }
        if (kh) { LDSF4(lred + (tq * 2 + 0) * KB + lane * 16) = a0; LDSF4(lred + (tq * 2 + 1) * KB + lane * 16) = a1; }
#pragma unroll
        for (int k = 0; k < 4; ++k) { g[k] = LDS16(lG + (4 * w + k) * KB + lane * 16); g[4 + k] = LDS16(lG + (16 + 4 * w + k) * KB + lane * 16); }
        BAR();
        if (!kh) {
          a0 += LDSF4(lred + (tq * 2 + 0) * KB + lane * 16);
          a1 += LDSF4(lred + (tq * 2 + 1) * KB + lane * 16);
          const int lim = tok_of(tq, i16) - 8 * q;
          float m = NEG;
#pragma unroll
          for (int r = 0; r < 4; ++r) {
            a0[r] = (r <= lim) ? a0[r] : NEG;
            a1[r] = (4 + r <= lim) ? a1[r] : NEG;
            m = fmaxf(m, fmaxf(a0[r], a1[r]));
          }
          m = rmax4(m);
          const float ml = m * LOG2E;
          float lsum = 0.f;
#pragma unroll
          for (int r = 0; r < 4; ++r) {
            a0[r] = (r <= lim) ? ex2(a0[r] * LOG2E - ml) : 0.f;
            a1[r] = (4 + r <= lim) ? ex2(a1[r] * LOG2E - ml) : 0.f;
            lsum += a0[r] + a1[r];
          }
          lsum = rsum4(lsum);
          const unsigned o1 = R_ETT + (unsigned)(h * 2 + tq) * KB + lane * 16, o2 = R_ETS + (unsigned)((tq * 16 + h) * 16 + i16) * 8;
          st16c(c.rs, rec_off(l, s) + o1, ns4(pk8(a0, a1)));
          if (q == 0) st8c(c.rs, rec_off(l, s) + o2, ns2(v2u{__float_as_uint(m), __float_as_uint(lsum)}));
          if (s + AD < NSEG) {
            st16c(c.rs, rec_off(l, s + AD) + o1, S4);
            if (q == 0) st8c(c.rs, rec_off(l, s + AD) + o2, S2);
          }
        }
      }
    } else {
#pragma unroll
      for (int k = 0; k < 8; ++k) g[k] = v4u{0u, 0u, 0u, 0u};
    }
    STAMP(t, 3);
    {
      const unsigned slot = (unsigned)(t & 3) * 32 * KB;
      v4u m[4];
      CBAR();
#pragma unroll
      for (int k = 0; k < 4; ++k) m[k] = ld16c(c.rs, mo[k] + slot);
      POLL(anysent(mx4(mx4(mx4(mx4(0u, m[0]), m[1]), m[2]), m[3])),
           { _Pragma("unroll") for (int k = 0; k < 4; ++k) m[k] = ld16c(c.rs, mo[k] + slot); }, 0);
      STAMP(t, 4);
      f4 a0 = Z4, a1 = Z4;
#pragma unroll
      for (int k = 0; k < 4; ++k) { a0 = mfma32(g[k], m[k], a0); a1 = mfma32(g[4 + k], m[k], a1); }
      LDSF4(lred + 4 * KB + w * KB + lane * 16) = (i16 < 8) ? a0 : a1;
      BAR();
      if (w == 0) {
        const unsigned char* rp = lred + 4 * KB + lane * 16;
        f4 v = (LDSF4(rp) + LDSF4(rp + KB)) + (LDSF4(rp + 2 * KB) + LDSF4(rp + 3 * KB));
        float m1 = fmaxf(fmaxf(v[0], v[1]), fmaxf(v[2], v[3]));
        m1 = fmaxf(m1, ror8(m1));
        m1 = rmax4(m1);
        const float ml = m1 * LOG2E;
#pragma unroll
        for (int r = 0; r < 4; ++r) v[r] = ex2(v[r] * LOG2E - ml);
        const unsigned eslot = (unsigned)(t & 7) * 32 * KB, xslot = (unsigned)(t & 7) * 16 * KB;
        st8c(c.rs, eo + eslot, act ? ns2(pk4(v)) : v2u{0u, 0u});
        float ls = (v[0] + v[1]) + (v[2] + v[3]);
        ls += ror8(ls);
        ls = rsum4(ls);
        if (lane < 8) st8c(c.rs, xo + xslot, act ? ns2(v2u{__float_as_uint(m1), __float_as_uint(ls)}) : v2u{__float_as_uint(-1.0e30f), 0u});
        if (t >= 2) {
          st8c(c.rs, eo + (unsigned)((t - 2) & 7) * 32 * KB, S2);
          if (lane < 8) st8c(c.rs, xo + (unsigned)((t - 2) & 7) * 16 * KB, S2);
        }
      }
      STAMP(t, 5);
    }
  }
}

constexpr int VD = 3;
DEVI void role_V(Ctx& c, int l, int ct, unsigned char* lds, __hip_bfloat16* out) {
  const int lane = c.lane, w = c.wave;
  const int i16 = lane & 15, q = lane >> 4;
  unsigned char* lX = lds;
  unsigned char* lU = lds + 28 * KB + w * 16 * KB;
  unsigned char* lred = lds + 92 * KB;
  v4u W[56];
  {
    const unsigned b0 = WU_OFF + (unsigned)(ct * 16 + 4 * w) * 14 * KB + lane * 16;
#pragma unroll
    for (int f = 0; f < 56; ++f) W[f] = ld16(c.rs, b0 + f * KB);
#pragma unroll
    for (int f = 0; f < 56; ++f) PIN_A(W[f]);
  }
  v4u x[7];
  auto load_x = [&](int s) {
    const unsigned xb = rec_off(l, s) + R_XN + c.tid * 16;
#pragma unroll
    for (int f = 0; f < 7; ++f) x[f] = ld16c(c.rs, xb + f * 4096);
  };
  v4u e[8], sv[8], z;
  v2u es[2], xr;
  const unsigned po = (unsigned)((w & 1) * 14 + (ct >> 1)) * KB + (unsigned)((2 * (ct & 1) + (q >> 1)) * 16 + i16) * 16 + (q & 1) * 8;
  auto load_e = [&](int s) {
    const unsigned rb = rec_off(l, s);
#pragma unroll
    for (int k = 0; k < 8; ++k) e[k] = ld16c(c.rs, rb + R_ETT + (unsigned)(8 * w + k) * KB + lane * 16);
#pragma unroll
    for (int tq = 0; tq < 2; ++tq) es[tq] = ld8c(c.rs, rb + R_ETS + (unsigned)((tq * 16 + 4 * w) * 16 + lane) * 8);
    xr = ld8c(c.rs, rb + R_XR + po);
  };
  auto load_s = [&](int s) {
    const unsigned rb = rec_off(l, s);
#pragma unroll
    for (int k = 0; k < 8; ++k) sv[k] = ld16c(c.rs, rb + R_STM + (unsigned)(k * 16 + 4 * w) * 256 + lane * 16);
    z = ld16c(c.rs, rb + R_ZB + (unsigned)(ct * 4 + w) * KB + lane * 16);
  };
  CBAR();
  load_x(0);
  for (int it = 0; it < NSEG + VD; ++it) {
    const int su = it, so = it - VD;
    STAMP(it, 0);
    if (su < NSEG) {
      POLL(anysent(mx4(mx4(mx4(mx4(mx4(mx4(mx4(0u, x[0]), x[1]), x[2]), x[3]), x[4]), x[5]), x[6])), load_x(su), 2);
#pragma unroll
      for (int f = 0; f < 7; ++f) LDS16(lX + f * 4096 + c.tid * 16) = x[f];
    }
    BAR();
    STAMP(it, 1);
    CBAR();
    if (so >= 0) load_e(so);
    if (su < NSEG) {
      const unsigned char* xp = lX + lane * 16;
      f4 acc[8] = {Z4, Z4, Z4, Z4, Z4, Z4, Z4, Z4};
      v4u xa = LDS16(xp), xb = LDS16(xp + 14 * KB), ya = LDS16(xp + KB), yb = LDS16(xp + 15 * KB);
      SB();
#pragma unroll
      for (int ks = 0; ks < 14; ++ks) {
        const v4u x0 = xa, x1 = xb;
        xa = ya; xb = yb;
        if (ks + 2 < 14) { ya = LDS16(xp + (ks + 2) * KB); yb = LDS16(xp + (16 + ks) * KB); }
#pragma unroll
        for (int hh = 0; hh < 4; ++hh) {
          acc[hh * 2 + 0] = mfma32(x0, W[hh * 14 + ks], acc[hh * 2 + 0]);
          acc[hh * 2 + 1] = mfma32(x1, W[hh * 14 + ks], acc[hh * 2 + 1]);
        }
        SB();
      }
      const unsigned ub = U_OFF + (unsigned)(l * NSEG + su) * SEGG + (unsigned)(ct * 16 + 4 * w) * KB + lane * 16;
#pragma unroll
      for (int hh = 0; hh < 4; ++hh) {
        const v4u u = pk8(acc[hh * 2], acc[hh * 2 + 1]);
        st16c(c.rs, ub + hh * KB, u);
        LDS16(lU + (su & 3) * 4 * KB + hh * KB + lane * 16) = u;
      }
      DRAIN();
      if (lane == 0) st4c(c.rs, F_U + (unsigned)(l * NSEG + su) * 512 + (unsigned)(ct * 4 + w) * 4, 1u);
    }
    STAMP(it, 2);
    CBAR();
    if (so >= 0) load_s(so);
    if (su + 1 < NSEG) load_x(su + 1);
    if (so >= 0) {
      POLL(anysent(mx2(mx2(mx2(mx4(mx4(mx4(mx4(mx4(mx4(mx4(mx4(0u, e[0]), e[1]), e[2]), e[3]), e[4]), e[5]), e[6]), e[7]), es[0]), es[1]), xr)), load_e(so), 2);
      f4 ott[8];
#pragma unroll
      for (int hh = 0; hh < 4; ++hh) {
        const v4u u = LDS16(lU + (so & 3) * 4 * KB + hh * KB + lane * 16);
        ott[hh * 2 + 0] = mfma32(u, e[hh * 2 + 0], Z4);
        ott[hh * 2 + 1] = mfma32(u, e[hh * 2 + 1], Z4);
      }
      STAMP(it, 3);
      POLL(anysent(mx4(mx4(mx4(mx4(mx4(mx4(mx4(mx4(mx4(0u, sv[0]), sv[1]), sv[2]), sv[3]), sv[4]), sv[5]), sv[6]), sv[7]), z)), load_s(so), 1);
      STAMP(it, 4);
#pragma unroll
      for (int tq = 0; tq < 2; ++tq) {
        f4 a = u2f(sv[0 + tq]) + u2f(sv[4 + tq]), b = u2f(sv[2 + tq]) + u2f(sv[6 + tq]);
        const float mt = __uint_as_float(es[tq].x), lt = __uint_as_float(es[tq].y);
        const float mm = fmaxf(fmaxf(fmaxf(fmaxf(a[0], a[1]), fmaxf(a[2], a[3])), fmaxf(fmaxf(b[0], b[1]), fmaxf(b[2], b[3]))), mt);
        const float ml = mm * LOG2E;
#pragma unroll
        for (int r = 0; r < 4; ++r) { a[r] = ex2(a[r] * LOG2E - ml); b[r] = ex2(b[r] * LOG2E - ml); }
        const float at = ex2(mt * LOG2E - ml);
        const float den = ((a[0] + a[1]) + (a[2] + a[3])) + ((b[0] + b[1]) + (b[2] + b[3])) + at * lt;
        const float inv = 1.0f / den;
        f4 acc = mfma32(z, pk8(a * inv, b * inv), Z4);
        float wt[4];
        rbcast4(at * inv, wt);
        acc += (ott[0 + tq] * wt[0] + ott[2 + tq] * wt[1]) + (ott[4 + tq] * wt[2] + ott[6 + tq] * wt[3]);
        LDSF4(lred + (tq * 4 + w) * KB + lane * 16) = acc;
      }
    }
    BAR();
    if (so >= 0 && w < 2) {
      const unsigned char* rp = lred + w * 4 * KB + lane * 16;
      const f4 acc = (LDSF4(rp) + LDSF4(rp + KB)) + (LDSF4(rp + 2 * KB) + LDSF4(rp + 3 * KB));
      const v2u y = pk4(acc);
      const v2u o = v2u{pk2(bflo(xr.x) + bflo(y.x), bfhi(xr.x) + bfhi(y.x)), pk2(bflo(xr.y) + bflo(y.y), bfhi(xr.y) + bfhi(y.y))};
      if (l == 0) {
        st8c(c.rs, rec_off(0, so) + R_X2 + po, ns2(o));
        if (so + AD < NSEG) st8c(c.rs, rec_off(0, so + AD) + R_X2 + po, S2);
      } else {
        *(v2u*)((unsigned char*)out + (size_t)(so * 32 + tok_of(w, i16)) * 896 + (ct * 16 + 4 * q) * 2) = o;
      }
    }
    STAMP(it, 5);
  }
}

DEVI void role_AH(Ctx& c, int l, int a, unsigned char* lds, const unsigned char* x0) {
  const int lane = c.lane, w = c.wave;
  const int i16 = lane & 15, q = lane >> 4;
  unsigned char* lX = lds;
  unsigned char* lK = lds + 28 * KB;
  unsigned char* lV = lds + 46 * KB;
  unsigned char* lQ = lds + 64 * KB;
  unsigned char* lst = lds + 66 * KB;
  unsigned char* lmg = lds + 67 * KB;
  unsigned char* lms = lds + 71 * KB;
  const int tau = w & 1, g = w >> 1;
  v4u W[42];
#pragma unroll
  for (int tl = 0; tl < 3; ++tl) {
    const unsigned b0 = WQ_OFF + (unsigned)(a * 6 + (tl < 2 ? 2 * g + tl : 4 + g)) * 14 * KB + lane * 16;
#pragma unroll
    for (int k = 0; k < 14; ++k) W[tl * 14 + k] = ld16(c.rs, b0 + (unsigned)(k < 7 ? 7 * g + k : 7 * (1 - g) + k - 7) * KB);
  }
  for (int o = c.tid * 16; o < 36 * (int)KB; o += 4096) LDS16(lK + o) = v4u{0u, 0u, 0u, 0u};
  f4 cs0 = Z4, cs1 = Z4, cs2 = Z4;
  {
    const v4u one = {0x3F803F80u, 0x3F803F80u, 0x3F803F80u, 0x3F803F80u};
#pragma unroll
    for (int k = 0; k < 14; ++k) { cs0 = mfma32(W[k], one, cs0); cs1 = mfma32(W[14 + k], one, cs1); cs2 = mfma32(one, W[28 + k], cs2); }
  }
#pragma unroll
  for (int f = 0; f < 42; ++f) PIN_A(W[f]);
  const float csv = cs2[0];
  const float sc = 0.18898223650461362f * LOG2E;
  const int lim = tok_of(tau, i16) - 8 * q;
  v4u dm;
#pragma unroll
  for (int e = 0; e < 4; ++e) dm[e] = ((2 * e <= lim) ? 0xFFFFu : 0u) | ((2 * e + 1 <= lim) ? 0xFFFF0000u : 0u);
  v4u x[7];
  const rsrc_t rx = __builtin_amdgcn_make_buffer_rsrc((void*)x0, 0, 2048 * 896, 0x00020000);
  auto load_x = [&](int s) {
    if (l == 0) {
      const unsigned p = (unsigned)(s * 32 + tok_of(tau, i16)) * 896 + (g * 7 * 32 + 8 * q) * 2;
#pragma unroll
      for (int f = 0; f < 7; ++f) x[f] = ld16(rx, p + f * 64);
    } else {
      const unsigned xb = rec_off(0, s) + R_X2 + (unsigned)(tau * 14 + g * 7) * KB + lane * 16;
#pragma unroll
      for (int f = 0; f < 7; ++f) x[f] = ld16c(c.rs, xb + f * KB);
    }
  };
  CBAR();
  load_x(0);
  int slot = 0;
  for (int s = 0; s < NSEG; ++s) {
    STAMP(s, 0);
    if (l == 1) POLL(anysent(mx4(mx4(mx4(mx4(mx4(mx4(mx4(0u, x[0]), x[1]), x[2]), x[3]), x[4]), x[5]), x[6])), load_x(s), 1);
    STAMP(s, 1);
#pragma unroll
    for (int f = 0; f < 7; ++f) LDS16(lX + (tau * 14 + g * 7 + f) * KB + lane * 16) = x[f];
    f4 a0 = Z4, a1 = Z4, a2 = Z4;
    {
      float sum = 0.f, sq = 0.f;
#pragma unroll
      for (int f = 0; f < 7; ++f) {
        a0 = mfma32(W[f], x[f], a0);
        a1 = mfma32(W[14 + f], x[f], a1);
        a2 = mfma32(x[f], W[28 + f], a2);
#pragma unroll
        for (int e = 0; e < 4; ++e) { sum = dot2(x[f][e], 0x3F803F80u, sum); sq = dot2(x[f][e], x[f][e], sq); }
        SB();
      }
      sum = rsum4(sum);
      sq = rsum4(sq);
      if (q == 0) *(f2*)(lst + ((g * 2 + tau) * 16 + i16) * 8) = f2{sum, sq};
    }
    BAR();
    {
      v4u y[7];
#pragma unroll
      for (int f = 0; f < 7; ++f) y[f] = LDS16(lX + (tau * 14 + (1 - g) * 7 + f) * KB + lane * 16);
      const f2 sa = *(const f2*)(lst + (tau * 16 + i16) * 8), sb = *(const f2*)(lst + ((2 + tau) * 16 + i16) * 8);
      const f4 t0 = LDSF4(lst + (tau * 16 + 4 * q) * 8), t1 = LDSF4(lst + (tau * 16 + 4 * q) * 8 + 16);
      const f4 t2 = LDSF4(lst + ((2 + tau) * 16 + 4 * q) * 8), t3 = LDSF4(lst + ((2 + tau) * 16 + 4 * q) * 8 + 16);
      CBAR();
      if (s + 1 < NSEG) load_x(s + 1);
      SB();
#pragma unroll
      for (int f = 0; f < 7; ++f) {
        a0 = mfma32(W[7 + f], y[f], a0);
        a1 = mfma32(W[21 + f], y[f], a1);
        a2 = mfma32(y[f], W[35 + f], a2);
      }
      const float mean = (sa[0] + sb[0]) * (1.0f / 448.0f);
      const float rstd = 1.0f / sqrtf(fmaxf((sa[1] + sb[1]) * (1.0f / 448.0f) - mean * mean, 0.f) + 1e-5f);
      const float mr = mean * rstd;
      a0 = a0 * rstd - cs0 * mr;
      a1 = a1 * rstd - cs1 * mr;
      LDS16((g == 0 ? lQ + tau * KB : lK + (slot * 2 + tau) * KB) + lane * 16) = pk8(a0, a1);
      const f4 sm = f4{t0[0] + t2[0], t0[2] + t2[2], t1[0] + t3[0], t1[2] + t3[2]} * (1.0f / 448.0f);
      const f4 s2 = f4{t0[1] + t2[1], t0[3] + t2[3], t1[1] + t3[1], t1[3] + t3[3]} * (1.0f / 448.0f);
#pragma unroll
      for (int r = 0; r < 4; ++r) {
        const float rs = 1.0f / sqrtf(fmaxf(s2[r] - sm[r] * sm[r], 0.f) + 1e-5f);
        a2[r] = (a2[r] - sm[r] * csv) * rs;
      }
      *(v2u*)(lV + (slot * 2 + g) * KB + lane * 16 + tau * 8) = pk4(a2);
    }
    BAR();
    STAMP(s, 2);
    {
      const v4u qf = LDS16(lQ + tau * KB + lane * 16);
      int sl[5];
      v4u kf[10];
#pragma unroll
      for (int b = 0; b < 5; ++b) {
        const int d = (b < 4) ? 4 * g + b : 8;
        int k = slot - d;
        k += (k < 0) ? 9 : 0;
        sl[b] = k;
        if (b < 4 || g == 0) { kf[2 * b] = LDS16(lK + (k * 2) * KB + lane * 16); kf[2 * b + 1] = LDS16(lK + (k * 2 + 1) * KB + lane * 16); }
      }
      SB();
      f4 scv[4][2];
#pragma unroll
      for (int b = 0; b < 4; ++b) {
        const float bias = (s - (4 * g + b) >= 0) ? 0.f : NEG;
        const f4 bz = {bias, bias, bias, bias};
        scv[b][0] = mfma32(kf[2 * b], qf, bz);
        scv[b][1] = mfma32(kf[2 * b + 1], qf, bz);
      }
      v4u vf[10];
      if (g == 0) {
        const float bias = (s - 8 >= 0) ? 0.f : NEG;
        const f4 bz = {bias, bias, bias, bias};
        const f4 e0 = mfma32(kf[8], qf, bz), e1 = mfma32(kf[9], qf, bz);
#pragma unroll
        for (int r = 0; r < 4; ++r) {
          scv[0][0][r] = (r <= lim) ? scv[0][0][r] : e0[r];
          scv[0][1][r] = (4 + r <= lim) ? scv[0][1][r] : e1[r];
        }
      }
      SB();
#pragma unroll
      for (int b = 0; b < 5; ++b)
        if (b < 4 || g == 0) { vf[2 * b] = LDS16(lV + (sl[b] * 2) * KB + lane * 16); vf[2 * b + 1] = LDS16(lV + (sl[b] * 2 + 1) * KB + lane * 16); }
      SB();
      float m = NEG;
#pragma unroll
      for (int b = 0; b < 4; ++b)
#pragma unroll
        for (int r = 0; r < 4; ++r) m = fmaxf(m, fmaxf(scv[b][0][r], scv[b][1][r]));
      m = fmaxf(rmax4(m), -1.0e30f);
      const float ml = m * sc;
      float lsum = 0.f;
      f4 o0 = Z4, o1 = Z4;
#pragma unroll
      for (int bb = 0; bb < 4; ++bb) {
        const int b = 3 - bb;
#pragma unroll
        for (int r = 0; r < 4; ++r) {
          scv[b][0][r] = ex2(scv[b][0][r] * sc - ml);
          scv[b][1][r] = ex2(scv[b][1][r] * sc - ml);
          lsum += scv[b][0][r] + scv[b][1][r];
        }
        v4u p = pk8(scv[b][0], scv[b][1]);
        if (b == 0 && g == 0) {
          const v4u p8 = p & ~dm;
          p = p & dm;
          o0 = mfma32(vf[8], p8, o0);
          o1 = mfma32(vf[9], p8, o1);
        }
        o0 = mfma32(vf[2 * b], p, o0);
        o1 = mfma32(vf[2 * b + 1], p, o1);
      }
      lsum = rsum4(lsum);
      if (g == 1) {
        LDSF4(lmg + (tau * 2 + 0) * KB + lane * 16) = o0;
        LDSF4(lmg + (tau * 2 + 1) * KB + lane * 16) = o1;
        *(f2*)(lms + (tau * 64 + lane) * 8) = f2{m, lsum};
      }
      BAR();
      if (g == 0) {
        const f2 ot = *(const f2*)(lms + (tau * 64 + lane) * 8);
        const float mt = fmaxf(m, ot[0]);
        const float fa = ex2((m - mt) * sc), fb = ex2((ot[0] - mt) * sc);
        const float inv = 1.0f / (lsum * fa + ot[1] * fb);
        const f4 r0 = (o0 * fa + LDSF4(lmg + (tau * 2 + 0) * KB + lane * 16) * fb) * inv;
        const f4 r1 = (o1 * fa + LDSF4(lmg + (tau * 2 + 1) * KB + lane * 16) * fb) * inv;
        const int c0 = 28 * a + 4 * q, c1 = c0 + 16;
        const unsigned oa = R_OA + (unsigned)tau * 14 * KB + (c0 >> 5) * KB + (((c0 >> 3) & 3) * 16 + i16) * 16 + (c0 & 4) * 2;
        const unsigned ob = R_OA + (unsigned)tau * 14 * KB + (c1 >> 5) * KB + (((c1 >> 3) & 3) * 16 + i16) * 16 + (c1 & 4) * 2;
        st8c(c.rs, rec_off(l, s) + oa, ns2(pk4(r0)));
        if (q < 3) st8c(c.rs, rec_off(l, s) + ob, ns2(pk4(r1)));
        if (s + AD < NSEG) {
          st8c(c.rs, rec_off(l, s + AD) + oa, S2);
          if (q < 3) st8c(c.rs, rec_off(l, s + AD) + ob, S2);
        }
      }
      STAMP(s, 3);
    }
    slot = (slot == 8) ? 0 : slot + 1;
  }
}

DEVI void role_AO(Ctx& c, int l, int tau, int par, unsigned char* lds, const unsigned char* x0) {
  const int lane = c.lane, w = c.wave;
  const int i16 = lane & 15, q = lane >> 4;
  unsigned char* lO = lds;
  unsigned char* lx = lds + 14 * KB;
  unsigned char* lst = lds + 18 * KB;
  v4u W[98];
  {
    const unsigned b0 = WO_OFF + (unsigned)(3 * w) * 28 * KB + lane * 16;
#pragma unroll
    for (int f = 0; f < 84; ++f) W[f] = ld16(c.rs, b0 + f * KB);
    const unsigned b1 = WO_OFF + (unsigned)(12 + (w >> 1)) * 28 * KB + (w & 1) * 7 * KB + lane * 16;
#pragma unroll
    for (int k = 0; k < 7; ++k) { W[84 + k] = ld16(c.rs, b1 + k * KB); W[91 + k] = ld16(c.rs, b1 + (14 + k) * KB); }
#pragma unroll
    for (int f = 0; f < 62; ++f) PIN_A(W[f]);
  }
  const int ux = 12 + (w >> 1), kx = 7 * (w & 1);
  const bool own4 = !(w & 1);
  const rsrc_t rx = __builtin_amdgcn_make_buffer_rsrc((void*)x0, 0, 2048 * 896, 0x00020000);
  for (int s = par; s < NSEG; s += 2) {
    const unsigned rb = rec_off(l, s);
    STAMP(s, 0);
    {
      const unsigned ob = rb + R_OA + (unsigned)tau * 14 * KB;
      v4u v[4];
      auto ldo = [&]() {
        v[0] = ld16c(c.rs, ob + c.tid * 16); v[1] = ld16c(c.rs, ob + 4096 + c.tid * 16); v[2] = ld16c(c.rs, ob + 8192 + c.tid * 16);
        v[3] = ld16c(c.rs, ob + 12288 + (c.tid & 127) * 16);
      };
      CBAR();
      ldo();
      POLL(anysent(mx4(mx4(mx4(mx4(0u, v[0]), v[1]), v[2]), v[3])), ldo(), 1);
      LDS16(lO + c.tid * 16) = v[0];
      LDS16(lO + 4096 + c.tid * 16) = v[1];
      LDS16(lO + 8192 + c.tid * 16) = v[2];
      LDS16(lO + 12288 + (c.tid & 127) * 16) = v[3];
    }
    STAMP(s, 1);
    v4u xr[4];
    auto ldr = [&]() {
#pragma unroll
      for (int p = 0; p < 4; ++p) {
        const int u = (p < 3) ? 3 * w + p : ux;
        if (l == 0) xr[p] = ld16(rx, (unsigned)(s * 32 + tok_of(tau, i16)) * 896 + (u * 32 + 8 * q) * 2);
        else xr[p] = ld16c(c.rs, rec_off(0, s) + R_X2 + (unsigned)(tau * 14 + u) * KB + lane * 16);
      }
    };
    BAR();
    f4 acc[8] = {Z4, Z4, Z4, Z4, Z4, Z4, Z4, Z4};
    {
      const unsigned char* xp = lO + lane * 16;
      v4u xs[7];
#pragma unroll
      for (int k = 0; k < 7; ++k) xs[k] = LDS16(xp + (kx + k) * KB);
      v4u y0 = LDS16(xp), y1 = LDS16(xp + KB);
      SB();
#pragma unroll
      for (int k = 0; k < 7; ++k) {
        acc[6] = mfma32(W[84 + k], xs[k], acc[6]);
        acc[7] = mfma32(W[91 + k], xs[k], acc[7]);
      }
      SB();
      ldr();
      SB();
#pragma unroll
      for (int ks = 0; ks < 14; ++ks) {
        const v4u x = y0;
        y0 = y1;
        if (ks + 2 < 14) y1 = LDS16(xp + (ks + 2) * KB);
        if (ks == 2 && !own4) { LDSF4(lx + ((w >> 1) * 2 + 0) * KB + lane * 16) = acc[6]; LDSF4(lx + ((w >> 1) * 2 + 1) * KB + lane * 16) = acc[7]; }
#pragma unroll
        for (int pp = 0; pp < 3; ++pp) {
          acc[pp * 2 + 0] = mfma32(W[pp * 28 + ks], x, acc[pp * 2 + 0]);
          acc[pp * 2 + 1] = mfma32(W[pp * 28 + 14 + ks], x, acc[pp * 2 + 1]);
        }
        SB();
      }
    }
    if (l == 1) POLL(anysent(mx4(mx4(mx4(mx4(0u, xr[0]), xr[1]), xr[2]), xr[3])), ldr(), 1);
    BAR();
    STAMP(s, 2);
    v4u xb[4];
    if (own4) {
      acc[6] += LDSF4(lx + ((w >> 1) * 2 + 0) * KB + lane * 16);
      acc[7] += LDSF4(lx + ((w >> 1) * 2 + 1) * KB + lane * 16);
    }
    float sum = 0.f, sq = 0.f;
#pragma unroll
    for (int p = 0; p < 4; ++p) {
      xb[p] = resid8(xr[p], acc[2 * p], acc[2 * p + 1]);
      if (p == 3 && !own4) xb[p] = v4u{0u, 0u, 0u, 0u};
#pragma unroll
      for (int e = 0; e < 4; ++e) { sum = dot2(xb[p][e], 0x3F803F80u, sum); sq = dot2(xb[p][e], xb[p][e], sq); }
    }
    sum = rsum4(sum);
    sq = rsum4(sq);
    if (q == 0) *(f2*)(lst + (w * 16 + i16) * 8) = f2{sum, sq};
    BAR();
    const f2 st = (*(const f2*)(lst + i16 * 8) + *(const f2*)(lst + (16 + i16) * 8)) + (*(const f2*)(lst + (32 + i16) * 8) + *(const f2*)(lst + (48 + i16) * 8));
    const float mean = st[0] * (1.0f / 448.0f);
    const float rstd = 1.0f / sqrtf(fmaxf(st[1] * (1.0f / 448.0f) - mean * mean, 0.f) + 1e-5f);
    const float mr = mean * rstd;
#pragma unroll
    for (int p = 0; p < 4; ++p)
      if (p < 3 || own4) {
        const int u = (p < 3) ? 3 * w + p : ux;
        const unsigned fo = (unsigned)(tau * 14 + u) * KB + lane * 16;
        v4u o;
#pragma unroll
        for (int e = 0; e < 4; ++e) o[e] = pk2(bflo(xb[p][e]) * rstd - mr, bfhi(xb[p][e]) * rstd - mr);
        st16c(c.rs, rb + R_XN + fo, ns4(o));
      }
#pragma unroll
    for (int p = 0; p < 4; ++p)
      if (p < 3 || own4) {
        const int u = (p < 3) ? 3 * w + p : ux;
        const unsigned fo = (unsigned)(tau * 14 + u) * KB + lane * 16;
        st16c(c.rs, rb + R_XR + fo, ns4(xb[p]));
        if (s + AD < NSEG) { st16c(c.rs, rec_off(l, s + AD) + R_XN + fo, S4); st16c(c.rs, rec_off(l, s + AD) + R_XR + fo, S4); }
      }
    STAMP(s, 3);
  }
}

DEVI unsigned ring_offset(const unsigned char* ws) {
  const unsigned lo = (unsigned)((size_t)ws & (2u * MB - 1u));
  return ((lo + FLAG_BYTES + 2u * MB - 1u) & ~(2u * MB - 1u)) - lo;
}

__global__ void __launch_bounds__(256, 1) mega_kernel(unsigned char* ws, const unsigned char* x0, const unsigned char* mem0, __hip_bfloat16* out) {
  extern __shared__ __attribute__((aligned(16))) unsigned char lds[];
  Ctx c;
  c.ws = ws;
  c.rs = __builtin_amdgcn_make_buffer_rsrc(ws, 0, WS_END, 0x00020000);
  c.ring = ring_offset(ws);
  c.tid = threadIdx.x;
  c.lane = threadIdx.x & 63;
  c.wave = __builtin_amdgcn_readfirstlane(threadIdx.x >> 6);
  c.dead = false;
  const int b = blockIdx.x, x = b & 7, r = b >> 3;
  if (x == 0) {
    role_H(c, r >> 1, r & 1, lds);
  } else if (x == 1) {
    if (r < 28) role_C(c, r, lds, mem0);
  } else if (x < 4) {
    role_GX(c, r >> 1, r & 1, x - 2, lds);
  } else if (x < 6) {
    if (r < 28) role_V(c, x - 4, r, lds, out); else role_AO(c, x - 4, (r - 28) & 1, (r - 28) >> 1, lds, x0);
  } else if (x == 6) {
    role_AH(c, r >> 4, r & 15, lds, x0);
  }
}

__global__ void __launch_bounds__(256) prologue_kernel(unsigned char* ws, const unsigned short* wqkv, const unsigned short* wo,
                                                       const unsigned short* mem0, const unsigned short* Wq, const unsigned short* Wk,
                                                       const unsigned short* Wv, const unsigned short* Wo) {
  __shared__ __attribute__((aligned(16))) unsigned short lt[64 * 264];
  const int b = blockIdx.x, tid = threadIdx.x, lane = tid & 63, w = tid >> 6;
  const int i16 = lane & 15, q = lane >> 4;
  if (b < 224) {
    const bool isG = b < 112;
    const int bb = isG ? b : b - 112, h = bb / 7, cg = bb % 7;
    v4u B[4][8];
    if (isG) {
#pragma unroll
      for (int nt = 0; nt < 4; ++nt) {
        const int cc = 64 * cg + 32 * (nt >> 1) + 8 * (i16 >> 2) + 4 * (nt & 1) + (i16 & 3);
#pragma unroll
        for (int es = 0; es < 8; ++es) B[nt][es] = *(const v4u*)(Wq + ((size_t)cc * 16 + h) * 256 + es * 32 + 8 * q);
      }
    } else {
      for (int idx = tid; idx < 256 * 8; idx += 256) {
        const int e = idx >> 3, c8 = idx & 7;
        const v4u v = *(const v4u*)(Wo + ((size_t)h * 256 + e) * 448 + 64 * cg + 8 * c8);
#pragma unroll
        for (int k = 0; k < 4; ++k) {
          lt[(8 * c8 + 2 * k) * 264 + e] = (unsigned short)(v[k] & 0xFFFFu);
          lt[(8 * c8 + 2 * k + 1) * 264 + e] = (unsigned short)(v[k] >> 16);
        }
      }
      __syncthreads();
#pragma unroll
      for (int nt = 0; nt < 4; ++nt)
#pragma unroll
        for (int es = 0; es < 8; ++es) B[nt][es] = *(const v4u*)(lt + (16 * nt + i16) * 264 + es * 32 + 8 * q);
    }
    const unsigned short* Wa = isG ? Wk : Wv;
    const float sc = isG ? 0.0625f : 1.0f;
    for (int ks = w; ks < 14; ks += 4) {
      v4u A[2][8];
#pragma unroll
      for (int mt = 0; mt < 2; ++mt) {
        const int d = 32 * ks + 8 * (i16 >> 2) + 4 * mt + (i16 & 3);
#pragma unroll
        for (int es = 0; es < 8; ++es) A[mt][es] = *(const v4u*)(Wa + ((size_t)d * 16 + h) * 256 + es * 32 + 8 * q);
      }
#pragma unroll
      for (int nt = 0; nt < 4; ++nt) {
        f4 a0 = Z4, a1 = Z4;
#pragma unroll
        for (int es = 0; es < 8; ++es) {
          a0 = mfma32(A[0][es], B[nt][es], a0);
          a1 = mfma32(A[1][es], B[nt][es], a1);
        }
        const v4u o = pk8(a0 * sc, a1 * sc);
        size_t off;
        if (isG) off = WG_OFF + (size_t)(((h * 14 + 2 * cg + (nt >> 1)) * 2 + (nt & 1)) * 14 + ks) * KB;
        else off = WU_OFF + (size_t)(((4 * cg + nt) * 16 + h) * 14 + ks) * KB;
        *(v4u*)(ws + off + lane * 16) = o;
      }
    }
  } else {
    const int nb = gridDim.x - 224, bb = b - 224;
    const int gtid = bb * 256 + tid, gstride = nb * 256;
    for (int idx = gtid; idx < 16 * 6 * 14 * 64; idx += gstride) {
      const int ln = idx & 63, f = idx >> 6, ks = f % 14, tl = (f / 14) % 6, a = f / 84;
      const int n = ln & 15, qq = ln >> 4;
      int dh, col;
      if (tl < 4) { dh = 8 * (n >> 2) + 4 * (tl & 1) + (n & 3); col = (tl >> 1) * 448 + 28 * a + dh; }
      else { dh = 16 * (tl - 4) + n; col = 896 + 28 * a + dh; }
      v4u o = {0u, 0u, 0u, 0u};
      if (dh < 28) {
#pragma unroll
        for (int jj = 0; jj < 4; ++jj) {
          const int d = 32 * ks + 8 * qq + 2 * jj;
          o[jj] = (unsigned)wqkv[(size_t)d * 1344 + col] | ((unsigned)wqkv[(size_t)(d + 1) * 1344 + col] << 16);
        }
      }
      *(v4u*)(ws + WQ_OFF + (size_t)idx * 16) = o;
    }
    for (int idx = gtid; idx < 14 * 2 * 14 * 64; idx += gstride) {
      const int ln = idx & 63, f = idx >> 6, ks = f % 14, tt = (f / 14) & 1, u = f / 28;
      const int n = ln & 15, qq = ln >> 4;
      const int col = 32 * u + 8 * (n >> 2) + 4 * tt + (n & 3);
      v4u o;
#pragma unroll
      for (int jj = 0; jj < 4; ++jj) {
        const int d = 32 * ks + 8 * qq + 2 * jj;
        o[jj] = (unsigned)wo[(size_t)d * 448 + col] | ((unsigned)wo[(size_t)(d + 1) * 448 + col] << 16);
      }
      *(v4u*)(ws + WO_OFF + (size_t)idx * 16) = o;
    }
    for (unsigned idx = gtid; idx < FLAG_BYTES / 16; idx += gstride) *(v4u*)(ws + (size_t)idx * 16) = v4u{0u, 0u, 0u, 0u};
    const unsigned ring = ring_offset(ws);
    for (unsigned idx = gtid; idx < RING_BYTES / 16; idx += gstride) {
      const unsigned po = idx * 16, lo = ((po >> 14) << 13) | (po & 8191u);
      v4u o = S4;
      if (lo < L_MEMG && (lo & (64 * KB - 1)) < 14 * KB) {
        const unsigned r = lo & (64 * KB - 1), ln = (r >> 4) & 63, ks = r >> 10;
        o = ns4(*(const v4u*)(mem0 + (size_t)(ln & 7) * 448 + 32 * ks + 8 * (ln >> 4)));
      } else if (lo >= L_MEMG && lo < L_MEMG + 16 * KB && (lo & 8191u) < 7 * KB) {
        const unsigned r = lo & 8191u, ks = r >> 9, qq = (r >> 7) & 3, row = (r >> 4) & 7;
        o = ns4(*(const v4u*)(mem0 + (size_t)row * 448 + 32 * ks + 8 * qq));
      }
      *(v4u*)(ws + ring + po) = o;
    }
    for (unsigned idx = gtid; idx < 2 * AD * REC / 16; idx += gstride) {
      const unsigned l = idx / (AD * REC / 16), r = idx % (AD * REC / 16);
      *(v4u*)(ws + REC_OFF + (size_t)l * NSEG * REC + (size_t)r * 16) = S4;
    }
  }
}

extern "C" void kernel_launch(void* const* d_in, const int* in_sizes, int n_in, void* d_out, int out_size, void* d_ws, size_t ws_size,
                              hipStream_t stream) {
  constexpr int LDS_BYTES = 100 * 1024;
  (void)hipFuncSetAttribute((const void*)mega_kernel, hipFuncAttributeMaxDynamicSharedMemorySize, LDS_BYTES);
  unsigned char* ws = (unsigned char*)d_ws;
  prologue_kernel<<<256 + 32, 256, 0, stream>>>(ws, (const unsigned short*)d_in[1], (const unsigned short*)d_in[2], (const unsigned short*)d_in[4],
                                          (const unsigned short*)d_in[5], (const unsigned short*)d_in[6], (const unsigned short*)d_in[7],
                                          (const unsigned short*)d_in[8]);
  mega_kernel<<<256, 256, LDS_BYTES, stream>>>(ws, (const unsigned char*)d_in[0], (const unsigned char*)d_in[4], (__hip_bfloat16*)d_out);
}
```

```cpp
#include <hip/hip_runtime.h>
#include <hip/hip_bf16.h>

typedef unsigned v4u __attribute__((ext_vector_type(4)));
typedef unsigned v2u __attribute__((ext_vector_type(2)));
typedef float f4 __attribute__((ext_vector_type(4)));
typedef float f2 __attribute__((ext_vector_type(2)));
typedef short s8v __attribute__((ext_vector_type(8)));
typedef __bf16 b2v __attribute__((ext_vector_type(2)));
typedef __amdgpu_buffer_rsrc_t rsrc_t;
#define DEVI __device__ __forceinline__
#define RLX_AGENT __ATOMIC_RELAXED, __HIP_MEMORY_SCOPE_AGENT

#define LAG 6
#define ZEARLY 0
#define GXS 1
#define PD 7
#define PP 0
#define USTREAM 1
constexpr int NSEG = 64, NT = NSEG + LAG, AD = 4;
constexpr unsigned SENT = 0xFFFFFFFFu;
constexpr unsigned SPINCAP = 1u << 20;
constexpr float LOG2E = 1.4426950408889634f;
constexpr float NEG = -3.0e38f;

constexpr unsigned KB = 1024u, MB = 1024u * 1024u;
constexpr unsigned FLAG_BYTES = 8 * KB;
constexpr unsigned F_U = 1024, F_XR = 7168;
constexpr unsigned OOB = 0x80000000u;
constexpr unsigned F_ABORT = 0, F_SIDE = 128;
constexpr unsigned RING_BYTES = 1 * MB;
constexpr unsigned L_MEMH = 0, L_MEMC = 64 * KB, L_MEMG = 128 * KB, L_S = 192 * KB, L_E = 256 * KB, L_XS = 384 * KB, L_PROBE = 504 * KB;
constexpr unsigned WG_OFF = 4 * MB;
constexpr unsigned WG_BYTES = 16 * 14 * 2 * 14 * KB;
constexpr unsigned WU_OFF = WG_OFF + WG_BYTES;
constexpr unsigned WQ_OFF = WU_OFF + WG_BYTES;
constexpr unsigned WO_OFF = WQ_OFF + 16 * 6 * 14 * KB;
constexpr unsigned SEGX = 28 * KB;
constexpr unsigned R_XN = 0, R_XR = SEGX, R_OA = 2 * SEGX, R_X2 = 3 * SEGX, R_ETT = 4 * SEGX, R_ETS = R_ETT + 32 * KB, R_STM = R_ETS + 4 * KB,
                   R_ZB = R_STM + 32 * KB, REC = R_ZB + 112 * KB;
constexpr unsigned REC_OFF = WO_OFF + 392 * KB;
constexpr unsigned SEGG = 448 * KB;
constexpr unsigned U_OFF = REC_OFF + 2 * NSEG * REC;
constexpr unsigned WS_END = U_OFF + 2 * NSEG * SEGG;
static_assert(WS_END < 200u * MB, "workspace");

DEVI f4 mfma32(v4u a, v4u b, f4 c) {
  return __builtin_amdgcn_mfma_f32_16x16x32_bf16(__builtin_bit_cast(s8v, a), __builtin_bit_cast(s8v, b), c, 0, 0, 0);
}
DEVI unsigned pk2(float a, float b) {
  f2 v = {a, b};
  return __builtin_bit_cast(unsigned, __builtin_convertvector(v, b2v));
}
DEVI float bflo(unsigned u) { return __uint_as_float(u << 16); }
DEVI float bfhi(unsigned u) { return __uint_as_float(u & 0xFFFF0000u); }
DEVI float rbf(float a) { return bflo(pk2(a, 0.f)); }
DEVI v4u pk8(f4 a, f4 b) { return v4u{pk2(a[0], a[1]), pk2(a[2], a[3]), pk2(b[0], b[1]), pk2(b[2], b[3])}; }
DEVI v2u pk4(f4 a) { return v2u{pk2(a[0], a[1]), pk2(a[2], a[3])}; }
DEVI v4u resid8(v4u x, f4 a, f4 b) {
  const v4u y = pk8(a, b);
  return v4u{pk2(bflo(x.x) + bflo(y.x), bfhi(x.x) + bfhi(y.x)), pk2(bflo(x.y) + bflo(y.y), bfhi(x.y) + bfhi(y.y)),
             pk2(bflo(x.z) + bflo(y.z), bfhi(x.z) + bfhi(y.z)), pk2(bflo(x.w) + bflo(y.w), bfhi(x.w) + bfhi(y.w))};
}
DEVI unsigned ns(unsigned x) { return min(x, 0xFFFFFFFEu); }
DEVI v4u ns4(v4u x) { return v4u{ns(x.x), ns(x.y), ns(x.z), ns(x.w)}; }
DEVI v2u ns2(v2u x) { return v2u{ns(x.x), ns(x.y)}; }
DEVI v4u f2u(f4 x) { return v4u{__float_as_uint(x[0]), __float_as_uint(x[1]), __float_as_uint(x[2]), __float_as_uint(x[3])}; }
DEVI f4 u2f(v4u x) { return f4{__uint_as_float(x.x), __uint_as_float(x.y), __uint_as_float(x.z), __uint_as_float(x.w)}; }
DEVI unsigned mx4(unsigned m, v4u v) { return max(max(max(max(m, v.x), v.y), v.z), v.w); }
DEVI unsigned mx2(unsigned m, v2u v) { return max(max(m, v.x), v.y); }
DEVI bool anysent(unsigned m) { return __builtin_amdgcn_ballot_w64(m == SENT) != 0ull; }
DEVI float ex2(float x) { return __builtin_amdgcn_exp2f(x); }
DEVI float rcp(float x) { return __builtin_amdgcn_rcpf(x); }
DEVI float rsq(float x) { return __builtin_amdgcn_rsqf(x); }
DEVI float mx3(float a, float b, float c) { return __builtin_fmaxf(__builtin_fmaxf(a, b), c); }
DEVI float mxf4(f4 a, float m) { return mx3(a[2], a[3], mx3(a[0], a[1], m)); }
DEVI float dot2(unsigned a, unsigned b, float c) {
  return __builtin_amdgcn_fdot2_f32_bf16(__builtin_bit_cast(b2v, a), __builtin_bit_cast(b2v, b), c, false);
}
DEVI void swap16(unsigned& a, unsigned& b) { auto r = __builtin_amdgcn_permlane16_swap(a, b, false, false); a = r[0]; b = r[1]; }
DEVI void swap32(unsigned& a, unsigned& b) { auto r = __builtin_amdgcn_permlane32_swap(a, b, false, false); a = r[0]; b = r[1]; }
DEVI float rmax4(float x) {
  unsigned a = __float_as_uint(x), b = a;
  swap16(a, b);
  a = b = __float_as_uint(mx3(__uint_as_float(a), __uint_as_float(b), NEG));
  swap32(a, b);
  return mx3(__uint_as_float(a), __uint_as_float(b), NEG);
}
DEVI float rsum4(float x) {
  unsigned a = __float_as_uint(x), b = a;
  swap16(a, b);
  a = b = __float_as_uint(__uint_as_float(a) + __uint_as_float(b));
  swap32(a, b);
  return __uint_as_float(a) + __uint_as_float(b);
}
DEVI float ror8(float x) { return __uint_as_float(__builtin_amdgcn_update_dpp(0u, __float_as_uint(x), 0x128, 0xF, 0xF, false)); }
DEVI void rbcast4(float x, float* o) {
  unsigned a = __float_as_uint(x), b = a;
  swap16(a, b);
  unsigned a2 = a, b2 = b;
  swap32(a, a2);
  swap32(b, b2);
  o[0] = __uint_as_float(a); o[1] = __uint_as_float(b); o[2] = __uint_as_float(a2); o[3] = __uint_as_float(b2);
}

DEVI v4u ld16(rsrc_t r, unsigned off) { return __builtin_amdgcn_raw_buffer_load_b128(r, off, 0, 0); }
DEVI v4u ld16c(rsrc_t r, unsigned off) { return __builtin_amdgcn_raw_buffer_load_b128(r, off, 0, 16); }
DEVI v2u ld8c(rsrc_t r, unsigned off) { return __builtin_amdgcn_raw_buffer_load_b64(r, off, 0, 16); }
DEVI unsigned ld4c(rsrc_t r, unsigned off) { return __builtin_amdgcn_raw_buffer_load_b32(r, off, 0, 16); }
DEVI void st16c(rsrc_t r, unsigned off, v4u v) { __builtin_amdgcn_raw_buffer_store_b128(v, r, off, 0, 16); }
DEVI void st8c(rsrc_t r, unsigned off, v2u v) { __builtin_amdgcn_raw_buffer_store_b64(v, r, off, 0, 16); }
DEVI void st4c(rsrc_t r, unsigned off, unsigned v) { __builtin_amdgcn_raw_buffer_store_b32(v, r, off, 0, 16); }
#define PIN_A(x) asm volatile("" : "+a"(x))
#define TOUCH(x) asm volatile("" :: "v"(x))
#define PIN_V(x) asm volatile("" : "+v"(x))
#define DRAIN() asm volatile("s_waitcnt vmcnt(0)" ::: "memory")
#define CBAR() asm volatile("" ::: "memory")
#define SB() __builtin_amdgcn_sched_barrier(0)
#define BAR() do { asm volatile("s_waitcnt lgkmcnt(0)" ::: "memory"); __builtin_amdgcn_s_barrier(); asm volatile("" ::: "memory"); } while (0)
#define LDS16(p) (*(v4u*)(p))
#define LDSF4(p) (*(f4*)(p))
#define S4 (v4u{SENT, SENT, SENT, SENT})
#define S2 (v2u{SENT, SENT})
#define Z4 (f4{0.f, 0.f, 0.f, 0.f})

#define STAMP(it, e) do { } while (0)
#define STAMPC(it, e) do { } while (0)
struct Ctx {
  unsigned char* ws;
  rsrc_t rs;
  unsigned ring;
  int tid, lane, wave;
  bool dead;
};
DEVI bool give_up(Ctx& c, unsigned& spins) {
  if (c.dead) return true;
  ++spins;
  bool d = false;
  if (spins > SPINCAP) { __hip_atomic_store((unsigned*)(c.ws + F_ABORT), 1u, RLX_AGENT); d = true; }
  else if ((spins & 255u) == 0u) d = __builtin_amdgcn_readfirstlane(__hip_atomic_load((unsigned*)(c.ws + F_ABORT), RLX_AGENT)) != 0u;
  c.dead = d;
  return d;
}
#define POLL2(FIRST, BADEXPR, RELOAD, SLEEP) do { if (FIRST) { unsigned spins_ = 0; do { if (give_up(c, spins_)) break; if (SLEEP) __builtin_amdgcn_s_sleep(SLEEP); CBAR(); RELOAD; } while (BADEXPR); } } while (0)
#define POLL(BADEXPR, RELOAD, SLEEP) do { unsigned spins_ = 0; while (BADEXPR) { if (give_up(c, spins_)) break; if (SLEEP) __builtin_amdgcn_s_sleep(SLEEP); CBAR(); RELOAD; } } while (0)

template <class LD>
DEVI void poll2(Ctx& c, v4u* r, LD ld) {
  v4u a[4] = {r[0], r[1], r[2], r[3]}, b[4];
  unsigned spins = 0;
  __builtin_amdgcn_s_sleep(PD);
  while (true) {
    CBAR();
    ld(b);
    SB();
    if (!anysent(mx4(mx4(mx4(mx4(0u, a[0]), a[1]), a[2]), a[3])) || give_up(c, spins)) {
#pragma unroll
      for (int k = 0; k < 4; ++k) r[k] = a[k];
      break;
    }
    CBAR();
    ld(a);
    SB();
    if (!anysent(mx4(mx4(mx4(mx4(0u, b[0]), b[1]), b[2]), b[3]))) {
#pragma unroll
      for (int k = 0; k < 4; ++k) r[k] = b[k];
      break;
    }
  }
}

DEVI unsigned rmap(const Ctx& c, unsigned lo, unsigned side) { return c.ring + ((lo >> 13) << 14) + (side << 13) + (lo & 8191u); }
DEVI unsigned rec_off(int l, int s) { return REC_OFF + (unsigned)(l * NSEG + s) * REC; }
DEVI unsigned rec_in(int l, int s) { return (s >= 0 && s < NSEG) ? rec_off(l, s) : OOB; }
DEVI unsigned u_in(int l, int s) { return (s >= 0 && s < NSEG) ? U_OFF + (unsigned)(l * NSEG + s) * SEGG : OOB; }
DEVI int tok_of(int tau, int m) { return 8 * (m >> 2) + 4 * tau + (m & 3); }

DEVI void probe_side(Ctx& c, int g) {
  if (c.wave != 0) return;
  unsigned best[2] = {0xFFFFFFFFu, 0xFFFFFFFFu};
#pragma unroll 1
  for (int k = 0; k < 4; ++k) {
#pragma unroll
    for (int sd = 0; sd < 2; ++sd) {
      DRAIN();
      const unsigned t0 = (unsigned)__builtin_amdgcn_s_memtime();
      if (c.lane < 8) st16c(c.rs, rmap(c, L_PROBE + g * 256 + c.lane * 16, sd), S4);
      DRAIN();
      const unsigned t1 = (unsigned)__builtin_amdgcn_s_memtime();
      best[sd] = min(best[sd], t1 - t0);
    }
  }
  if (c.lane == 0) st4c(c.rs, F_SIDE + g * 128, best[1] < best[0] ? 2u : 1u);
}
DEVI unsigned get_sides(Ctx& c) {
  const unsigned o = F_SIDE + min(c.lane, 4) * 128;
  unsigned v = ld4c(c.rs, o);
  POLL(__builtin_amdgcn_ballot_w64(v == 0u) != 0ull, v = ld4c(c.rs, o), 2);
  return (unsigned)__builtin_amdgcn_ballot_w64(v == 2u) & 31u;
}

DEVI void role_H(Ctx& c, int h, int a, unsigned char* lds) {
  const int lane = c.lane, w = c.wave;
  const int i16 = lane & 15, q = lane >> 4;
  unsigned char* lmem = lds;
  unsigned char* lred = lds + 8 * KB;
  unsigned char* ltb = lds + 24 * KB;
  const int odd = w & 1;
  const int ks0 = (w >> 1) * 7 + odd * 4, ksx = (w >> 1) * 7 + 3;
  if (h == 0 && a == 0) probe_side(c, 0);
  v4u W[42], Wx[8];
#pragma unroll
  for (int lp = 0; lp < 7; ++lp) {
    const int gp = odd ? 6 - lp : lp;
    const unsigned b0 = WG_OFF + (unsigned)(h * 14 + 7 * a + gp) * 28 * KB + lane * 16;
#pragma unroll
    for (int t = 0; t < 2; ++t)
#pragma unroll
      for (int k = 0; k < 3; ++k) W[(lp * 2 + t) * 3 + k] = ld16(c.rs, b0 + (t * 14 + ks0 + k) * KB);
    if (lp < 4) {
#pragma unroll
      for (int t = 0; t < 2; ++t) {
        Wx[lp * 2 + t] = ld16(c.rs, b0 + (t * 14 + ksx) * KB);
        if (lp == 3 && t != odd) Wx[lp * 2 + t] = v4u{0u, 0u, 0u, 0u};
      }
    }
  }
#pragma unroll
  for (int f = 0; f < 42; ++f) PIN_A(W[f]);
#pragma unroll
  for (int f = 0; f < 8; ++f) PIN_A(Wx[f]);
  const unsigned sides = get_sides(c);
  unsigned mo[4];
#pragma unroll
  for (int k = 0; k < 4; ++k) mo[k] = rmap(c, L_MEMH + (k < 3 ? ks0 + k : ksx) * KB + lane * 16, sides & 1);
  const unsigned so0 = rmap(c, L_S + ((a * 2 + (q & 1)) * 16 + h) * 256 + i16 * 16, 0);
  const unsigned sd0 = ((sides >> 1) & 1) << 13, sd1 = ((sides >> 2) & 1) << 13;
  const bool sval = (q >> 1) == (i16 >> 3);
  const bool stager = (w >> 1) == a;
  const int gp1 = (w < 3) ? w + 4 : 3;
  v4u X[8];
  auto load_x = [&](int t) {
#pragma unroll
    for (int l = 0; l < 2; ++l) {
      const unsigned xb = rec_in(l, t - l * LAG) + R_XN + (unsigned)(7 * a) * KB + lane * 16;
#pragma unroll
      for (int tau = 0; tau < 2; ++tau) {
        X[(l * 2 + tau) * 2 + 0] = ld16c(c.rs, xb + (tau * 14 + w) * KB);
        X[(l * 2 + tau) * 2 + 1] = ld16c(c.rs, xb + (tau * 14 + gp1) * KB);
      }
    }
  };
  CBAR();
  load_x(0);
  for (int t = 0; t < NT; ++t) {
    const unsigned slot = (unsigned)(t & 3) * 32 * KB;
    STAMP(t, 0);
    v4u m[4];
    CBAR();
#pragma unroll
    for (int k = 0; k < 4; ++k) m[k] = ld16c(c.rs, mo[k] + slot);
    POLL(anysent(mx4(mx4(mx4(mx4(0u, m[0]), m[1]), m[2]), m[3])),
         { _Pragma("unroll") for (int k = 0; k < 4; ++k) m[k] = ld16c(c.rs, mo[k] + slot); }, 0);
    STAMP(t, 1);
    if (stager) {
#pragma unroll
      for (int k = 0; k < 4; ++k) LDS16(lmem + ((k < 3 ? ks0 + k : ksx) - 7 * a) * KB + lane * 16) = m[k];
    }
    SB();
    v4u tb[7], mu[7];
    f4 acc[7][2];
    f4 sacc = Z4;
#pragma unroll
    for (int lp = 0; lp < 7; ++lp) {
#define HPK(e) do { if (lp >= 2) { SB(); tb[lp - 2][e] = pk2(acc[lp - 2][(e) >> 1][((e) & 1) * 2], acc[lp - 2][(e) >> 1][((e) & 1) * 2 + 1]); SB(); } } while (0)
      acc[lp][0] = mfma32(W[(lp * 2 + 0) * 3 + 0], m[0], Z4);
      HPK(0);
      acc[lp][1] = mfma32(W[(lp * 2 + 1) * 3 + 0], m[0], Z4);
      HPK(1);
      acc[lp][0] = mfma32(W[(lp * 2 + 0) * 3 + 1], m[1], acc[lp][0]);
      HPK(2);
      acc[lp][1] = mfma32(W[(lp * 2 + 1) * 3 + 1], m[1], acc[lp][1]);
      HPK(3);
      acc[lp][0] = mfma32(W[(lp * 2 + 0) * 3 + 2], m[2], acc[lp][0]);
      if (lp == 6) { SB(); tb[5][0] = pk2(acc[5][0][0], acc[5][0][1]); SB(); }
      acc[lp][1] = mfma32(W[(lp * 2 + 1) * 3 + 2], m[2], acc[lp][1]);
      if (lp == 6) { SB(); tb[5][1] = pk2(acc[5][0][2], acc[5][0][3]); SB(); }
      if (lp < 4) {
        acc[lp][0] = mfma32(Wx[lp * 2 + 0], m[3], acc[lp][0]);
        acc[lp][1] = mfma32(Wx[lp * 2 + 1], m[3], acc[lp][1]);
      }
      if (lp >= 2) sacc = mfma32(tb[lp - 2], mu[lp - 2], sacc);
      SB();
      if (lp == 0) {
        BAR();
#pragma unroll
        for (int k = 0; k < 7; ++k) mu[k] = LDS16(lmem + (odd ? 6 - k : k) * KB + lane * 16);
        SB();
      }
    }
    tb[5][2] = pk2(acc[5][1][0], acc[5][1][1]);
    tb[5][3] = pk2(acc[5][1][2], acc[5][1][3]);
    sacc = mfma32(tb[5], mu[5], sacc);
    SB();
    tb[6] = pk8(acc[6][0], acc[6][1]);
    sacc = mfma32(tb[6], mu[6], sacc);
    if (w != 0) LDSF4(lred + w * KB + lane * 16) = sacc;
    BAR();
    if (w == 0) {
      const f4 s = (sacc + LDSF4(lred + KB + lane * 16)) + (LDSF4(lred + 2 * KB + lane * 16) + LDSF4(lred + 3 * KB + lane * 16));
      if (sval) {
        st16c(c.rs, so0 + slot + ((t & 1) ? sd1 : sd0), ns4(f2u(s)));
        if (t >= 1) st16c(c.rs, so0 + (unsigned)((t - 1) & 3) * 32 * KB + ((t & 1) ? sd0 : sd1), S4);
      }
    }
    STAMP(t, 2);
#pragma unroll
    for (int lp = 0; lp < 7; ++lp) LDS16(ltb + (w * 7 + (odd ? 6 - lp : lp)) * KB + lane * 16) = tb[lp];
    BAR();
    v4u tp[8];
#pragma unroll
    for (int ww = 0; ww < 4; ++ww) {
      tp[ww * 2 + 0] = LDS16(ltb + (ww * 7 + w) * KB + lane * 16);
      tp[ww * 2 + 1] = LDS16(ltb + (ww * 7 + gp1) * KB + lane * 16);
      if (w == 3) tp[ww * 2 + 1] = v4u{0u, 0u, 0u, 0u};
    }
    POLL(anysent(mx4(mx4(mx4(mx4(mx4(mx4(mx4(mx4(0u, X[0]), X[1]), X[2]), X[3]), X[4]), X[5]), X[6]), X[7])), load_x(t), 1);
    SB();
    {
      f4 s[4] = {Z4, Z4, Z4, Z4};
#pragma unroll
      for (int k = 0; k < 8; ++k)
#pragma unroll
        for (int lt = 0; lt < 4; ++lt) s[lt] = mfma32(tp[k], X[lt * 2 + (k & 1)], s[lt]);
#pragma unroll
      for (int lt = 0; lt < 4; ++lt) LDSF4(lred + (lt * 4 + w) * KB + lane * 16) = s[lt];
    }
    BAR();
    {
      const int l = w >> 1, tau = w & 1, s = t - l * LAG;
      const f4 v = (LDSF4(lred + (w * 4 + 0) * KB + lane * 16) + LDSF4(lred + (w * 4 + 1) * KB + lane * 16)) +
                   (LDSF4(lred + (w * 4 + 2) * KB + lane * 16) + LDSF4(lred + (w * 4 + 3) * KB + lane * 16));
      if ((q >> 1) == l) {
        const unsigned o = R_STM + (unsigned)((((a * 2 + (q & 1)) * 2 + tau) * 16 + h) * 256 + i16 * 16);
        st16c(c.rs, rec_in(l, s) + o, ns4(f2u(v)));
        if (s >= 0) st16c(c.rs, rec_in(l, s + AD) + o, S4);
      }
    }
    CBAR();
    load_x(t + 1);
    STAMP(t, 3);
  }
}

DEVI void role_CV(Ctx& c, int j, int p, unsigned char* lds, const unsigned char* mem0) {
  const int lane = c.lane, w = c.wave;
  const int i16 = lane & 15, q = lane >> 4;
  unsigned char* lmem = lds;
  unsigned char* lred = lds + 16 * KB;
  unsigned char* lXb = lds + 20 * KB;
  if (j == 0) probe_side(c, 1 + p);
  v4u W[56];
  {
    const unsigned b0 = WU_OFF + (unsigned)(j * 16 + 4 * w) * 14 * KB + lane * 16;
#pragma unroll
    for (int f = 0; f < 56; ++f) W[f] = ld16(c.rs, b0 + f * KB);
#pragma unroll
    for (int f = 0; f < 56; ++f) PIN_A(W[f]);
  }
  const v2u m0 = ns2(*(const v2u*)(mem0 + (size_t)(i16 & 7) * 896 + (j * 16 + 4 * q) * 2));
  const unsigned sides = get_sides(c);
  const unsigned sC = (sides >> (1 + p)) & 1;
  unsigned mo[4], so[4], eo[4];
#pragma unroll
  for (int k = 0; k < 4; ++k) {
    mo[k] = rmap(c, L_MEMC + (k < 3 ? k * 4096 + c.tid * 16 : 12288 + (c.tid & 127) * 16), sC);
    so[k] = rmap(c, L_S + (k * 16 + 4 * w) * 256 + lane * 16, sC);
    eo[k] = rmap(c, L_E + (4 * w + k) * KB + lane * 16, sC);
  }
  const unsigned xo = rmap(c, L_XS + ((4 * w + q) * 2 + (i16 >> 3)) * 128 + (i16 & 7) * 8, sC);
  const unsigned pf = (unsigned)(j >> 1) * KB + (unsigned)((2 * (j & 1) + (q >> 1)) * 16 + i16) * 16 + (q & 1) * 8;
  const unsigned pg = (unsigned)(i16 >> 3) * 8 * KB + (unsigned)(j >> 1) * 512 + (unsigned)(2 * (j & 1) + (q >> 1)) * 128 + (i16 & 7) * 16 + (q & 1) * 8;
  const unsigned pH = rmap(c, L_MEMH + pf, sides & 1), pC = rmap(c, L_MEMC + pf, (sides >> (2 - p)) & 1), pG = rmap(c, L_MEMG + pg, (sides >> (4 - p)) & 1);
  const unsigned pR = (w == 1) ? pH : (w == 2) ? pG : (w == 3) ? pC : OOB;
  const unsigned zo = R_ZB + (unsigned)(j * 4 + w) * KB + lane * 16;
  const unsigned uo = (unsigned)(j * 16 + 4 * w) * KB + lane * 16;
  const unsigned fo = (lane == 0) ? F_U + (unsigned)(p * 28 + j) * 64 + w * 4 : OOB;
#define XBAD anysent(mx4(mx4(mx4(mx4(mx4(mx4(mx4(0u, x[0]), x[1]), x[2]), x[3]), x[4]), x[5]), x[6]))
  v4u U[8], x[7];
  auto load_x = [&](int l, int s) {
    const unsigned xb = rec_in(l, s) + R_XN + c.tid * 16;
#pragma unroll
    for (int f = 0; f < 7; ++f) x[f] = ld16c(c.rs, xb + f * 4096);
  };
  auto stage_x = [&](int l) {
#pragma unroll
    for (int f = 0; f < 7; ++f) LDS16(lXb + l * 28 * KB + f * 4096 + c.tid * 16) = x[f];
  };
  unsigned* lhave = (unsigned*)(lds + 15 * KB);
  bool have = false, allhave = false;
  for (int t = p; t < NT; t += 2) {
    const unsigned slot = (unsigned)(t & 3) * 32 * KB, eslot = (unsigned)(t & 7) * 32 * KB, xslot = (unsigned)(t & 7) * 16 * KB;
    const int s0 = t, s1 = t - LAG;
    STAMP(t, 0);
    if (!allhave) {
      if (!have) {
        CBAR();
        load_x(0, s0);
        POLL(XBAD, load_x(0, s0), 1);
        stage_x(0);
      }
      BAR();
    }
    CBAR();
    STAMPC(t, 7);
    load_x(1, s1);
    SB();
    STAMP(72 + (t >> 1), 0);
    f4 acc[8];
    const unsigned ub0 = u_in(0, s0) + uo, za0 = rec_in(0, s0 + AD) + zo, xb0 = rec_in(0, s0 + 2) + R_XN + c.tid * 16;
    auto side_op = [&](int k) {
      if (k < 4) st16c(c.rs, ub0 + k * KB, U[k]);
      else if (k == 4) st16c(c.rs, za0, S4);
      else if (k < 12) x[k - 5] = ld16c(c.rs, xb0 + (k - 5) * 4096);
    };
#pragma unroll
    for (int l = 0; l < 2; ++l) {
      const int s = l ? s1 : s0;
      const bool act = (s >= 0 && s < NSEG);
#pragma unroll
      for (int k = 0; k < 8; ++k) acc[k] = Z4;
      if (act) {
        const unsigned char* xp = lXb + l * 28 * KB + lane * 16;
        v4u xa = LDS16(xp), xb = LDS16(xp + 14 * KB), ya = LDS16(xp + KB), yb = LDS16(xp + 15 * KB);
        SB();
#pragma unroll
        for (int ks = 0; ks < 14; ++ks) {
          const v4u x0 = xa, x1 = xb;
          xa = ya; xb = yb;
          if (ks + 2 < 14) { ya = LDS16(xp + (ks + 2) * KB); yb = LDS16(xp + (16 + ks) * KB); }
#pragma unroll
          for (int hh = 0; hh < 4; ++hh) {
            acc[hh * 2 + 0] = mfma32(x0, W[hh * 14 + ks], acc[hh * 2 + 0]);
            acc[hh * 2 + 1] = mfma32(x1, W[hh * 14 + ks], acc[hh * 2 + 1]);
            if (l == 1 && hh == 1) { SB(); side_op(ks); SB(); }
          }
          SB();
        }
      }
      else if (l == 1) {
#pragma unroll
        for (int k = 0; k < 12; ++k) side_op(k);
      }
      STAMP(72 + (t >> 1), l ? 6 : 1);
      if (l == 0) {
        const bool bad = XBAD;
#pragma unroll
        for (int f = 0; f < 7; ++f) {
          LDS16(lXb + 28 * KB + f * 4096 + c.tid * 16) = x[f];
          if (f < 4) { U[f] = pk8(acc[f * 2], acc[f * 2 + 1]); PIN_V(U[f]); }
          SB();
        }
        STAMP(72 + (t >> 1), 2);
        if (bad) {
          POLL2(true, XBAD, load_x(1, s1), 1);
          stage_x(1);
        }
        CBAR();
        STAMP(72 + (t >> 1), 4);
        BAR();
        SB();
        STAMP(72 + (t >> 1), 5);
      }
    }
    STAMP(t, 1);
    v4u m[4];
    CBAR();
#pragma unroll
    for (int k = 0; k < 4; ++k) m[k] = ld16c(c.rs, mo[k] + slot);
    SB();
#pragma unroll
    for (int hh = 0; hh < 4; ++hh) { U[4 + hh] = pk8(acc[hh * 2], acc[hh * 2 + 1]); PIN_V(U[4 + hh]); }
    SB();
    have = !XBAD;
    if (lane == 0) lhave[w] = have ? 1u : 0u;
    SB();
    STAMP(72 + (t >> 1), 7);
    POLL(anysent(mx4(mx4(mx4(mx4(0u, m[0]), m[1]), m[2]), m[3])),
         { _Pragma("unroll") for (int k = 0; k < 4; ++k) m[k] = ld16c(c.rs, mo[k] + slot); }, 0);
#pragma unroll
    for (int k = 0; k < 3; ++k) LDS16(lmem + k * 4096 + c.tid * 16) = m[k];
    LDS16(lmem + 12288 + (c.tid & 127) * 16) = m[3];
    DRAIN();
    st4c(c.rs, fo, (unsigned)min(s0 + 1, NSEG));
    {
      const unsigned ub = u_in(1, s1) + uo;
#pragma unroll
      for (int hh = 0; hh < 4; ++hh) st16c(c.rs, ub + hh * KB, U[4 + hh]);
      st16c(c.rs, (s1 >= 0 ? rec_in(1, s1 + AD) : OOB) + zo, S4);
    }
    STAMP(t, 2);
    BAR();
    {
      const v4u hv = LDS16((unsigned char*)lhave);
      allhave = __builtin_amdgcn_readfirstlane(hv.x & hv.y & hv.z & hv.w) != 0u;
    }
    if (t >= 1) st8c(c.rs, pR + (unsigned)((t - 1) & 3) * 32 * KB, S2);
    v4u zA[2];
    {
      v4u mf[14];
#pragma unroll
      for (int ks = 0; ks < 14; ++ks) mf[ks] = LDS16(lmem + ks * KB + lane * 16);
      SB();
      f4 z[4] = {Z4, Z4, Z4, Z4};
#pragma unroll
      for (int ks = 0; ks < 14; ++ks) {
#pragma unroll
        for (int hh = 0; hh < 4; ++hh) z[hh] = mfma32(mf[ks], W[hh * 14 + ks], z[hh]);
        SB();
      }
      unsigned zl[4], zh[4];
#pragma unroll
      for (int hh = 0; hh < 4; ++hh) { zl[hh] = pk2(z[hh][0], z[hh][1]); zh[hh] = pk2(z[hh][2], z[hh][3]); }
      swap16(zl[0], zl[1]); swap16(zl[2], zl[3]); swap32(zl[0], zl[2]); swap32(zl[1], zl[3]);
      swap16(zh[0], zh[1]); swap16(zh[2], zh[3]); swap32(zh[0], zh[2]); swap32(zh[1], zh[3]);
      zA[0] = v4u{zl[0], zh[0], zl[1], zh[1]};
      zA[1] = v4u{zl[2], zh[2], zl[3], zh[3]};
    }
    STAMP(t, 3);
    v4u E[4], sv[4];
    v2u xs;
    auto load_e = [&]() {
#pragma unroll
      for (int hh = 0; hh < 4; ++hh) E[hh] = ld16c(c.rs, eo[hh] + eslot);
      xs = ld8c(c.rs, xo + xslot);
    };
    auto load_s = [&]() {
#pragma unroll
      for (int k = 0; k < 4; ++k) sv[k] = ld16c(c.rs, so[k] + slot);
    };
    CBAR();
    load_e();
    SB();
    stage_x(0);
    SB();
    load_s();
    POLL(anysent(mx2(mx4(mx4(mx4(mx4(0u, E[0]), E[1]), E[2]), E[3]), xs)), { load_e(); load_s(); }, 0);
    SB();
    f4 ox[4];
#pragma unroll
    for (int hh = 0; hh < 4; ++hh) {
      const f4 o0 = mfma32(U[hh], E[hh], Z4), o1 = mfma32(U[4 + hh], E[hh], Z4);
      ox[hh] = (i16 < 8) ? o0 : o1;
    }
#pragma unroll
    for (int hh = 0; hh < 4; ++hh) PIN_V(ox[hh]);
    STAMP(t, 4);
    SB();
    POLL(anysent(mx4(mx4(mx4(mx4(0u, sv[0]), sv[1]), sv[2]), sv[3])), load_s(), 0);
    STAMP(t, 5);
    DRAIN();
    st4c(c.rs, fo + 16, (unsigned)max(s1 + 1, 0));
    {
      f4 a = u2f(sv[0]) + u2f(sv[2]), b = u2f(sv[1]) + u2f(sv[3]);
      const float mxx = __uint_as_float(xs.x), lx = __uint_as_float(xs.y);
      const float mm = mxf4(b, mxf4(a, mxx));
      const float ml = mm * LOG2E;
#pragma unroll
      for (int r = 0; r < 4; ++r) { a[r] = ex2(a[r] * LOG2E - ml); b[r] = ex2(b[r] * LOG2E - ml); }
      const float ax = ex2(mxx * LOG2E - ml);
      const float den = ((a[0] + a[1]) + (a[2] + a[3])) + ((b[0] + b[1]) + (b[2] + b[3])) + ax * lx;
      const float inv = rcp(den);
      const v4u P = pk8(a * inv, b * inv);
      const f4 d0 = mfma32(zA[0], P, Z4), d1 = mfma32(zA[1], P, Z4);
      float wx[4];
      rbcast4(ax * inv, wx);
      f4 acc = (ox[0] * wx[0] + ox[1] * wx[1]) + (ox[2] * wx[2] + ox[3] * wx[3]);
      acc += (i16 < 8) ? d0 : d1;
      if (w != 0) LDSF4(lred + w * KB + lane * 16) = acc;
      BAR();
      if (w == 0) {
        acc = (acc + LDSF4(lred + KB + lane * 16)) + (LDSF4(lred + 2 * KB + lane * 16) + LDSF4(lred + 3 * KB + lane * 16));
        v2u o = ns2(pk4(acc));
        if (i16 >= 8 && t + 1 <= LAG) o = m0;
        const unsigned nslot = (unsigned)((t + 1) & 3) * 32 * KB;
        st8c(c.rs, pH + nslot, o);
        st8c(c.rs, pG + nslot, o);
        st8c(c.rs, pC + nslot, o);
      }
      st16c(c.rs, rec_in(0, s0) + zo, ns4(zA[0]));
      st16c(c.rs, rec_in(1, s1) + zo, ns4(zA[1]));
    }
    STAMP(t, 6);
  }
}

template <int L>
DEVI void role_GX(Ctx& c, int h, int p, unsigned char* lds) {
  const int lane = c.lane, w = c.wave;
  const int i16 = lane & 15, q = lane >> 4;
  unsigned char* lX = lds;
  unsigned char* lG = lds + 28 * KB;
  unsigned char* lred = lds + 60 * KB;
  if (h == 0 && L == 0) probe_side(c, 3 + p);
  v4u W[98];
  {
    const unsigned b0 = WG_OFF + (unsigned)(h * 14 + 3 * w) * 28 * KB + lane * 16;
#pragma unroll
    for (int f = 0; f < 84; ++f) W[f] = ld16(c.rs, b0 + f * KB);
    const unsigned b1 = WG_OFF + (unsigned)(h * 14 + 12 + (w >> 1)) * 28 * KB + (w & 1) * 7 * KB + lane * 16;
#pragma unroll
    for (int k = 0; k < 7; ++k) { W[84 + k] = ld16(c.rs, b1 + k * KB); W[91 + k] = ld16(c.rs, b1 + (14 + k) * KB); }
#pragma unroll
    for (int f = 0; f < 56; ++f) PIN_A(W[f]);
  }
  const unsigned sides = get_sides(c);
  const unsigned sC = (sides >> (1 + p)) & 1;
  const int kx = 7 * (w & 1);
  unsigned mo[4];
#pragma unroll
  for (int k = 0; k < 4; ++k) {
    const int vu = 4 * w + k, ks = vu < 12 ? vu : 12 + ((vu - 12) >> 1);
    mo[k] = rmap(c, L_MEMG + L * 8 * KB + ks * 512 + q * 128 + (i16 & 7) * 16, (sides >> (3 + p)) & 1);
  }
  const unsigned eo = rmap(c, L_E + h * KB + (q * 16 + L * 8 + (i16 & 7)) * 16 + (i16 >> 3) * 8, sC);
  const unsigned xo = rmap(c, L_XS + (h * 2 + L) * 128 + (i16 & 7) * 8, sC);
  v4u x[7];
  auto load_x = [&](int s) {
    const unsigned xb = rec_in(L, s) + R_XN + c.tid * 16;
#pragma unroll
    for (int f = 0; f < 7; ++f) x[f] = ld16c(c.rs, xb + f * 4096);
  };
  bool have = false;
  for (int t = p; t < NT; t += 2) {
    const int s = t - L * LAG;
    const bool act = (s >= 0 && s < NSEG);
    v4u g[8];
    STAMP(t, 0);
    if (act) {
      if (!have) {
        CBAR();
        load_x(s);
        POLL(anysent(mx4(mx4(mx4(mx4(mx4(mx4(mx4(0u, x[0]), x[1]), x[2]), x[3]), x[4]), x[5]), x[6])), load_x(s), 1);
      }
#pragma unroll
      for (int f = 0; f < 7; ++f) LDS16(lX + f * 4096 + c.tid * 16) = x[f];
      BAR();
      STAMP(t, 1);
#pragma unroll 1
      for (int tau = 0; tau < 2; ++tau) {
        const unsigned char* xp = lX + tau * 14 * KB + lane * 16;
        const unsigned xnext = (tau ? rec_in(L, s + 2) : OOB) + R_XN + c.tid * 16;
        f4 acc[8] = {Z4, Z4, Z4, Z4, Z4, Z4, Z4, Z4};
        v4u xs[7];
#pragma unroll
        for (int k = 0; k < 7; ++k) xs[k] = LDS16(xp + (kx + k) * KB);
        v4u x0 = LDS16(xp), x1 = LDS16(xp + KB);
        SB();
#pragma unroll
        for (int k = 0; k < 7; ++k) {
          acc[6] = mfma32(W[84 + k], xs[k], acc[6]);
          acc[7] = mfma32(W[91 + k], xs[k], acc[7]);
        }
        SB();
        unsigned char* gp = lG + tau * 16 * KB + lane * 16;
#pragma unroll
        for (int ks = 0; ks < 14; ++ks) {
          const v4u xk = x0;
          x0 = x1;
          if (ks + 2 < 14) x1 = LDS16(xp + (ks + 2) * KB);
          if (ks == 2) LDS16(gp + (12 + w) * KB) = pk8(acc[6], acc[7]);
#pragma unroll
          for (int pp = 0; pp < 3; ++pp) {
            acc[pp * 2 + 0] = mfma32(W[pp * 28 + ks], xk, acc[pp * 2 + 0]);
            acc[pp * 2 + 1] = mfma32(W[pp * 28 + 14 + ks], xk, acc[pp * 2 + 1]);
            if (L == 0 && pp == 0 && ks >= 3 && ks < 10) { SB(); x[ks - 3] = ld16c(c.rs, xnext + (ks - 3) * 4096); SB(); }
          }
          SB();
        }
#pragma unroll
        for (int pp = 0; pp < 3; ++pp) LDS16(gp + (3 * w + pp) * KB) = pk8(acc[pp * 2], acc[pp * 2 + 1]);
      }
      BAR();
#pragma unroll
      for (int k = 0; k < 4; ++k) { g[k] = LDS16(lG + (4 * w + k) * KB + lane * 16); g[4 + k] = LDS16(lG + (16 + 4 * w + k) * KB + lane * 16); }
    } else {
#pragma unroll
      for (int k = 0; k < 8; ++k) g[k] = v4u{0u, 0u, 0u, 0u};
    }
    STAMP(t, 2);
    {
      const unsigned slot = (unsigned)(t & 3) * 32 * KB;
      v4u m[4];
      CBAR();
#pragma unroll
      for (int k = 0; k < 4; ++k) m[k] = ld16c(c.rs, mo[k] + slot);
      POLL(anysent(mx4(mx4(mx4(mx4(0u, m[0]), m[1]), m[2]), m[3])),
           { _Pragma("unroll") for (int k = 0; k < 4; ++k) m[k] = ld16c(c.rs, mo[k] + slot); }, 0);
      STAMP(t, 3);
      f4 a0 = Z4, a1 = Z4;
#pragma unroll
      for (int k = 0; k < 4; ++k) { a0 = mfma32(g[k], m[k], a0); a1 = mfma32(g[4 + k], m[k], a1); }
      LDSF4(lred + 4 * KB + w * KB + lane * 16) = (i16 < 8) ? a0 : a1;
      BAR();
      if (w == 0) {
        const unsigned char* rp = lred + 4 * KB + lane * 16;
        f4 v = (LDSF4(rp) + LDSF4(rp + KB)) + (LDSF4(rp + 2 * KB) + LDSF4(rp + 3 * KB));
        float m1 = mxf4(v, NEG);
        m1 = mx3(m1, ror8(m1), NEG);
        m1 = rmax4(m1);
        const float ml = m1 * LOG2E;
#pragma unroll
        for (int r = 0; r < 4; ++r) v[r] = ex2(v[r] * LOG2E - ml);
        const unsigned eslot = (unsigned)(t & 7) * 32 * KB, xslot = (unsigned)(t & 7) * 16 * KB;
        st8c(c.rs, eo + eslot, act ? ns2(pk4(v)) : v2u{0u, 0u});
        float ls = (v[0] + v[1]) + (v[2] + v[3]);
        ls += ror8(ls);
        ls = rsum4(ls);
        if (lane < 8) st8c(c.rs, xo + xslot, act ? ns2(v2u{__float_as_uint(m1), __float_as_uint(ls)}) : v2u{__float_as_uint(-1.0e30f), 0u});
        if (t >= 2) {
          st8c(c.rs, eo + (unsigned)((t - 2) & 7) * 32 * KB, S2);
          if (lane < 8) st8c(c.rs, xo + (unsigned)((t - 2) & 7) * 16 * KB, S2);
        }
      }
      STAMP(t, 4);
    }
    if (act) {
      const int tq = w & 1, kh = w >> 1;
      f4 a0 = Z4, a1 = Z4;
#pragma unroll
      for (int hf = 0; hf < 3; ++hf) {
        v4u ga[3], gb[3], xq[3];
#pragma unroll
        for (int k = 0; k < 3; ++k)
          if (hf * 3 + k < 8) {
            const int vu = kh * 8 + hf * 3 + k, ks = vu < 12 ? vu : 12 + ((vu - 12) >> 1);
            ga[k] = LDS16(lG + vu * KB + lane * 16);
            gb[k] = LDS16(lG + (16 + vu) * KB + lane * 16);
            xq[k] = LDS16(lX + (tq * 14 + ks) * KB + lane * 16);
          }
        SB();
#pragma unroll
        for (int k = 0; k < 3; ++k)
          if (hf * 3 + k < 8) { a0 = mfma32(ga[k], xq[k], a0); a1 = mfma32(gb[k], xq[k], a1); }
        SB();
      }
      if (!kh) { LDSF4(lred + (tq * 2 + 0) * KB + lane * 16) = a0; LDSF4(lred + (tq * 2 + 1) * KB + lane * 16) = a1; }
      BAR();
      if (L == 0) have = !anysent(mx4(mx4(mx4(mx4(mx4(mx4(mx4(0u, x[0]), x[1]), x[2]), x[3]), x[4]), x[5]), x[6]));
      if (kh) {
        a0 += LDSF4(lred + (tq * 2 + 0) * KB + lane * 16);
        a1 += LDSF4(lred + (tq * 2 + 1) * KB + lane * 16);
        const int lim = tok_of(tq, i16) - 8 * q;
#pragma unroll
        for (int r = 0; r < 4; ++r) {
          a0[r] = (r <= lim) ? a0[r] : NEG;
          a1[r] = (4 + r <= lim) ? a1[r] : NEG;
        }
        const float m = rmax4(mxf4(a1, mxf4(a0, NEG)));
        const float ml = m * LOG2E;
        float lsum = 0.f;
#pragma unroll
        for (int r = 0; r < 4; ++r) {
          a0[r] = (r <= lim) ? ex2(a0[r] * LOG2E - ml) : 0.f;
          a1[r] = (4 + r <= lim) ? ex2(a1[r] * LOG2E - ml) : 0.f;
          lsum += a0[r] + a1[r];
        }
        lsum = rsum4(lsum);
        const unsigned o1 = R_ETT + (unsigned)(h * 2 + tq) * KB + lane * 16, o2 = R_ETS + (unsigned)((tq * 16 + h) * 16 + i16) * 8;
        st16c(c.rs, rec_off(L, s) + o1, ns4(pk8(a0, a1)));
        if (q == 0) st8c(c.rs, rec_off(L, s) + o2, ns2(v2u{__float_as_uint(m), __float_as_uint(lsum)}));
        st16c(c.rs, rec_in(L, s + AD) + o1, S4);
        if (q == 0) st8c(c.rs, rec_in(L, s + AD) + o2, S2);
      }
    }
    STAMP(t, 5);
  }
}

template <int L>
DEVI void role_O(Ctx& c, int tau, int cg, unsigned char* lds, __hip_bfloat16* out) {
  const int lane = c.lane, w = c.wave;
  const int i16 = lane & 15, q = lane >> 4;
  const int ct = 4 * cg + w;
  const unsigned po = (unsigned)(tau * 14 + (ct >> 1)) * KB + (unsigned)((2 * (ct & 1) + (q >> 1)) * 16 + i16) * 16 + (q & 1) * 8;
  v4u U[16], e[4], sv[4], z[4];
  v2u es, xr;
  auto load_u = [&](int s) {
    const unsigned ub = u_in(L, s) + (unsigned)(4 * cg * 16 + 4 * w) * KB + lane * 16;
#pragma unroll
    for (int k = 0; k < 4; ++k)
#pragma unroll
      for (int hh = 0; hh < 4; ++hh) U[k * 4 + hh] = ld16c(c.rs, ub + (k * 16 + hh) * KB);
    xr = ld8c(c.rs, rec_in(L, s) + R_XR + po);
  };
  auto load_e = [&](int s) {
    const unsigned rb = rec_off(L, s);
#pragma unroll
    for (int hh = 0; hh < 4; ++hh) e[hh] = ld16c(c.rs, rb + R_ETT + (unsigned)((4 * w + hh) * 2 + tau) * KB + lane * 16);
    es = ld8c(c.rs, rb + R_ETS + (unsigned)((tau * 16 + 4 * w) * 16 + lane) * 8);
  };
  auto load_s = [&](int s) {
    const unsigned rb = rec_off(L, s);
#pragma unroll
    for (int k = 0; k < 4; ++k) sv[k] = ld16c(c.rs, rb + R_STM + (unsigned)((k * 2 + tau) * 16 + 4 * w) * 256 + lane * 16);
#pragma unroll
    for (int k = 0; k < 4; ++k) z[k] = ld16c(c.rs, rb + R_ZB + (unsigned)((4 * cg + k) * 4 + w) * KB + lane * 16);
  };
  const int fpar = (lane >> 2) & 1;
  const unsigned fo = (lane & 8) == 0 ? F_U + (unsigned)(fpar * 28 + 4 * cg + (lane & 3)) * 64 + L * 16 + w * 4
                                      : F_XR + (unsigned)((L * 2 + tau) * 2 + fpar) * 64 + (lane & 3) * 4;
  int fl = 0;
  for (int s = 0; s < NSEG; ++s) {
    unsigned char* lred = lds + (s & 1) * 16 * KB;
    STAMP(s, 0);
    {
      const int need = (fpar != ((((lane & 8) == 0 ? s + L * LAG : s)) & 1)) ? 0 : s + 1;
      POLL(__builtin_amdgcn_ballot_w64(fl < need) != 0ull, fl = (int)ld4c(c.rs, fo), 1);
      CBAR();
      load_u(s);
    }
    STAMP(s, 1);
    CBAR();
    load_e(s);
    load_s(s);
    fl = (int)ld4c(c.rs, fo);
    POLL(anysent(mx4(mx4(mx4(mx4(mx4(mx4(mx4(mx4(mx2(mx4(mx4(mx4(mx4(0u, e[0]), e[1]), e[2]), e[3]), es), sv[0]), sv[1]), sv[2]), sv[3]), z[0]), z[1]), z[2]), z[3])),
         { load_e(s); load_s(s); fl = (int)ld4c(c.rs, fo); }, 1);
    TOUCH(fl);
    SB();
    f4 ott[16];
#pragma unroll
    for (int k = 0; k < 4; ++k)
#pragma unroll
      for (int hh = 0; hh < 4; ++hh) ott[k * 4 + hh] = mfma32(U[k * 4 + hh], e[hh], Z4);
    STAMP(s, 2);
    {
      f4 a = u2f(sv[0]) + u2f(sv[2]), b = u2f(sv[1]) + u2f(sv[3]);
      const float mt = __uint_as_float(es.x), lt = __uint_as_float(es.y);
      const float mm = mxf4(b, mxf4(a, mt));
      const float ml = mm * LOG2E;
#pragma unroll
      for (int r = 0; r < 4; ++r) { a[r] = ex2(a[r] * LOG2E - ml); b[r] = ex2(b[r] * LOG2E - ml); }
      const float at = ex2(mt * LOG2E - ml);
      const float den = ((a[0] + a[1]) + (a[2] + a[3])) + ((b[0] + b[1]) + (b[2] + b[3])) + at * lt;
      const float inv = rcp(den);
      const v4u P = pk8(a * inv, b * inv);
      float wt[4];
      rbcast4(at * inv, wt);
#pragma unroll
      for (int k = 0; k < 4; ++k) {
        f4 acc = mfma32(z[k], P, Z4);
        acc += (ott[k * 4 + 0] * wt[0] + ott[k * 4 + 1] * wt[1]) + (ott[k * 4 + 2] * wt[2] + ott[k * 4 + 3] * wt[3]);
        LDSF4(lred + (k * 4 + w) * KB + lane * 16) = acc;
      }
    }
    BAR();
    {
      const unsigned char* rp = lred + w * 4 * KB + lane * 16;
      const f4 acc = (LDSF4(rp) + LDSF4(rp + KB)) + (LDSF4(rp + 2 * KB) + LDSF4(rp + 3 * KB));
      const v2u y = pk4(acc);
      const v2u o = v2u{pk2(bflo(xr.x) + bflo(y.x), bfhi(xr.x) + bfhi(y.x)), pk2(bflo(xr.y) + bflo(y.y), bfhi(xr.y) + bfhi(y.y))};
      if (L == 0) {
        st8c(c.rs, rec_off(0, s) + R_X2 + po, ns2(o));
        st8c(c.rs, rec_in(0, s + AD) + R_X2 + po, S2);
      } else {
        *(v2u*)((unsigned char*)out + (size_t)(s * 32 + tok_of(tau, i16)) * 896 + (ct * 16 + 4 * q) * 2) = o;
      }
    }
    STAMP(s, 3);
  }
}

template <int L>
DEVI void role_AH(Ctx& c, int a, unsigned char* lds, const unsigned char* x0) {
  const int lane = c.lane, w = c.wave;
  const int i16 = lane & 15, q = lane >> 4;
  unsigned char* lX = lds;
  unsigned char* lK = lds + 28 * KB;
  unsigned char* lV = lds + 46 * KB;
  unsigned char* lQ = lds + 64 * KB;
  unsigned char* lst = lds + 66 * KB;
  unsigned char* lmg = lds + 67 * KB;
  unsigned char* lms = lds + 71 * KB;
  const int tau = w & 1, g = w >> 1;
  v4u W[42];
#pragma unroll
  for (int tl = 0; tl < 3; ++tl) {
    const unsigned b0 = WQ_OFF + (unsigned)(a * 6 + (tl < 2 ? 2 * g + tl : 4 + g)) * 14 * KB + lane * 16;
#pragma unroll
    for (int k = 0; k < 14; ++k) W[tl * 14 + k] = ld16(c.rs, b0 + (unsigned)(k < 7 ? 7 * g + k : 7 * (1 - g) + k - 7) * KB);
  }
  for (int o = c.tid * 16; o < 36 * (int)KB; o += 4096) LDS16(lK + o) = v4u{0u, 0u, 0u, 0u};
  f4 cs0 = Z4, cs1 = Z4, cs2 = Z4;
  {
    const v4u one = {0x3F803F80u, 0x3F803F80u, 0x3F803F80u, 0x3F803F80u};
#pragma unroll
    for (int k = 0; k < 14; ++k) { cs0 = mfma32(W[k], one, cs0); cs1 = mfma32(W[14 + k], one, cs1); cs2 = mfma32(one, W[28 + k], cs2); }
  }
#pragma unroll
  for (int f = 0; f < 42; ++f) PIN_A(W[f]);
  const float csv = cs2[0];
  const float vone = (g == 1 && i16 == 12) ? 1.0f : 0.0f;
  const float sc = 0.18898223650461362f * LOG2E;
  const int lim = tok_of(tau, i16) - 8 * q;
  v4u dm;
#pragma unroll
  for (int e = 0; e < 4; ++e) dm[e] = ((2 * e <= lim) ? 0xFFFFu : 0u) | ((2 * e + 1 <= lim) ? 0xFFFF0000u : 0u);
  v4u x[7];
  const rsrc_t rx = __builtin_amdgcn_make_buffer_rsrc((void*)x0, 0, 2048 * 896, 0x00020000);
  auto load_x = [&](int s) {
    if (L == 0) {
      const unsigned p = (unsigned)(s * 32 + tok_of(tau, i16)) * 896 + (g * 7 * 32 + 8 * q) * 2;
#pragma unroll
      for (int f = 0; f < 7; ++f) x[f] = ld16(rx, p + f * 64);
    } else {
      const unsigned xb = rec_off(0, s) + R_X2 + (unsigned)(tau * 14 + g * 7) * KB + lane * 16;
#pragma unroll
      for (int f = 0; f < 7; ++f) x[f] = ld16c(c.rs, xb + f * KB);
    }
  };
  const int c0 = 28 * a + 4 * q, c1 = c0 + 16;
  const unsigned oa = R_OA + (unsigned)tau * 14 * KB + (c0 >> 5) * KB + (((c0 >> 3) & 3) * 16 + i16) * 16 + (c0 & 4) * 2;
  const unsigned ob = (q < 3) ? R_OA + (unsigned)tau * 14 * KB + (c1 >> 5) * KB + (((c1 >> 3) & 3) * 16 + i16) * 16 + (c1 & 4) * 2 : OOB;
  CBAR();
  if (L == 0) {
    load_x(0);
#pragma unroll
    for (int f = 0; f < 7; ++f) TOUCH(x[f]);
  }
  int slot = 0;
  bool have = false;
  for (int s = 0; s < NSEG; ++s) {
    STAMP(s, 0);
    if (L == 1 && !have) {
      CBAR();
      load_x(s);
      POLL(XBAD, load_x(s), 1);
    }
    STAMP(s, 1);
#pragma unroll
    for (int f = 0; f < 7; ++f) LDS16(lX + (tau * 14 + g * 7 + f) * KB + lane * 16) = x[f];
    f4 a0 = Z4, a1 = Z4, a2 = Z4;
    {
      float sum = 0.f, sq = 0.f;
#pragma unroll
      for (int f = 0; f < 7; ++f) {
        a0 = mfma32(W[f], x[f], a0);
        a1 = mfma32(W[14 + f], x[f], a1);
        a2 = mfma32(x[f], W[28 + f], a2);
#pragma unroll
        for (int e = 0; e < 4; ++e) { sum = dot2(x[f][e], 0x3F803F80u, sum); sq = dot2(x[f][e], x[f][e], sq); }
        SB();
      }
      sum = rsum4(sum);
      sq = rsum4(sq);
      if (q == 0) *(f2*)(lst + ((g * 2 + tau) * 16 + i16) * 8) = f2{sum, sq};
    }
    BAR();
    {
      v4u y[7];
#pragma unroll
      for (int f = 0; f < 7; ++f) y[f] = LDS16(lX + (tau * 14 + (1 - g) * 7 + f) * KB + lane * 16);
      const f2 sa = *(const f2*)(lst + (tau * 16 + i16) * 8), sb = *(const f2*)(lst + ((2 + tau) * 16 + i16) * 8);
      const f4 t0 = LDSF4(lst + (tau * 16 + 4 * q) * 8), t1 = LDSF4(lst + (tau * 16 + 4 * q) * 8 + 16);
      const f4 t2 = LDSF4(lst + ((2 + tau) * 16 + 4 * q) * 8), t3 = LDSF4(lst + ((2 + tau) * 16 + 4 * q) * 8 + 16);
      CBAR();
      if (L == 0) load_x(min(s + 1, NSEG - 1));
      SB();
#pragma unroll
      for (int f = 0; f < 7; ++f) {
        a0 = mfma32(W[7 + f], y[f], a0);
        a1 = mfma32(W[21 + f], y[f], a1);
        a2 = mfma32(y[f], W[35 + f], a2);
      }
      const float mean = (sa[0] + sb[0]) * (1.0f / 448.0f);
      const float rstd = rsq(fmaxf((sa[1] + sb[1]) * (1.0f / 448.0f) - mean * mean, 0.f) + 1e-5f);
      const float mr = mean * rstd;
      const f4 sm = f4{t0[0] + t2[0], t0[2] + t2[2], t1[0] + t3[0], t1[2] + t3[2]} * (1.0f / 448.0f);
      const f4 s2 = f4{t0[1] + t2[1], t0[3] + t2[3], t1[1] + t3[1], t1[3] + t3[3]} * (1.0f / 448.0f);
      f4 rs;
#pragma unroll
      for (int r = 0; r < 4; ++r) rs[r] = rsq(fmaxf(s2[r] - sm[r] * sm[r], 0.f) + 1e-5f);
      a0 = a0 * rstd - cs0 * mr;
      a1 = a1 * rstd - cs1 * mr;
      LDS16((g == 0 ? lQ + tau * KB : lK + (slot * 2 + tau) * KB) + lane * 16) = pk8(a0, a1);
      a2 = (a2 - sm * csv) * rs + vone;
      *(v2u*)(lV + (slot * 2 + g) * KB + lane * 16 + tau * 8) = pk4(a2);
    }
    BAR();
    STAMP(s, 2);
    {
      const v4u qf = LDS16(lQ + tau * KB + lane * 16);
      int sl[5];
      v4u kf[10];
#pragma unroll
      for (int b = 0; b < 5; ++b) {
        const int d = (b < 4) ? 4 * g + b : 8;
        int k = slot - d;
        k += (k < 0) ? 9 : 0;
        sl[b] = k;
        if (b < 4 || g == 0) { kf[2 * b] = LDS16(lK + (k * 2) * KB + lane * 16); kf[2 * b + 1] = LDS16(lK + (k * 2 + 1) * KB + lane * 16); }
      }
      SB();
      f4 scv[4][2];
#pragma unroll
      for (int b = 0; b < 4; ++b) {
        scv[b][0] = mfma32(kf[2 * b], qf, Z4);
        scv[b][1] = mfma32(kf[2 * b + 1], qf, Z4);
      }
      v4u vf[10];
      f4 e0 = Z4, e1 = Z4;
      if (g == 0) { e0 = mfma32(kf[8], qf, Z4); e1 = mfma32(kf[9], qf, Z4); }
      SB();
#pragma unroll
      for (int b = 0; b < 5; ++b)
        if (b < 4 || g == 0) { vf[2 * b] = LDS16(lV + (sl[b] * 2) * KB + lane * 16); vf[2 * b + 1] = LDS16(lV + (sl[b] * 2 + 1) * KB + lane * 16); }
      if (L == 1) { CBAR(); load_x(min(s + 1, NSEG - 1)); }
      SB();
#pragma unroll
      for (int b = 0; b < 4; ++b) { PIN_V(scv[b][0]); PIN_V(scv[b][1]); }
      if (g == 0) {
        PIN_V(e0); PIN_V(e1);
        if (s < 8) { e0 = f4{NEG, NEG, NEG, NEG}; e1 = e0; }
#pragma unroll
        for (int r = 0; r < 4; ++r) {
          scv[0][0][r] = (r <= lim) ? scv[0][0][r] : e0[r];
          scv[0][1][r] = (4 + r <= lim) ? scv[0][1][r] : e1[r];
        }
      }
      if (s < 8) {
#pragma unroll
        for (int b = 0; b < 4; ++b)
          if (s - (4 * g + b) < 0) { scv[b][0] = f4{NEG, NEG, NEG, NEG}; scv[b][1] = scv[b][0]; }
      }
      float m = -1.0e30f;
#pragma unroll
      for (int b = 0; b < 4; ++b) m = mxf4(scv[b][1], mxf4(scv[b][0], m));
      m = rmax4(m);
      const float ml = m * sc;
      f4 o0 = Z4, o1 = Z4;
#pragma unroll
      for (int bb = 0; bb < 4; ++bb) {
        const int b = 3 - bb;
#pragma unroll
        for (int r = 0; r < 4; ++r) {
          scv[b][0][r] = ex2(scv[b][0][r] * sc - ml);
          scv[b][1][r] = ex2(scv[b][1][r] * sc - ml);
        }
        v4u p = pk8(scv[b][0], scv[b][1]);
        if (b == 0 && g == 0) {
          const v4u p8 = p & ~dm;
          p = p & dm;
          o0 = mfma32(vf[8], p8, o0);
          o1 = mfma32(vf[9], p8, o1);
        }
        o0 = mfma32(vf[2 * b], p, o0);
        o1 = mfma32(vf[2 * b + 1], p, o1);
      }
      if (g == 1) {
        LDSF4(lmg + (tau * 2 + 0) * KB + lane * 16) = o0;
        LDSF4(lmg + (tau * 2 + 1) * KB + lane * 16) = o1;
        *(float*)(lms + (tau * 64 + lane) * 4) = m;
      }
      if (L == 0) {
#pragma unroll
        for (int f = 0; f < 7; ++f) TOUCH(x[f]);
      } else {
        have = !XBAD;
      }
      BAR();
      if (g == 0) {
        const float mo = *(const float*)(lms + (tau * 64 + lane) * 4);
        const float mt = mx3(m, mo, NEG);
        const float fa = ex2((m - mt) * sc), fb = ex2((mo - mt) * sc);
        const f4 r0 = o0 * fa + LDSF4(lmg + (tau * 2 + 0) * KB + lane * 16) * fb;
        const f4 r1 = o1 * fa + LDSF4(lmg + (tau * 2 + 1) * KB + lane * 16) * fb;
        float dn[4];
        rbcast4(r1[0], dn);
        const float inv = rcp(dn[3]);
        st8c(c.rs, rec_off(L, s) + oa, ns2(pk4(r0 * inv)));
        st8c(c.rs, rec_off(L, s) + ob, ns2(pk4(r1 * inv)));
        if (s + AD < NSEG) {
          st8c(c.rs, rec_off(L, s + AD) + oa, S2);
          st8c(c.rs, rec_off(L, s + AD) + ob, S2);
        }
      }
      STAMP(s, 3);
    }
    slot = (slot == 8) ? 0 : slot + 1;
  }
}

template <int l>
DEVI void role_AO(Ctx& c, int tau, int par, unsigned char* lds, const unsigned char* x0) {
  const int lane = c.lane, w = c.wave;
  const int i16 = lane & 15, q = lane >> 4;
  unsigned char* lO = lds;
  unsigned char* lx = lds + 14 * KB;
  unsigned char* lst = lds + 18 * KB;
  v4u W[98];
  {
    const unsigned b0 = WO_OFF + (unsigned)(3 * w) * 28 * KB + lane * 16;
#pragma unroll
    for (int f = 0; f < 84; ++f) W[f] = ld16(c.rs, b0 + f * KB);
    const unsigned b1 = WO_OFF + (unsigned)(12 + (w >> 1)) * 28 * KB + (w & 1) * 7 * KB + lane * 16;
#pragma unroll
    for (int k = 0; k < 7; ++k) { W[84 + k] = ld16(c.rs, b1 + k * KB); W[91 + k] = ld16(c.rs, b1 + (14 + k) * KB); }
#pragma unroll
    for (int f = 0; f < 56; ++f) PIN_A(W[f]);
  }
  const int ux = 12 + (w >> 1), kx = 7 * (w & 1);
  const bool own4 = !(w & 1);
  const unsigned fo = (lane == 0) ? F_XR + (unsigned)((l * 2 + tau) * 2 + par) * 64 + w * 4 : OOB;
  const rsrc_t rx = __builtin_amdgcn_make_buffer_rsrc((void*)x0, 0, 2048 * 896, 0x00020000);
  for (int s = par; s < NSEG; s += 2) {
    const unsigned rb = rec_off(l, s);
    STAMP(s, 0);
    {
      const unsigned ob = rb + R_OA + (unsigned)tau * 14 * KB;
      v4u v[4];
      auto ldo = [&]() {
        v[0] = ld16c(c.rs, ob + c.tid * 16); v[1] = ld16c(c.rs, ob + 4096 + c.tid * 16); v[2] = ld16c(c.rs, ob + 8192 + c.tid * 16);
        v[3] = ld16c(c.rs, ob + 12288 + (c.tid & 127) * 16);
      };
      CBAR();
      ldo();
      DRAIN();
      st4c(c.rs, fo, (unsigned)max(s - 1, 0));
      POLL(anysent(mx4(mx4(mx4(mx4(0u, v[0]), v[1]), v[2]), v[3])), ldo(), 1);
      LDS16(lO + c.tid * 16) = v[0];
      LDS16(lO + 4096 + c.tid * 16) = v[1];
      LDS16(lO + 8192 + c.tid * 16) = v[2];
      LDS16(lO + 12288 + (c.tid & 127) * 16) = v[3];
    }
    STAMP(s, 1);
    v4u xr[4];
    auto ldr = [&]() {
#pragma unroll
      for (int p = 0; p < 4; ++p) {
        const int u = (p < 3) ? 3 * w + p : ux;
        if (l == 0) xr[p] = ld16(rx, (unsigned)(s * 32 + tok_of(tau, i16)) * 896 + (u * 32 + 8 * q) * 2);
        else xr[p] = ld16c(c.rs, rec_off(0, s) + R_X2 + (unsigned)(tau * 14 + u) * KB + lane * 16);
      }
    };
    BAR();
    f4 acc[8] = {Z4, Z4, Z4, Z4, Z4, Z4, Z4, Z4};
    {
      const unsigned char* xp = lO + lane * 16;
      v4u xs[7];
#pragma unroll
      for (int k = 0; k < 7; ++k) xs[k] = LDS16(xp + (kx + k) * KB);
      v4u y0 = LDS16(xp), y1 = LDS16(xp + KB);
      SB();
#pragma unroll
      for (int k = 0; k < 7; ++k) {
        acc[6] = mfma32(W[84 + k], xs[k], acc[6]);
        acc[7] = mfma32(W[91 + k], xs[k], acc[7]);
      }
      SB();
      ldr();
      SB();
#pragma unroll
      for (int ks = 0; ks < 14; ++ks) {
        const v4u x = y0;
        y0 = y1;
        if (ks + 2 < 14) y1 = LDS16(xp + (ks + 2) * KB);
        if (ks == 2 && !own4) { LDSF4(lx + ((w >> 1) * 2 + 0) * KB + lane * 16) = acc[6]; LDSF4(lx + ((w >> 1) * 2 + 1) * KB + lane * 16) = acc[7]; }
#pragma unroll
        for (int pp = 0; pp < 3; ++pp) {
          acc[pp * 2 + 0] = mfma32(W[pp * 28 + ks], x, acc[pp * 2 + 0]);
          acc[pp * 2 + 1] = mfma32(W[pp * 28 + 14 + ks], x, acc[pp * 2 + 1]);
        }
        SB();
      }
    }
    if (l == 1) POLL(anysent(mx4(mx4(mx4(mx4(0u, xr[0]), xr[1]), xr[2]), xr[3])), ldr(), 1);
    BAR();
    STAMP(s, 2);
    v4u xb[4];
    if (own4) {
      acc[6] += LDSF4(lx + ((w >> 1) * 2 + 0) * KB + lane * 16);
      acc[7] += LDSF4(lx + ((w >> 1) * 2 + 1) * KB + lane * 16);
    }
    float sum = 0.f, sq = 0.f;
#pragma unroll
    for (int p = 0; p < 4; ++p) {
      xb[p] = resid8(xr[p], acc[2 * p], acc[2 * p + 1]);
      if (p == 3 && !own4) xb[p] = v4u{0u, 0u, 0u, 0u};
#pragma unroll
      for (int e = 0; e < 4; ++e) { sum = dot2(xb[p][e], 0x3F803F80u, sum); sq = dot2(xb[p][e], xb[p][e], sq); }
    }
    sum = rsum4(sum);
    sq = rsum4(sq);
    if (q == 0) *(f2*)(lst + (w * 16 + i16) * 8) = f2{sum, sq};
    BAR();
    const f2 st = (*(const f2*)(lst + i16 * 8) + *(const f2*)(lst + (16 + i16) * 8)) + (*(const f2*)(lst + (32 + i16) * 8) + *(const f2*)(lst + (48 + i16) * 8));
    const float mean = st[0] * (1.0f / 448.0f);
    const float rstd = rsq(fmaxf(st[1] * (1.0f / 448.0f) - mean * mean, 0.f) + 1e-5f);
    const float mr = mean * rstd;
#pragma unroll
    for (int p = 0; p < 4; ++p)
      if (p < 3 || own4) {
        const int u = (p < 3) ? 3 * w + p : ux;
        const unsigned fo = (unsigned)(tau * 14 + u) * KB + lane * 16;
        v4u o;
#pragma unroll
        for (int e = 0; e < 4; ++e) o[e] = pk2(bflo(xb[p][e]) * rstd - mr, bfhi(xb[p][e]) * rstd - mr);
        st16c(c.rs, rb + R_XN + fo, ns4(o));
      }
#pragma unroll
    for (int p = 0; p < 4; ++p)
      if (p < 3 || own4) {
        const int u = (p < 3) ? 3 * w + p : ux;
        const unsigned fo = (unsigned)(tau * 14 + u) * KB + lane * 16;
        st16c(c.rs, rb + R_XR + fo, xb[p]);
        st16c(c.rs, rec_in(l, s + AD) + R_XN + fo, S4);
      }
    STAMP(s, 3);
  }
  DRAIN();
  st4c(c.rs, fo, (unsigned)NSEG);
}

DEVI unsigned ring_offset(const unsigned char* ws) {
  const unsigned lo = (unsigned)((size_t)ws & (2u * MB - 1u));
  return ((lo + FLAG_BYTES + 2u * MB - 1u) & ~(2u * MB - 1u)) - lo;
}

__global__ void __launch_bounds__(256, 1) mega_kernel(unsigned char* ws, const unsigned char* x0, const unsigned char* mem0, __hip_bfloat16* out) {
  extern __shared__ __attribute__((aligned(16))) unsigned char lds[];
  Ctx c;
  c.ws = ws;
  c.rs = __builtin_amdgcn_make_buffer_rsrc(ws, 0, WS_END, 0x00020000);
  c.ring = ring_offset(ws);
  c.tid = threadIdx.x;
  c.lane = threadIdx.x & 63;
  c.wave = __builtin_amdgcn_readfirstlane(threadIdx.x >> 6);
  c.dead = false;
  const int b = blockIdx.x, x = b & 7, r = b >> 3;
  if (x == 0) {
    role_H(c, r >> 1, r & 1, lds);
  } else if (x < 3) {
    if (r < 28) role_CV(c, r, x - 1, lds, mem0);
  } else if (x < 5) {
    if (r & 1) role_GX<1>(c, r >> 1, x - 3, lds); else role_GX<0>(c, r >> 1, x - 3, lds);
  } else if (x == 5) {
    if (r < 14) role_O<0>(c, r / 7, r % 7, lds, out); else if (r < 28) role_O<1>(c, (r - 14) / 7, (r - 14) % 7, lds, out);
  } else if (x == 6) {
    if (r >> 4) role_AH<1>(c, r & 15, lds, x0); else role_AH<0>(c, r & 15, lds, x0);
  } else {
    if (r < 4) role_AO<0>(c, r & 1, r >> 1, lds, x0); else if (r < 8) role_AO<1>(c, r & 1, (r >> 1) & 1, lds, x0);
  }
}

__global__ void __launch_bounds__(256) prologue_kernel(unsigned char* ws, const unsigned short* wqkv, const unsigned short* wo,
                                                       const unsigned short* mem0, const unsigned short* Wq, const unsigned short* Wk,
                                                       const unsigned short* Wv, const unsigned short* Wo) {
  __shared__ __attribute__((aligned(16))) unsigned short lt[64 * 264];
  const int b = blockIdx.x, tid = threadIdx.x, lane = tid & 63, w = tid >> 6;
  const int i16 = lane & 15, q = lane >> 4;
  if (b < 224) {
    const bool isG = b < 112;
    const int bb = isG ? b : b - 112, h = bb / 7, cg = bb % 7;
    v4u B[4][8];
    if (isG) {
#pragma unroll
      for (int nt = 0; nt < 4; ++nt) {
        const int cc = 64 * cg + 32 * (nt >> 1) + 8 * (i16 >> 2) + 4 * (nt & 1) + (i16 & 3);
#pragma unroll
        for (int es = 0; es < 8; ++es) B[nt][es] = *(const v4u*)(Wq + ((size_t)cc * 16 + h) * 256 + es * 32 + 8 * q);
      }
    } else {
      for (int idx = tid; idx < 256 * 8; idx += 256) {
        const int e = idx >> 3, c8 = idx & 7;
        const v4u v = *(const v4u*)(Wo + ((size_t)h * 256 + e) * 448 + 64 * cg + 8 * c8);
#pragma unroll
        for (int k = 0; k < 4; ++k) {
          lt[(8 * c8 + 2 * k) * 264 + e] = (unsigned short)(v[k] & 0xFFFFu);
          lt[(8 * c8 + 2 * k + 1) * 264 + e] = (unsigned short)(v[k] >> 16);
        }
      }
      __syncthreads();
#pragma unroll
      for (int nt = 0; nt < 4; ++nt)
#pragma unroll
        for (int es = 0; es < 8; ++es) B[nt][es] = *(const v4u*)(lt + (16 * nt + i16) * 264 + es * 32 + 8 * q);
    }
    const unsigned short* Wa = isG ? Wk : Wv;
    const float sc = isG ? 0.0625f : 1.0f;
    for (int ks = w; ks < 14; ks += 4) {
      v4u A[2][8];
#pragma unroll
      for (int mt = 0; mt < 2; ++mt) {
        const int d = 32 * ks + 8 * (i16 >> 2) + 4 * mt + (i16 & 3);
#pragma unroll
        for (int es = 0; es < 8; ++es) A[mt][es] = *(const v4u*)(Wa + ((size_t)d * 16 + h) * 256 + es * 32 + 8 * q);
      }
#pragma unroll
      for (int nt = 0; nt < 4; ++nt) {
        f4 a0 = Z4, a1 = Z4;
#pragma unroll
        for (int es = 0; es < 8; ++es) {
          a0 = mfma32(A[0][es], B[nt][es], a0);
          a1 = mfma32(A[1][es], B[nt][es], a1);
        }
        const v4u o = pk8(a0 * sc, a1 * sc);
        size_t off;
        if (isG) off = WG_OFF + (size_t)(((h * 14 + 2 * cg + (nt >> 1)) * 2 + (nt & 1)) * 14 + ks) * KB;
        else off = WU_OFF + (size_t)(((4 * cg + nt) * 16 + h) * 14 + ks) * KB;
        *(v4u*)(ws + off + lane * 16) = o;
      }
    }
  } else {
    const int nb = gridDim.x - 224, bb = b - 224;
    const int gtid = bb * 256 + tid, gstride = nb * 256;
    for (int idx = gtid; idx < 16 * 6 * 14 * 64; idx += gstride) {
      const int ln = idx & 63, f = idx >> 6, ks = f % 14, tl = (f / 14) % 6, a = f / 84;
      const int n = ln & 15, qq = ln >> 4;
      int dh, col;
      if (tl < 4) { dh = 8 * (n >> 2) + 4 * (tl & 1) + (n & 3); col = (tl >> 1) * 448 + 28 * a + dh; }
      else { dh = 16 * (tl - 4) + n; col = 896 + 28 * a + dh; }
      v4u o = {0u, 0u, 0u, 0u};
      if (dh < 28) {
#pragma unroll
        for (int jj = 0; jj < 4; ++jj) {
          const int d = 32 * ks + 8 * qq + 2 * jj;
          o[jj] = (unsigned)wqkv[(size_t)d * 1344 + col] | ((unsigned)wqkv[(size_t)(d + 1) * 1344 + col] << 16);
        }
      }
      *(v4u*)(ws + WQ_OFF + (size_t)idx * 16) = o;
    }
    for (int idx = gtid; idx < 14 * 2 * 14 * 64; idx += gstride) {
      const int ln = idx & 63, f = idx >> 6, ks = f % 14, tt = (f / 14) & 1, u = f / 28;
      const int n = ln & 15, qq = ln >> 4;
      const int col = 32 * u + 8 * (n >> 2) + 4 * tt + (n & 3);
      v4u o;
#pragma unroll
      for (int jj = 0; jj < 4; ++jj) {
        const int d = 32 * ks + 8 * qq + 2 * jj;
        o[jj] = (unsigned)wo[(size_t)d * 448 + col] | ((unsigned)wo[(size_t)(d + 1) * 448 + col] << 16);
      }
      *(v4u*)(ws + WO_OFF + (size_t)idx * 16) = o;
    }
    for (unsigned idx = gtid; idx < FLAG_BYTES / 16; idx += gstride) *(v4u*)(ws + (size_t)idx * 16) = v4u{0u, 0u, 0u, 0u};
    const unsigned ring = ring_offset(ws);
    for (unsigned idx = gtid; idx < RING_BYTES / 16; idx += gstride) {
      const unsigned po = idx * 16, lo = ((po >> 14) << 13) | (po & 8191u);
      v4u o = S4;
      if (lo < L_MEMG && (lo & (64 * KB - 1)) < 14 * KB) {
        const unsigned r = lo & (64 * KB - 1), ln = (r >> 4) & 63, ks = r >> 10;
        o = ns4(*(const v4u*)(mem0 + (size_t)(ln & 7) * 448 + 32 * ks + 8 * (ln >> 4)));
      } else if (lo >= L_MEMG && lo < L_MEMG + 16 * KB && (lo & 8191u) < 7 * KB) {
        const unsigned r = lo & 8191u, ks = r >> 9, qq = (r >> 7) & 3, row = (r >> 4) & 7;
        o = ns4(*(const v4u*)(mem0 + (size_t)row * 448 + 32 * ks + 8 * qq));
      }
      *(v4u*)(ws + ring + po) = o;
    }
    for (unsigned idx = gtid; idx < 2 * AD * REC / 16; idx += gstride) {
      const unsigned l = idx / (AD * REC / 16), r = idx % (AD * REC / 16);
      *(v4u*)(ws + REC_OFF + (size_t)l * NSEG * REC + (size_t)r * 16) = S4;
    }
  }
}

extern "C" void kernel_launch(void* const* d_in, const int* in_sizes, int n_in, void* d_out, int out_size, void* d_ws, size_t ws_size,
                              hipStream_t stream) {
  constexpr int LDS_BYTES = 100 * 1024;
  (void)hipFuncSetAttribute((const void*)mega_kernel, hipFuncAttributeMaxDynamicSharedMemorySize, LDS_BYTES);
  unsigned char* ws = (unsigned char*)d_ws;
  prologue_kernel<<<256 + 32, 256, 0, stream>>>(ws, (const unsigned short*)d_in[1], (const unsigned short*)d_in[2], (const unsigned short*)d_in[4],
                                          (const unsigned short*)d_in[5], (const unsigned short*)d_in[6], (const unsigned short*)d_in[7],
                                          (const unsigned short*)d_in[8]);
  mega_kernel<<<256, 256, LDS_BYTES, stream>>>(ws, (const unsigned char*)d_in[0], (const unsigned char*)d_in[4], (__hip_bfloat16*)d_out);
}
```

```cpp
#include <hip/hip_runtime.h>
#include <hip/hip_bf16.h>

typedef unsigned v4u __attribute__((ext_vector_type(4)));
typedef unsigned v2u __attribute__((ext_vector_type(2)));
typedef float f4 __attribute__((ext_vector_type(4)));
typedef float f2 __attribute__((ext_vector_type(2)));
typedef short s8v __attribute__((ext_vector_type(8)));
typedef __bf16 b2v __attribute__((ext_vector_type(2)));
typedef __amdgpu_buffer_rsrc_t rsrc_t;
#define DEVI __device__ __forceinline__
#define RLX_AGENT __ATOMIC_RELAXED, __HIP_MEMORY_SCOPE_AGENT

#define LAG 6
#define ZEARLY 0
#define GXS 1
#define PD 7
#define PP 0
#define USTREAM 1
constexpr int NSEG = 64, NT = NSEG + LAG, AD = 4;
constexpr unsigned SENT = 0xFFFFFFFFu;
constexpr unsigned SPINCAP = 1u << 20;
constexpr float LOG2E = 1.4426950408889634f;
constexpr float NEG = -3.0e38f;

constexpr unsigned KB = 1024u, MB = 1024u * 1024u;
constexpr unsigned FLAG_BYTES = 8 * KB;
constexpr unsigned F_U = 1024, F_XR = 7168;
constexpr unsigned OOB = 0x80000000u;
constexpr unsigned F_ABORT = 0, F_SIDE = 128;
constexpr unsigned RING_BYTES = 1 * MB;
constexpr unsigned L_MEMH = 0, L_MEMC = 64 * KB, L_MEMG = 128 * KB, L_S = 192 * KB, L_E = 256 * KB, L_XS = 384 * KB, L_PROBE = 504 * KB;
constexpr unsigned WG_OFF = 4 * MB;
constexpr unsigned WG_BYTES = 16 * 14 * 2 * 14 * KB;
constexpr unsigned WU_OFF = WG_OFF + WG_BYTES;
constexpr unsigned WQ_OFF = WU_OFF + WG_BYTES;
constexpr unsigned WO_OFF = WQ_OFF + 16 * 6 * 14 * KB;
constexpr unsigned SEGX = 28 * KB;
constexpr unsigned R_XN = 0, R_XR = SEGX, R_OA = 2 * SEGX, R_X2 = 3 * SEGX, R_ETT = 4 * SEGX, R_ETS = R_ETT + 32 * KB, R_STM = R_ETS + 4 * KB,
                   R_ZB = R_STM + 32 * KB, REC = R_ZB + 112 * KB;
constexpr unsigned X0_OFF = WO_OFF + 392 * KB;
constexpr unsigned REC_OFF = X0_OFF + NSEG * SEGX;
constexpr unsigned SEGG = 448 * KB;
constexpr unsigned U_OFF = REC_OFF + 2 * NSEG * REC;
constexpr unsigned WS_END = U_OFF + 2 * NSEG * SEGG;
static_assert(WS_END < 200u * MB, "workspace");

DEVI f4 mfma32(v4u a, v4u b, f4 c) {
  return __builtin_amdgcn_mfma_f32_16x16x32_bf16(__builtin_bit_cast(s8v, a), __builtin_bit_cast(s8v, b), c, 0, 0, 0);
}
DEVI unsigned pk2(float a, float b) {
  f2 v = {a, b};
  return __builtin_bit_cast(unsigned, __builtin_convertvector(v, b2v));
}
DEVI float bflo(unsigned u) { return __uint_as_float(u << 16); }
DEVI float bfhi(unsigned u) { return __uint_as_float(u & 0xFFFF0000u); }
DEVI float rbf(float a) { return bflo(pk2(a, 0.f)); }
DEVI v4u pk8(f4 a, f4 b) { return v4u{pk2(a[0], a[1]), pk2(a[2], a[3]), pk2(b[0], b[1]), pk2(b[2], b[3])}; }
DEVI v2u pk4(f4 a) { return v2u{pk2(a[0], a[1]), pk2(a[2], a[3])}; }
DEVI v4u resid8(v4u x, f4 a, f4 b) {
  const v4u y = pk8(a, b);
  return v4u{pk2(bflo(x.x) + bflo(y.x), bfhi(x.x) + bfhi(y.x)), pk2(bflo(x.y) + bflo(y.y), bfhi(x.y) + bfhi(y.y)),
             pk2(bflo(x.z) + bflo(y.z), bfhi(x.z) + bfhi(y.z)), pk2(bflo(x.w) + bflo(y.w), bfhi(x.w) + bfhi(y.w))};
}
DEVI unsigned ns(unsigned x) { return min(x, 0xFFFFFFFEu); }
DEVI v4u ns4(v4u x) { return v4u{ns(x.x), ns(x.y), ns(x.z), ns(x.w)}; }
DEVI v2u ns2(v2u x) { return v2u{ns(x.x), ns(x.y)}; }
DEVI v4u f2u(f4 x) { return v4u{__float_as_uint(x[0]), __float_as_uint(x[1]), __float_as_uint(x[2]), __float_as_uint(x[3])}; }
DEVI f4 u2f(v4u x) { return f4{__uint_as_float(x.x), __uint_as_float(x.y), __uint_as_float(x.z), __uint_as_float(x.w)}; }
DEVI unsigned mx4(unsigned m, v4u v) { return max(max(max(max(m, v.x), v.y), v.z), v.w); }
DEVI unsigned mx2(unsigned m, v2u v) { return max(max(m, v.x), v.y); }
DEVI bool anysent(unsigned m) { return __builtin_amdgcn_ballot_w64(m == SENT) != 0ull; }
DEVI float ex2(float x) { return __builtin_amdgcn_exp2f(x); }
DEVI float rcp(float x) { return __builtin_amdgcn_rcpf(x); }
DEVI float rsq(float x) { return __builtin_amdgcn_rsqf(x); }
DEVI float mx3(float a, float b, float c) { return __builtin_fmaxf(__builtin_fmaxf(a, b), c); }
DEVI float mxf4(f4 a, float m) { return mx3(a[2], a[3], mx3(a[0], a[1], m)); }
DEVI float dot2(unsigned a, unsigned b, float c) {
  return __builtin_amdgcn_fdot2_f32_bf16(__builtin_bit_cast(b2v, a), __builtin_bit_cast(b2v, b), c, false);
}
DEVI void swap16(unsigned& a, unsigned& b) { auto r = __builtin_amdgcn_permlane16_swap(a, b, false, false); a = r[0]; b = r[1]; }
DEVI void swap32(unsigned& a, unsigned& b) { auto r = __builtin_amdgcn_permlane32_swap(a, b, false, false); a = r[0]; b = r[1]; }
DEVI float rmax4(float x) {
  unsigned a = __float_as_uint(x), b = a;
  swap16(a, b);
  a = b = __float_as_uint(mx3(__uint_as_float(a), __uint_as_float(b), NEG));
  swap32(a, b);
  return mx3(__uint_as_float(a), __uint_as_float(b), NEG);
}
DEVI float rsum4(float x) {
  unsigned a = __float_as_uint(x), b = a;
  swap16(a, b);
  a = b = __float_as_uint(__uint_as_float(a) + __uint_as_float(b));
  swap32(a, b);
  return __uint_as_float(a) + __uint_as_float(b);
}
DEVI float ror8(float x) { return __uint_as_float(__builtin_amdgcn_update_dpp(0u, __float_as_uint(x), 0x128, 0xF, 0xF, false)); }
DEVI void rbcast4(float x, float* o) {
  unsigned a = __float_as_uint(x), b = a;
  swap16(a, b);
  unsigned a2 = a, b2 = b;
  swap32(a, a2);
  swap32(b, b2);
  o[0] = __uint_as_float(a); o[1] = __uint_as_float(b); o[2] = __uint_as_float(a2); o[3] = __uint_as_float(b2);
}

DEVI v4u ld16(rsrc_t r, unsigned off) { return __builtin_amdgcn_raw_buffer_load_b128(r, off, 0, 0); }
DEVI v4u ld16c(rsrc_t r, unsigned off) { return __builtin_amdgcn_raw_buffer_load_b128(r, off, 0, 16); }
DEVI v2u ld8c(rsrc_t r, unsigned off) { return __builtin_amdgcn_raw_buffer_load_b64(r, off, 0, 16); }
DEVI unsigned ld4c(rsrc_t r, unsigned off) { return __builtin_amdgcn_raw_buffer_load_b32(r, off, 0, 16); }
DEVI void st16c(rsrc_t r, unsigned off, v4u v) { __builtin_amdgcn_raw_buffer_store_b128(v, r, off, 0, 16); }
DEVI void st8c(rsrc_t r, unsigned off, v2u v) { __builtin_amdgcn_raw_buffer_store_b64(v, r, off, 0, 16); }
DEVI void st4c(rsrc_t r, unsigned off, unsigned v) { __builtin_amdgcn_raw_buffer_store_b32(v, r, off, 0, 16); }
#define PIN_A(x) asm volatile("" : "+a"(x))
#define TOUCH(x) asm volatile("" :: "v"(x))
#define PIN_V(x) asm volatile("" : "+v"(x))
#define DRAIN() asm volatile("s_waitcnt vmcnt(0)" ::: "memory")
#define CBAR() asm volatile("" ::: "memory")
#define SB() __builtin_amdgcn_sched_barrier(0)
#define BAR() do { asm volatile("s_waitcnt lgkmcnt(0)" ::: "memory"); __builtin_amdgcn_s_barrier(); asm volatile("" ::: "memory"); } while (0)
#define LDS16(p) (*(v4u*)(p))
#define LDSF4(p) (*(f4*)(p))
#define S4 (v4u{SENT, SENT, SENT, SENT})
#define S2 (v2u{SENT, SENT})
#define Z4 (f4{0.f, 0.f, 0.f, 0.f})

#define STAMP(it, e) do { } while (0)
#define STAMPC(it, e) do { } while (0)
struct Ctx {
  unsigned char* ws;
  rsrc_t rs;
  unsigned ring;
  int tid, lane, wave;
  bool dead;
};
DEVI bool give_up(Ctx& c, unsigned& spins) {
  if (c.dead) return true;
  ++spins;
  bool d = false;
  if (spins > SPINCAP) { __hip_atomic_store((unsigned*)(c.ws + F_ABORT), 1u, RLX_AGENT); d = true; }
  else if ((spins & 255u) == 0u) d = __builtin_amdgcn_readfirstlane(__hip_atomic_load((unsigned*)(c.ws + F_ABORT), RLX_AGENT)) != 0u;
  c.dead = d;
  return d;
}
#define POLL2(FIRST, BADEXPR, RELOAD, SLEEP) do { if (FIRST) { unsigned spins_ = 0; do { if (give_up(c, spins_)) break; if (SLEEP) __builtin_amdgcn_s_sleep(SLEEP); CBAR(); RELOAD; } while (BADEXPR); } } while (0)
#define POLL(BADEXPR, RELOAD, SLEEP) do { unsigned spins_ = 0; while (BADEXPR) { if (give_up(c, spins_)) break; if (SLEEP) __builtin_amdgcn_s_sleep(SLEEP); CBAR(); RELOAD; } } while (0)

template <class LD>
DEVI void poll2(Ctx& c, v4u* r, LD ld) {
  v4u a[4] = {r[0], r[1], r[2], r[3]}, b[4];
  unsigned spins = 0;
  __builtin_amdgcn_s_sleep(PD);
  while (true) {
    CBAR();
    ld(b);
    SB();
    if (!anysent(mx4(mx4(mx4(mx4(0u, a[0]), a[1]), a[2]), a[3])) || give_up(c, spins)) {
#pragma unroll
      for (int k = 0; k < 4; ++k) r[k] = a[k];
      break;
    }
    CBAR();
    ld(a);
    SB();
    if (!anysent(mx4(mx4(mx4(mx4(0u, b[0]), b[1]), b[2]), b[3]))) {
#pragma unroll
      for (int k = 0; k < 4; ++k) r[k] = b[k];
      break;
    }
  }
}

DEVI unsigned rmap(const Ctx& c, unsigned lo, unsigned side) { return c.ring + ((lo >> 13) << 14) + (side << 13) + (lo & 8191u); }
DEVI unsigned rec_off(int l, int s) { return REC_OFF + (unsigned)(l * NSEG + s) * REC; }
DEVI unsigned rec_in(int l, int s) { return (s >= 0 && s < NSEG) ? rec_off(l, s) : OOB; }
DEVI unsigned u_in(int l, int s) { return (s >= 0 && s < NSEG) ? U_OFF + (unsigned)(l * NSEG + s) * SEGG : OOB; }
DEVI int tok_of(int tau, int m) { return 8 * (m >> 2) + 4 * tau + (m & 3); }

DEVI void probe_side(Ctx& c, int g) {
  if (c.wave != 0) return;
  unsigned best[2] = {0xFFFFFFFFu, 0xFFFFFFFFu};
#pragma unroll 1
  for (int k = 0; k < 4; ++k) {
#pragma unroll
    for (int sd = 0; sd < 2; ++sd) {
      DRAIN();
      const unsigned t0 = (unsigned)__builtin_amdgcn_s_memtime();
      if (c.lane < 8) st16c(c.rs, rmap(c, L_PROBE + g * 256 + c.lane * 16, sd), S4);
      DRAIN();
      const unsigned t1 = (unsigned)__builtin_amdgcn_s_memtime();
      best[sd] = min(best[sd], t1 - t0);
    }
  }
  if (c.lane == 0) st4c(c.rs, F_SIDE + g * 128, best[1] < best[0] ? 2u : 1u);
}
DEVI unsigned get_sides(Ctx& c) {
  const unsigned o = F_SIDE + min(c.lane, 4) * 128;
  unsigned v = ld4c(c.rs, o);
  POLL(__builtin_amdgcn_ballot_w64(v == 0u) != 0ull, v = ld4c(c.rs, o), 2);
  return (unsigned)__builtin_amdgcn_ballot_w64(v == 2u) & 31u;
}

DEVI void role_H(Ctx& c, int h, int a, unsigned char* lds) {
  const int lane = c.lane, w = c.wave;
  const int i16 = lane & 15, q = lane >> 4;
  unsigned char* lmem = lds;
  unsigned char* lred = lds + 8 * KB;
  unsigned char* ltb = lds + 24 * KB;
  const int odd = w & 1;
  const int ks0 = (w >> 1) * 7 + odd * 4, ksx = (w >> 1) * 7 + 3;
  if (h == 0 && a == 0) probe_side(c, 0);
  v4u W[42], Wx[8];
#pragma unroll
  for (int lp = 0; lp < 7; ++lp) {
    const int gp = odd ? 6 - lp : lp;
    const unsigned b0 = WG_OFF + (unsigned)(h * 14 + 7 * a + gp) * 28 * KB + lane * 16;
#pragma unroll
    for (int t = 0; t < 2; ++t)
#pragma unroll
      for (int k = 0; k < 3; ++k) W[(lp * 2 + t) * 3 + k] = ld16(c.rs, b0 + (t * 14 + ks0 + k) * KB);
    if (lp < 4) {
#pragma unroll
      for (int t = 0; t < 2; ++t) {
        Wx[lp * 2 + t] = ld16(c.rs, b0 + (t * 14 + ksx) * KB);
        if (lp == 3 && t != odd) Wx[lp * 2 + t] = v4u{0u, 0u, 0u, 0u};
      }
    }
  }
#pragma unroll
  for (int f = 0; f < 42; ++f) PIN_A(W[f]);
#pragma unroll
  for (int f = 0; f < 8; ++f) PIN_A(Wx[f]);
  const unsigned sides = get_sides(c);
  unsigned mo[4];
#pragma unroll
  for (int k = 0; k < 4; ++k) mo[k] = rmap(c, L_MEMH + (k < 3 ? ks0 + k : ksx) * KB + lane * 16, sides & 1);
  const unsigned so0 = rmap(c, L_S + ((a * 2 + (q & 1)) * 16 + h) * 256 + i16 * 16, 0);
  const unsigned sd0 = ((sides >> 1) & 1) << 13, sd1 = ((sides >> 2) & 1) << 13;
  const bool sval = (q >> 1) == (i16 >> 3);
  const bool stager = (w >> 1) == a;
  const int gp1 = (w < 3) ? w + 4 : 3;
  v4u X[8];
  auto load_x = [&](int t) {
#pragma unroll
    for (int l = 0; l < 2; ++l) {
      const unsigned xb = rec_in(l, t - l * LAG) + R_XN + (unsigned)(7 * a) * KB + lane * 16;
#pragma unroll
      for (int tau = 0; tau < 2; ++tau) {
        X[(l * 2 + tau) * 2 + 0] = ld16c(c.rs, xb + (tau * 14 + w) * KB);
        X[(l * 2 + tau) * 2 + 1] = ld16c(c.rs, xb + (tau * 14 + gp1) * KB);
      }
    }
  };
  CBAR();
  load_x(0);
  for (int t = 0; t < NT; ++t) {
    const unsigned slot = (unsigned)(t & 3) * 32 * KB;
    STAMP(t, 0);
    v4u m[4];
    CBAR();
#pragma unroll
    for (int k = 0; k < 4; ++k) m[k] = ld16c(c.rs, mo[k] + slot);
    POLL(anysent(mx4(mx4(mx4(mx4(0u, m[0]), m[1]), m[2]), m[3])),
         { _Pragma("unroll") for (int k = 0; k < 4; ++k) m[k] = ld16c(c.rs, mo[k] + slot); }, 0);
    STAMP(t, 1);
    if (stager) {
#pragma unroll
      for (int k = 0; k < 4; ++k) LDS16(lmem + ((k < 3 ? ks0 + k : ksx) - 7 * a) * KB + lane * 16) = m[k];
    }
    SB();
    v4u tb[7], mu[7];
    f4 acc[7][2];
    f4 sacc = Z4;
#pragma unroll
    for (int lp = 0; lp < 7; ++lp) {
#define HPK(e) do { if (lp >= 2) { SB(); tb[lp - 2][e] = pk2(acc[lp - 2][(e) >> 1][((e) & 1) * 2], acc[lp - 2][(e) >> 1][((e) & 1) * 2 + 1]); SB(); } } while (0)
      acc[lp][0] = mfma32(W[(lp * 2 + 0) * 3 + 0], m[0], Z4);
      HPK(0);
      acc[lp][1] = mfma32(W[(lp * 2 + 1) * 3 + 0], m[0], Z4);
      HPK(1);
      acc[lp][0] = mfma32(W[(lp * 2 + 0) * 3 + 1], m[1], acc[lp][0]);
      HPK(2);
      acc[lp][1] = mfma32(W[(lp * 2 + 1) * 3 + 1], m[1], acc[lp][1]);
      HPK(3);
      acc[lp][0] = mfma32(W[(lp * 2 + 0) * 3 + 2], m[2], acc[lp][0]);
      if (lp == 6) { SB(); tb[5][0] = pk2(acc[5][0][0], acc[5][0][1]); SB(); }
      acc[lp][1] = mfma32(W[(lp * 2 + 1) * 3 + 2], m[2], acc[lp][1]);
      if (lp == 6) { SB(); tb[5][1] = pk2(acc[5][0][2], acc[5][0][3]); SB(); }
      if (lp < 4) {
        acc[lp][0] = mfma32(Wx[lp * 2 + 0], m[3], acc[lp][0]);
        acc[lp][1] = mfma32(Wx[lp * 2 + 1], m[3], acc[lp][1]);
      }
      if (lp >= 2) sacc = mfma32(tb[lp - 2], mu[lp - 2], sacc);
      SB();
      if (lp == 0) {
        BAR();
#pragma unroll
        for (int k = 0; k < 7; ++k) mu[k] = LDS16(lmem + (odd ? 6 - k : k) * KB + lane * 16);
        SB();
      }
    }
    tb[5][2] = pk2(acc[5][1][0], acc[5][1][1]);
    tb[5][3] = pk2(acc[5][1][2], acc[5][1][3]);
    sacc = mfma32(tb[5], mu[5], sacc);
    SB();
    tb[6] = pk8(acc[6][0], acc[6][1]);
    sacc = mfma32(tb[6], mu[6], sacc);
    if (w != 0) LDSF4(lred + w * KB + lane * 16) = sacc;
    BAR();
    if (w == 0) {
      const f4 s = (sacc + LDSF4(lred + KB + lane * 16)) + (LDSF4(lred + 2 * KB + lane * 16) + LDSF4(lred + 3 * KB + lane * 16));
      if (sval) {
        st16c(c.rs, so0 + slot + ((t & 1) ? sd1 : sd0), ns4(f2u(s)));
        if (t >= 1) st16c(c.rs, so0 + (unsigned)((t - 1) & 3) * 32 * KB + ((t & 1) ? sd0 : sd1), S4);
      }
    }
    STAMP(t, 2);
#pragma unroll
    for (int lp = 0; lp < 7; ++lp) LDS16(ltb + (w * 7 + (odd ? 6 - lp : lp)) * KB + lane * 16) = tb[lp];
    BAR();
    v4u tp[8];
#pragma unroll
    for (int ww = 0; ww < 4; ++ww) {
      tp[ww * 2 + 0] = LDS16(ltb + (ww * 7 + w) * KB + lane * 16);
      tp[ww * 2 + 1] = LDS16(ltb + (ww * 7 + gp1) * KB + lane * 16);
      if (w == 3) tp[ww * 2 + 1] = v4u{0u, 0u, 0u, 0u};
    }
    POLL(anysent(mx4(mx4(mx4(mx4(mx4(mx4(mx4(mx4(0u, X[0]), X[1]), X[2]), X[3]), X[4]), X[5]), X[6]), X[7])), load_x(t), 1);
    SB();
    {
      f4 s[4] = {Z4, Z4, Z4, Z4};
#pragma unroll
      for (int k = 0; k < 8; ++k)
#pragma unroll
        for (int lt = 0; lt < 4; ++lt) s[lt] = mfma32(tp[k], X[lt * 2 + (k & 1)], s[lt]);
#pragma unroll
      for (int lt = 0; lt < 4; ++lt) LDSF4(lred + (lt * 4 + w) * KB + lane * 16) = s[lt];
    }
    BAR();
    {
      const int l = w >> 1, tau = w & 1, s = t - l * LAG;
      const f4 v = (LDSF4(lred + (w * 4 + 0) * KB + lane * 16) + LDSF4(lred + (w * 4 + 1) * KB + lane * 16)) +
                   (LDSF4(lred + (w * 4 + 2) * KB + lane * 16) + LDSF4(lred + (w * 4 + 3) * KB + lane * 16));
      if ((q >> 1) == l) {
        const unsigned o = R_STM + (unsigned)((((a * 2 + (q & 1)) * 2 + tau) * 16 + h) * 256 + i16 * 16);
        st16c(c.rs, rec_in(l, s) + o, ns4(f2u(v)));
        if (s >= 0) st16c(c.rs, rec_in(l, s + AD) + o, S4);
      }
    }
    CBAR();
    load_x(t + 1);
    STAMP(t, 3);
  }
}

DEVI void role_CV(Ctx& c, int j, int p, unsigned char* lds, const unsigned char* mem0) {
  const int lane = c.lane, w = c.wave;
  const int i16 = lane & 15, q = lane >> 4;
  unsigned char* lmem = lds;
  unsigned char* lred = lds + 16 * KB;
  unsigned char* lXb = lds + 20 * KB;
  if (j == 0) probe_side(c, 1 + p);
  v4u W[56];
  {
    const unsigned b0 = WU_OFF + (unsigned)(j * 16 + 4 * w) * 14 * KB + lane * 16;
#pragma unroll
    for (int f = 0; f < 56; ++f) W[f] = ld16(c.rs, b0 + f * KB);
#pragma unroll
    for (int f = 0; f < 56; ++f) PIN_A(W[f]);
  }
  const v2u m0 = ns2(*(const v2u*)(mem0 + (size_t)(i16 & 7) * 896 + (j * 16 + 4 * q) * 2));
  const unsigned sides = get_sides(c);
  const unsigned sC = (sides >> (1 + p)) & 1;
  unsigned mo[4], so[4], eo[4];
#pragma unroll
  for (int k = 0; k < 4; ++k) {
    mo[k] = rmap(c, L_MEMC + (k < 3 ? k * 4096 + c.tid * 16 : 12288 + (c.tid & 127) * 16), sC);
    so[k] = rmap(c, L_S + (k * 16 + 4 * w) * 256 + lane * 16, sC);
    eo[k] = rmap(c, L_E + (4 * w + k) * KB + lane * 16, sC);
  }
  const unsigned xo = rmap(c, L_XS + ((4 * w + q) * 2 + (i16 >> 3)) * 128 + (i16 & 7) * 8, sC);
  const unsigned pf = (unsigned)(j >> 1) * KB + (unsigned)((2 * (j & 1) + (q >> 1)) * 16 + i16) * 16 + (q & 1) * 8;
  const unsigned pg = (unsigned)(i16 >> 3) * 8 * KB + (unsigned)(j >> 1) * 512 + (unsigned)(2 * (j & 1) + (q >> 1)) * 128 + (i16 & 7) * 16 + (q & 1) * 8;
  const unsigned pH = rmap(c, L_MEMH + pf, sides & 1), pC = rmap(c, L_MEMC + pf, (sides >> (2 - p)) & 1), pG = rmap(c, L_MEMG + pg, (sides >> (4 - p)) & 1);
  const unsigned pR = (w == 1) ? pH : (w == 2) ? pG : (w == 3) ? pC : OOB;
  const unsigned zo = R_ZB + (unsigned)(j * 4 + w) * KB + lane * 16;
  const unsigned uo = (unsigned)(j * 16 + 4 * w) * KB + lane * 16;
  const unsigned fo = (lane == 0) ? F_U + (unsigned)(p * 28 + j) * 64 + w * 4 : OOB;
#define XBAD anysent(mx4(mx4(mx4(mx4(mx4(mx4(mx4(0u, x[0]), x[1]), x[2]), x[3]), x[4]), x[5]), x[6]))
  v4u U[8], x[7];
  auto load_x = [&](int l, int s) {
    const unsigned xb = rec_in(l, s) + R_XN + c.tid * 16;
#pragma unroll
    for (int f = 0; f < 7; ++f) x[f] = ld16c(c.rs, xb + f * 4096);
  };
  auto stage_x = [&](int l) {
#pragma unroll
    for (int f = 0; f < 7; ++f) LDS16(lXb + l * 28 * KB + f * 4096 + c.tid * 16) = x[f];
  };
  unsigned* lhave = (unsigned*)(lds + 15 * KB);
  bool have = false, allhave = false;
  for (int t = p; t < NT; t += 2) {
    const unsigned slot = (unsigned)(t & 3) * 32 * KB, eslot = (unsigned)(t & 7) * 32 * KB, xslot = (unsigned)(t & 7) * 16 * KB;
    const int s0 = t, s1 = t - LAG;
    STAMP(t, 0);
    if (!allhave) {
      if (!have) {
        CBAR();
        load_x(0, s0);
        POLL(XBAD, load_x(0, s0), 1);
        stage_x(0);
      }
      BAR();
    }
    CBAR();
    STAMPC(t, 7);
    load_x(1, s1);
    SB();
    STAMP(72 + (t >> 1), 0);
    f4 acc[8];
    const unsigned ub0 = u_in(0, s0) + uo, za0 = rec_in(0, s0 + AD) + zo, xb0 = rec_in(0, s0 + 2) + R_XN + c.tid * 16;
    auto side_op = [&](int k) {
      if (k < 4) st16c(c.rs, ub0 + k * KB, U[k]);
      else if (k == 4) st16c(c.rs, za0, S4);
      else if (k < 12) x[k - 5] = ld16c(c.rs, xb0 + (k - 5) * 4096);
    };
#pragma unroll
    for (int l = 0; l < 2; ++l) {
      const int s = l ? s1 : s0;
      const bool act = (s >= 0 && s < NSEG);
#pragma unroll
      for (int k = 0; k < 8; ++k) acc[k] = Z4;
      if (act) {
        const unsigned char* xp = lXb + l * 28 * KB + lane * 16;
        v4u xa = LDS16(xp), xb = LDS16(xp + 14 * KB), ya = LDS16(xp + KB), yb = LDS16(xp + 15 * KB);
        SB();
#pragma unroll
        for (int ks = 0; ks < 14; ++ks) {
          const v4u x0 = xa, x1 = xb;
          xa = ya; xb = yb;
          if (ks + 2 < 14) { ya = LDS16(xp + (ks + 2) * KB); yb = LDS16(xp + (16 + ks) * KB); }
#pragma unroll
          for (int hh = 0; hh < 4; ++hh) {
            acc[hh * 2 + 0] = mfma32(x0, W[hh * 14 + ks], acc[hh * 2 + 0]);
            acc[hh * 2 + 1] = mfma32(x1, W[hh * 14 + ks], acc[hh * 2 + 1]);
            if (l == 1 && hh == 1) { SB(); side_op(ks); SB(); }
          }
          SB();
        }
      }
      else if (l == 1) {
#pragma unroll
        for (int k = 0; k < 12; ++k) side_op(k);
      }
      STAMP(72 + (t >> 1), l ? 6 : 1);
      if (l == 0) {
        const bool bad = XBAD;
#pragma unroll
        for (int f = 0; f < 7; ++f) {
          LDS16(lXb + 28 * KB + f * 4096 + c.tid * 16) = x[f];
          if (f < 4) { U[f] = pk8(acc[f * 2], acc[f * 2 + 1]); PIN_V(U[f]); }
          SB();
        }
        STAMP(72 + (t >> 1), 2);
        if (bad) {
          POLL2(true, XBAD, load_x(1, s1), 1);
          stage_x(1);
        }
        CBAR();
        STAMP(72 + (t >> 1), 4);
        BAR();
        SB();
        STAMP(72 + (t >> 1), 5);
      }
    }
    STAMP(t, 1);
    v4u m[4];
    CBAR();
#pragma unroll
    for (int k = 0; k < 4; ++k) m[k] = ld16c(c.rs, mo[k] + slot);
    SB();
#pragma unroll
    for (int hh = 0; hh < 4; ++hh) { U[4 + hh] = pk8(acc[hh * 2], acc[hh * 2 + 1]); PIN_V(U[4 + hh]); }
    SB();
    have = !XBAD;
    if (lane == 0) lhave[w] = have ? 1u : 0u;
    SB();
    STAMP(72 + (t >> 1), 7);
    POLL(anysent(mx4(mx4(mx4(mx4(0u, m[0]), m[1]), m[2]), m[3])),
         { _Pragma("unroll") for (int k = 0; k < 4; ++k) m[k] = ld16c(c.rs, mo[k] + slot); }, 0);
#pragma unroll
    for (int k = 0; k < 3; ++k) LDS16(lmem + k * 4096 + c.tid * 16) = m[k];
    LDS16(lmem + 12288 + (c.tid & 127) * 16) = m[3];
    DRAIN();
    st4c(c.rs, fo, (unsigned)min(s0 + 1, NSEG));
    {
      const unsigned ub = u_in(1, s1) + uo;
#pragma unroll
      for (int hh = 0; hh < 4; ++hh) st16c(c.rs, ub + hh * KB, U[4 + hh]);
      st16c(c.rs, (s1 >= 0 ? rec_in(1, s1 + AD) : OOB) + zo, S4);
    }
    STAMP(t, 2);
    BAR();
    {
      const v4u hv = LDS16((unsigned char*)lhave);
      allhave = __builtin_amdgcn_readfirstlane(hv.x & hv.y & hv.z & hv.w) != 0u;
    }
    if (t >= 1) st8c(c.rs, pR + (unsigned)((t - 1) & 3) * 32 * KB, S2);
    v4u zA[2];
    {
      v4u mf[14];
#pragma unroll
      for (int ks = 0; ks < 14; ++ks) mf[ks] = LDS16(lmem + ks * KB + lane * 16);
      SB();
      f4 z[4] = {Z4, Z4, Z4, Z4};
#pragma unroll
      for (int ks = 0; ks < 14; ++ks) {
#pragma unroll
        for (int hh = 0; hh < 4; ++hh) z[hh] = mfma32(mf[ks], W[hh * 14 + ks], z[hh]);
        SB();
      }
      unsigned zl[4], zh[4];
#pragma unroll
      for (int hh = 0; hh < 4; ++hh) { zl[hh] = pk2(z[hh][0], z[hh][1]); zh[hh] = pk2(z[hh][2], z[hh][3]); }
      swap16(zl[0], zl[1]); swap16(zl[2], zl[3]); swap32(zl[0], zl[2]); swap32(zl[1], zl[3]);
      swap16(zh[0], zh[1]); swap16(zh[2], zh[3]); swap32(zh[0], zh[2]); swap32(zh[1], zh[3]);
      zA[0] = v4u{zl[0], zh[0], zl[1], zh[1]};
      zA[1] = v4u{zl[2], zh[2], zl[3], zh[3]};
    }
    STAMP(t, 3);
    v4u E[4], sv[4];
    v2u xs;
    auto load_e = [&]() {
#pragma unroll
      for (int hh = 0; hh < 4; ++hh) E[hh] = ld16c(c.rs, eo[hh] + eslot);
      xs = ld8c(c.rs, xo + xslot);
    };
    auto load_s = [&]() {
#pragma unroll
      for (int k = 0; k < 4; ++k) sv[k] = ld16c(c.rs, so[k] + slot);
    };
    CBAR();
    load_e();
    SB();
    stage_x(0);
    SB();
    load_s();
    POLL(anysent(mx2(mx4(mx4(mx4(mx4(0u, E[0]), E[1]), E[2]), E[3]), xs)), { load_e(); load_s(); }, 0);
    SB();
    f4 ox[4];
#pragma unroll
    for (int hh = 0; hh < 4; ++hh) {
      const f4 o0 = mfma32(U[hh], E[hh], Z4), o1 = mfma32(U[4 + hh], E[hh], Z4);
      ox[hh] = (i16 < 8) ? o0 : o1;
    }
#pragma unroll
    for (int hh = 0; hh < 4; ++hh) PIN_V(ox[hh]);
    STAMP(t, 4);
    SB();
    POLL(anysent(mx4(mx4(mx4(mx4(0u, sv[0]), sv[1]), sv[2]), sv[3])), load_s(), 0);
    STAMP(t, 5);
    DRAIN();
    st4c(c.rs, fo + 16, (unsigned)max(s1 + 1, 0));
    {
      f4 a = u2f(sv[0]) + u2f(sv[2]), b = u2f(sv[1]) + u2f(sv[3]);
      const float mxx = __uint_as_float(xs.x), lx = __uint_as_float(xs.y);
      const float mm = mxf4(b, mxf4(a, mxx));
      const float ml = mm * LOG2E;
#pragma unroll
      for (int r = 0; r < 4; ++r) { a[r] = ex2(a[r] * LOG2E - ml); b[r] = ex2(b[r] * LOG2E - ml); }
      const float ax = ex2(mxx * LOG2E - ml);
      const float den = ((a[0] + a[1]) + (a[2] + a[3])) + ((b[0] + b[1]) + (b[2] + b[3])) + ax * lx;
      const float inv = rcp(den);
      const v4u P = pk8(a * inv, b * inv);
      const f4 d0 = mfma32(zA[0], P, Z4), d1 = mfma32(zA[1], P, Z4);
      float wx[4];
      rbcast4(ax * inv, wx);
      f4 acc = (ox[0] * wx[0] + ox[1] * wx[1]) + (ox[2] * wx[2] + ox[3] * wx[3]);
      acc += (i16 < 8) ? d0 : d1;
      if (w != 0) LDSF4(lred + w * KB + lane * 16) = acc;
      BAR();
      if (w == 0) {
        acc = (acc + LDSF4(lred + KB + lane * 16)) + (LDSF4(lred + 2 * KB + lane * 16) + LDSF4(lred + 3 * KB + lane * 16));
        v2u o = ns2(pk4(acc));
        if (i16 >= 8 && t + 1 <= LAG) o = m0;
        const unsigned nslot = (unsigned)((t + 1) & 3) * 32 * KB;
        st8c(c.rs, pH + nslot, o);
        st8c(c.rs, pG + nslot, o);
        st8c(c.rs, pC + nslot, o);
      }
      st16c(c.rs, rec_in(0, s0) + zo, ns4(zA[0]));
      st16c(c.rs, rec_in(1, s1) + zo, ns4(zA[1]));
    }
    STAMP(t, 6);
  }
}

template <int L>
DEVI void role_GX(Ctx& c, int h, int p, unsigned char* lds) {
  const int lane = c.lane, w = c.wave;
  const int i16 = lane & 15, q = lane >> 4;
  unsigned char* lX = lds;
  unsigned char* lG = lds + 28 * KB;
  unsigned char* lred = lds + 60 * KB;
  if (h == 0 && L == 0) probe_side(c, 3 + p);
  v4u W[98];
  {
    const unsigned b0 = WG_OFF + (unsigned)(h * 14 + 3 * w) * 28 * KB + lane * 16;
#pragma unroll
    for (int f = 0; f < 84; ++f) W[f] = ld16(c.rs, b0 + f * KB);
    const unsigned b1 = WG_OFF + (unsigned)(h * 14 + 12 + (w >> 1)) * 28 * KB + (w & 1) * 7 * KB + lane * 16;
#pragma unroll
    for (int k = 0; k < 7; ++k) { W[84 + k] = ld16(c.rs, b1 + k * KB); W[91 + k] = ld16(c.rs, b1 + (14 + k) * KB); }
#pragma unroll
    for (int f = 0; f < 56; ++f) PIN_A(W[f]);
  }
  const unsigned sides = get_sides(c);
  const unsigned sC = (sides >> (1 + p)) & 1;
  const int kx = 7 * (w & 1);
  unsigned mo[4];
#pragma unroll
  for (int k = 0; k < 4; ++k) {
    const int vu = 4 * w + k, ks = vu < 12 ? vu : 12 + ((vu - 12) >> 1);
    mo[k] = rmap(c, L_MEMG + L * 8 * KB + ks * 512 + q * 128 + (i16 & 7) * 16, (sides >> (3 + p)) & 1);
  }
  const unsigned eo = rmap(c, L_E + h * KB + (q * 16 + L * 8 + (i16 & 7)) * 16 + (i16 >> 3) * 8, sC);
  const unsigned xo = rmap(c, L_XS + (h * 2 + L) * 128 + (i16 & 7) * 8, sC);
  v4u x[7];
  auto load_x = [&](int s) {
    const unsigned xb = rec_in(L, s) + R_XN + c.tid * 16;
#pragma unroll
    for (int f = 0; f < 7; ++f) x[f] = ld16c(c.rs, xb + f * 4096);
  };
  bool have = false;
  for (int t = p; t < NT; t += 2) {
    const int s = t - L * LAG;
    const bool act = (s >= 0 && s < NSEG);
    v4u g[8];
    STAMP(t, 0);
    if (act) {
      if (!have) {
        CBAR();
        load_x(s);
        POLL(anysent(mx4(mx4(mx4(mx4(mx4(mx4(mx4(0u, x[0]), x[1]), x[2]), x[3]), x[4]), x[5]), x[6])), load_x(s), 1);
      }
#pragma unroll
      for (int f = 0; f < 7; ++f) LDS16(lX + f * 4096 + c.tid * 16) = x[f];
      BAR();
      STAMP(t, 1);
#pragma unroll 1
      for (int tau = 0; tau < 2; ++tau) {
        const unsigned char* xp = lX + tau * 14 * KB + lane * 16;
        const unsigned xnext = (tau ? rec_in(L, s + 2) : OOB) + R_XN + c.tid * 16;
        f4 acc[8] = {Z4, Z4, Z4, Z4, Z4, Z4, Z4, Z4};
        v4u xs[7];
#pragma unroll
        for (int k = 0; k < 7; ++k) xs[k] = LDS16(xp + (kx + k) * KB);
        v4u x0 = LDS16(xp), x1 = LDS16(xp + KB);
        SB();
#pragma unroll
        for (int k = 0; k < 7; ++k) {
          acc[6] = mfma32(W[84 + k], xs[k], acc[6]);
          acc[7] = mfma32(W[91 + k], xs[k], acc[7]);
        }
        SB();
        unsigned char* gp = lG + tau * 16 * KB + lane * 16;
#pragma unroll
        for (int ks = 0; ks < 14; ++ks) {
          const v4u xk = x0;
          x0 = x1;
          if (ks + 2 < 14) x1 = LDS16(xp + (ks + 2) * KB);
          if (ks == 2) LDS16(gp + (12 + w) * KB) = pk8(acc[6], acc[7]);
#pragma unroll
          for (int pp = 0; pp < 3; ++pp) {
            acc[pp * 2 + 0] = mfma32(W[pp * 28 + ks], xk, acc[pp * 2 + 0]);
            acc[pp * 2 + 1] = mfma32(W[pp * 28 + 14 + ks], xk, acc[pp * 2 + 1]);
            if (L == 0 && pp == 0 && ks >= 3 && ks < 10) { SB(); x[ks - 3] = ld16c(c.rs, xnext + (ks - 3) * 4096); SB(); }
          }
          SB();
        }
#pragma unroll
        for (int pp = 0; pp < 3; ++pp) LDS16(gp + (3 * w + pp) * KB) = pk8(acc[pp * 2], acc[pp * 2 + 1]);
      }
      BAR();
#pragma unroll
      for (int k = 0; k < 4; ++k) { g[k] = LDS16(lG + (4 * w + k) * KB + lane * 16); g[4 + k] = LDS16(lG + (16 + 4 * w + k) * KB + lane * 16); }
    } else {
#pragma unroll
      for (int k = 0; k < 8; ++k) g[k] = v4u{0u, 0u, 0u, 0u};
    }
    STAMP(t, 2);
    {
      const unsigned slot = (unsigned)(t & 3) * 32 * KB;
      v4u m[4];
      CBAR();
#pragma unroll
      for (int k = 0; k < 4; ++k) m[k] = ld16c(c.rs, mo[k] + slot);
      POLL(anysent(mx4(mx4(mx4(mx4(0u, m[0]), m[1]), m[2]), m[3])),
           { _Pragma("unroll") for (int k = 0; k < 4; ++k) m[k] = ld16c(c.rs, mo[k] + slot); }, 0);
      STAMP(t, 3);
      f4 a0 = Z4, a1 = Z4;
#pragma unroll
      for (int k = 0; k < 4; ++k) { a0 = mfma32(g[k], m[k], a0); a1 = mfma32(g[4 + k], m[k], a1); }
      LDSF4(lred + 4 * KB + w * KB + lane * 16) = (i16 < 8) ? a0 : a1;
      BAR();
      if (w == 0) {
        const unsigned char* rp = lred + 4 * KB + lane * 16;
        f4 v = (LDSF4(rp) + LDSF4(rp + KB)) + (LDSF4(rp + 2 * KB) + LDSF4(rp + 3 * KB));
        float m1 = mxf4(v, NEG);
        m1 = mx3(m1, ror8(m1), NEG);
        m1 = rmax4(m1);
        const float ml = m1 * LOG2E;
#pragma unroll
        for (int r = 0; r < 4; ++r) v[r] = ex2(v[r] * LOG2E - ml);
        const unsigned eslot = (unsigned)(t & 7) * 32 * KB, xslot = (unsigned)(t & 7) * 16 * KB;
        st8c(c.rs, eo + eslot, act ? ns2(pk4(v)) : v2u{0u, 0u});
        float ls = (v[0] + v[1]) + (v[2] + v[3]);
        ls += ror8(ls);
        ls = rsum4(ls);
        if (lane < 8) st8c(c.rs, xo + xslot, act ? ns2(v2u{__float_as_uint(m1), __float_as_uint(ls)}) : v2u{__float_as_uint(-1.0e30f), 0u});
        if (t >= 2) {
          st8c(c.rs, eo + (unsigned)((t - 2) & 7) * 32 * KB, S2);
          if (lane < 8) st8c(c.rs, xo + (unsigned)((t - 2) & 7) * 16 * KB, S2);
        }
      }
      STAMP(t, 4);
    }
    if (act) {
      const int tq = w & 1, kh = w >> 1;
      f4 a0 = Z4, a1 = Z4;
#pragma unroll
      for (int hf = 0; hf < 3; ++hf) {
        v4u ga[3], gb[3], xq[3];
#pragma unroll
        for (int k = 0; k < 3; ++k)
          if (hf * 3 + k < 8) {
            const int vu = kh * 8 + hf * 3 + k, ks = vu < 12 ? vu : 12 + ((vu - 12) >> 1);
            ga[k] = LDS16(lG + vu * KB + lane * 16);
            gb[k] = LDS16(lG + (16 + vu) * KB + lane * 16);
            xq[k] = LDS16(lX + (tq * 14 + ks) * KB + lane * 16);
          }
        SB();
#pragma unroll
        for (int k = 0; k < 3; ++k)
          if (hf * 3 + k < 8) { a0 = mfma32(ga[k], xq[k], a0); a1 = mfma32(gb[k], xq[k], a1); }
        SB();
      }
      if (!kh) { LDSF4(lred + (tq * 2 + 0) * KB + lane * 16) = a0; LDSF4(lred + (tq * 2 + 1) * KB + lane * 16) = a1; }
      BAR();
      if (L == 0) have = !anysent(mx4(mx4(mx4(mx4(mx4(mx4(mx4(0u, x[0]), x[1]), x[2]), x[3]), x[4]), x[5]), x[6]));
      if (kh) {
        a0 += LDSF4(lred + (tq * 2 + 0) * KB + lane * 16);
        a1 += LDSF4(lred + (tq * 2 + 1) * KB + lane * 16);
        const int lim = tok_of(tq, i16) - 8 * q;
#pragma unroll
        for (int r = 0; r < 4; ++r) {
          a0[r] = (r <= lim) ? a0[r] : NEG;
          a1[r] = (4 + r <= lim) ? a1[r] : NEG;
        }
        const float m = rmax4(mxf4(a1, mxf4(a0, NEG)));
        const float ml = m * LOG2E;
        float lsum = 0.f;
#pragma unroll
        for (int r = 0; r < 4; ++r) {
          a0[r] = (r <= lim) ? ex2(a0[r] * LOG2E - ml) : 0.f;
          a1[r] = (4 + r <= lim) ? ex2(a1[r] * LOG2E - ml) : 0.f;
          lsum += a0[r] + a1[r];
        }
        lsum = rsum4(lsum);
        const unsigned o1 = R_ETT + (unsigned)(h * 2 + tq) * KB + lane * 16, o2 = R_ETS + (unsigned)((tq * 16 + h) * 16 + i16) * 8;
        st16c(c.rs, rec_off(L, s) + o1, ns4(pk8(a0, a1)));
        if (q == 0) st8c(c.rs, rec_off(L, s) + o2, ns2(v2u{__float_as_uint(m), __float_as_uint(lsum)}));
        st16c(c.rs, rec_in(L, s + AD) + o1, S4);
        if (q == 0) st8c(c.rs, rec_in(L, s + AD) + o2, S2);
      }
    }
    STAMP(t, 5);
  }
}

template <int L>
DEVI void role_O(Ctx& c, int tau, int cg, unsigned char* lds, __hip_bfloat16* out) {
  const int lane = c.lane, w = c.wave;
  const int i16 = lane & 15, q = lane >> 4;
  const int ct = 4 * cg + w;
  const unsigned po = (unsigned)(tau * 14 + (ct >> 1)) * KB + (unsigned)((2 * (ct & 1) + (q >> 1)) * 16 + i16) * 16 + (q & 1) * 8;
  v4u U[16], e[4], sv[4], z[4];
  v2u es, xr;
  auto load_u = [&](int s) {
    const unsigned ub = u_in(L, s) + (unsigned)(4 * cg * 16 + 4 * w) * KB + lane * 16;
#pragma unroll
    for (int k = 0; k < 4; ++k)
#pragma unroll
      for (int hh = 0; hh < 4; ++hh) U[k * 4 + hh] = ld16c(c.rs, ub + (k * 16 + hh) * KB);
    xr = ld8c(c.rs, rec_in(L, s) + R_XR + po);
  };
  auto load_e = [&](int s) {
    const unsigned rb = rec_off(L, s);
#pragma unroll
    for (int hh = 0; hh < 4; ++hh) e[hh] = ld16c(c.rs, rb + R_ETT + (unsigned)((4 * w + hh) * 2 + tau) * KB + lane * 16);
    es = ld8c(c.rs, rb + R_ETS + (unsigned)((tau * 16 + 4 * w) * 16 + lane) * 8);
  };
  auto load_s = [&](int s) {
    const unsigned rb = rec_off(L, s);
#pragma unroll
    for (int k = 0; k < 4; ++k) sv[k] = ld16c(c.rs, rb + R_STM + (unsigned)((k * 2 + tau) * 16 + 4 * w) * 256 + lane * 16);
#pragma unroll
    for (int k = 0; k < 4; ++k) z[k] = ld16c(c.rs, rb + R_ZB + (unsigned)((4 * cg + k) * 4 + w) * KB + lane * 16);
  };
  const int fpar = (lane >> 2) & 1;
  const unsigned fo = (lane & 8) == 0 ? F_U + (unsigned)(fpar * 28 + 4 * cg + (lane & 3)) * 64 + L * 16 + w * 4
                                      : F_XR + (unsigned)((L * 2 + tau) * 2 + fpar) * 64 + (lane & 3) * 4;
  int fl = 0;
  for (int s = 0; s < NSEG; ++s) {
    unsigned char* lred = lds + (s & 1) * 16 * KB;
    STAMP(s, 0);
    {
      const int need = (fpar != ((((lane & 8) == 0 ? s + L * LAG : s)) & 1)) ? 0 : s + 1;
      POLL(__builtin_amdgcn_ballot_w64(fl < need) != 0ull, fl = (int)ld4c(c.rs, fo), 1);
      CBAR();
      load_u(s);
    }
    STAMP(s, 1);
    CBAR();
    load_e(s);
    load_s(s);
    fl = (int)ld4c(c.rs, fo);
    POLL(anysent(mx4(mx4(mx4(mx4(mx4(mx4(mx4(mx4(mx2(mx4(mx4(mx4(mx4(0u, e[0]), e[1]), e[2]), e[3]), es), sv[0]), sv[1]), sv[2]), sv[3]), z[0]), z[1]), z[2]), z[3])),
         { load_e(s); load_s(s); fl = (int)ld4c(c.rs, fo); }, 1);
    TOUCH(fl);
    SB();
    f4 ott[16];
#pragma unroll
    for (int k = 0; k < 4; ++k)
#pragma unroll
      for (int hh = 0; hh < 4; ++hh) ott[k * 4 + hh] = mfma32(U[k * 4 + hh], e[hh], Z4);
    STAMP(s, 2);
    {
      f4 a = u2f(sv[0]) + u2f(sv[2]), b = u2f(sv[1]) + u2f(sv[3]);
      const float mt = __uint_as_float(es.x), lt = __uint_as_float(es.y);
      const float mm = mxf4(b, mxf4(a, mt));
      const float ml = mm * LOG2E;
#pragma unroll
      for (int r = 0; r < 4; ++r) { a[r] = ex2(a[r] * LOG2E - ml); b[r] = ex2(b[r] * LOG2E - ml); }
      const float at = ex2(mt * LOG2E - ml);
      const float den = ((a[0] + a[1]) + (a[2] + a[3])) + ((b[0] + b[1]) + (b[2] + b[3])) + at * lt;
      const float inv = rcp(den);
      const v4u P = pk8(a * inv, b * inv);
      float wt[4];
      rbcast4(at * inv, wt);
#pragma unroll
      for (int k = 0; k < 4; ++k) {
        f4 acc = mfma32(z[k], P, Z4);
        acc += (ott[k * 4 + 0] * wt[0] + ott[k * 4 + 1] * wt[1]) + (ott[k * 4 + 2] * wt[2] + ott[k * 4 + 3] * wt[3]);
        LDSF4(lred + (k * 4 + w) * KB + lane * 16) = acc;
      }
    }
    BAR();
    {
      const unsigned char* rp = lred + w * 4 * KB + lane * 16;
      const f4 acc = (LDSF4(rp) + LDSF4(rp + KB)) + (LDSF4(rp + 2 * KB) + LDSF4(rp + 3 * KB));
      const v2u y = pk4(acc);
      const v2u o = v2u{pk2(bflo(xr.x) + bflo(y.x), bfhi(xr.x) + bfhi(y.x)), pk2(bflo(xr.y) + bflo(y.y), bfhi(xr.y) + bfhi(y.y))};
      if (L == 0) {
        st8c(c.rs, rec_off(0, s) + R_X2 + po, ns2(o));
        st8c(c.rs, rec_in(0, s + AD) + R_X2 + po, S2);
      } else {
        *(v2u*)((unsigned char*)out + (size_t)(s * 32 + tok_of(tau, i16)) * 896 + (ct * 16 + 4 * q) * 2) = o;
      }
    }
    STAMP(s, 3);
  }
}

template <int L>
DEVI void role_AH(Ctx& c, int a, unsigned char* lds) {
  const int lane = c.lane, w = c.wave;
  const int i16 = lane & 15, q = lane >> 4;
  unsigned char* lX = lds;
  unsigned char* lK = lds + 28 * KB;
  unsigned char* lV = lds + 46 * KB;
  unsigned char* lQ = lds + 64 * KB;
  unsigned char* lst = lds + 66 * KB;
  unsigned char* lmg = lds + 67 * KB;
  unsigned char* lms = lds + 71 * KB;
  const int tau = w & 1, g = w >> 1;
  v4u x[7];
  auto load_x = [&](int s) {
    const unsigned xb = (L == 0 ? X0_OFF + (unsigned)s * SEGX : rec_off(0, s) + R_X2) + (unsigned)(tau * 14 + g * 7) * KB + lane * 16;
#pragma unroll
    for (int f = 0; f < 7; ++f) x[f] = (L == 0) ? ld16(c.rs, xb + f * KB) : ld16c(c.rs, xb + f * KB);
  };
  if (L == 0) load_x(0);
  v4u W[42];
#pragma unroll
  for (int tl = 0; tl < 3; ++tl) {
    const unsigned b0 = WQ_OFF + (unsigned)(a * 6 + (tl < 2 ? 2 * g + tl : 4 + g)) * 14 * KB + lane * 16;
#pragma unroll
    for (int k = 0; k < 14; ++k) W[tl * 14 + k] = ld16(c.rs, b0 + (unsigned)(k < 7 ? 7 * g + k : 7 * (1 - g) + k - 7) * KB);
  }
  for (int o = c.tid * 16; o < 36 * (int)KB; o += 4096) LDS16(lK + o) = v4u{0u, 0u, (o < 18 * (int)KB && q == 3) ? 0xFF7Fu : 0u, 0u};
  f4 cs0 = Z4, cs1 = Z4, cs2 = Z4;
  {
    const v4u one = {0x3F803F80u, 0x3F803F80u, 0x3F803F80u, 0x3F803F80u};
#pragma unroll
    for (int k = 0; k < 14; ++k) { cs0 = mfma32(W[k], one, cs0); cs1 = mfma32(W[14 + k], one, cs1); cs2 = mfma32(one, W[28 + k], cs2); }
  }
#pragma unroll
  for (int f = 0; f < 42; ++f) PIN_A(W[f]);
  const float csv = cs2[0];
  const float vone = (g == 1 && i16 == 12) ? 1.0f : 0.0f;
  const float qone = (g == 0 && q == 3) ? 1.0f : 0.0f;
  const float sc = 0.18898223650461362f * LOG2E;
  const int lim = tok_of(tau, i16) - 8 * q;
  v4u dm;
#pragma unroll
  for (int e = 0; e < 4; ++e) dm[e] = ((2 * e <= lim) ? 0xFFFFu : 0u) | ((2 * e + 1 <= lim) ? 0xFFFF0000u : 0u);
  const int c0 = 28 * a + 4 * q, c1 = c0 + 16;
  const unsigned oa = R_OA + (unsigned)tau * 14 * KB + (c0 >> 5) * KB + (((c0 >> 3) & 3) * 16 + i16) * 16 + (c0 & 4) * 2;
  const unsigned ob = (q < 3) ? R_OA + (unsigned)tau * 14 * KB + (c1 >> 5) * KB + (((c1 >> 3) & 3) * 16 + i16) * 16 + (c1 & 4) * 2 : OOB;
  CBAR();
  if (L == 0) {
#pragma unroll
    for (int f = 0; f < 7; ++f) TOUCH(x[f]);
  }
  int slot = 0;
  bool have = false;
  for (int s = 0; s < NSEG; ++s) {
    STAMP(s, 0);
    if (L == 1 && !have) {
      CBAR();
      load_x(s);
      POLL(XBAD, load_x(s), 1);
    }
    STAMP(s, 1);
#pragma unroll
    for (int f = 0; f < 7; ++f) LDS16(lX + (tau * 14 + g * 7 + f) * KB + lane * 16) = x[f];
    f4 a0 = Z4, a1 = Z4, a2 = Z4;
    {
      float sum = 0.f, sq = 0.f;
#pragma unroll
      for (int f = 0; f < 7; ++f) {
        a0 = mfma32(W[f], x[f], a0);
        a1 = mfma32(W[14 + f], x[f], a1);
        a2 = mfma32(x[f], W[28 + f], a2);
#pragma unroll
        for (int e = 0; e < 4; ++e) { sum = dot2(x[f][e], 0x3F803F80u, sum); sq = dot2(x[f][e], x[f][e], sq); }
        SB();
      }
      sum = rsum4(sum);
      sq = rsum4(sq);
      if (q == 0) *(f2*)(lst + ((g * 2 + tau) * 16 + i16) * 8) = f2{sum, sq};
    }
    BAR();
    {
      v4u y[7];
#pragma unroll
      for (int f = 0; f < 7; ++f) y[f] = LDS16(lX + (tau * 14 + (1 - g) * 7 + f) * KB + lane * 16);
      const f2 sa = *(const f2*)(lst + (tau * 16 + i16) * 8), sb = *(const f2*)(lst + ((2 + tau) * 16 + i16) * 8);
      const f4 t0 = LDSF4(lst + (tau * 16 + 4 * q) * 8), t1 = LDSF4(lst + (tau * 16 + 4 * q) * 8 + 16);
      const f4 t2 = LDSF4(lst + ((2 + tau) * 16 + 4 * q) * 8), t3 = LDSF4(lst + ((2 + tau) * 16 + 4 * q) * 8 + 16);
      CBAR();
      if (L == 0) load_x(min(s + 1, NSEG - 1));
      SB();
#pragma unroll
      for (int f = 0; f < 7; ++f) {
        a0 = mfma32(W[7 + f], y[f], a0);
        a1 = mfma32(W[21 + f], y[f], a1);
        a2 = mfma32(y[f], W[35 + f], a2);
      }
      const float mean = (sa[0] + sb[0]) * (1.0f / 448.0f);
      const float rstd = rsq(fmaxf((sa[1] + sb[1]) * (1.0f / 448.0f) - mean * mean, 0.f) + 1e-5f);
      const float mr = mean * rstd;
      const f4 sm = f4{t0[0] + t2[0], t0[2] + t2[2], t1[0] + t3[0], t1[2] + t3[2]} * (1.0f / 448.0f);
      const f4 s2 = f4{t0[1] + t2[1], t0[3] + t2[3], t1[1] + t3[1], t1[3] + t3[3]} * (1.0f / 448.0f);
      f4 rs;
#pragma unroll
      for (int r = 0; r < 4; ++r) rs[r] = rsq(fmaxf(s2[r] - sm[r] * sm[r], 0.f) + 1e-5f);
      a0 = a0 * rstd - cs0 * mr;
      a1 = a1 * rstd - cs1 * mr;
      a1[0] += qone;
      LDS16((g == 0 ? lQ + tau * KB : lK + (slot * 2 + tau) * KB) + lane * 16) = pk8(a0, a1);
      a2 = (a2 - sm * csv) * rs + vone;
      *(v2u*)(lV + (slot * 2 + g) * KB + lane * 16 + tau * 8) = pk4(a2);
    }
    BAR();
    STAMP(s, 2);
    {
      const v4u qf = LDS16(lQ + tau * KB + lane * 16);
      int sl[5];
      v4u kf[10];
#pragma unroll
      for (int b = 0; b < 5; ++b) {
        const int d = (b < 4) ? 4 * g + b : 8;
        int k = slot - d;
        k += (k < 0) ? 9 : 0;
        sl[b] = k;
        if (b < 4 || g == 0) { kf[2 * b] = LDS16(lK + (k * 2) * KB + lane * 16); kf[2 * b + 1] = LDS16(lK + (k * 2 + 1) * KB + lane * 16); }
      }
      SB();
      f4 scv[4][2];
#pragma unroll
      for (int b = 0; b < 4; ++b) {
        scv[b][0] = mfma32(kf[2 * b], qf, Z4);
        scv[b][1] = mfma32(kf[2 * b + 1], qf, Z4);
      }
      v4u vf[10];
      f4 e0 = Z4, e1 = Z4;
      if (g == 0) { e0 = mfma32(kf[8], qf, Z4); e1 = mfma32(kf[9], qf, Z4); }
      SB();
#pragma unroll
      for (int b = 0; b < 5; ++b)
        if (b < 4 || g == 0) { vf[2 * b] = LDS16(lV + (sl[b] * 2) * KB + lane * 16); vf[2 * b + 1] = LDS16(lV + (sl[b] * 2 + 1) * KB + lane * 16); }
      if (L == 1) { CBAR(); load_x(min(s + 1, NSEG - 1)); }
      SB();
#pragma unroll
      for (int b = 0; b < 4; ++b) { PIN_V(scv[b][0]); PIN_V(scv[b][1]); }
      if (g == 0) {
        PIN_V(e0); PIN_V(e1);
#pragma unroll
        for (int r = 0; r < 4; ++r) {
          scv[0][0][r] = (r <= lim) ? scv[0][0][r] : e0[r];
          scv[0][1][r] = (4 + r <= lim) ? scv[0][1][r] : e1[r];
        }
      }
      float m = -1.0e30f;
#pragma unroll
      for (int b = 0; b < 4; ++b) m = mxf4(scv[b][1], mxf4(scv[b][0], m));
      m = rmax4(m);
      const float ml = m * sc;
      f4 o0 = Z4, o1 = Z4;
#pragma unroll
      for (int bb = 0; bb < 4; ++bb) {
        const int b = 3 - bb;
#pragma unroll
        for (int r = 0; r < 4; ++r) {
          scv[b][0][r] = ex2(scv[b][0][r] * sc - ml);
          scv[b][1][r] = ex2(scv[b][1][r] * sc - ml);
        }
        v4u p = pk8(scv[b][0], scv[b][1]);
        if (b == 0 && g == 0) {
          const v4u p8 = p & ~dm;
          p = p & dm;
          o0 = mfma32(vf[8], p8, o0);
          o1 = mfma32(vf[9], p8, o1);
        }
        o0 = mfma32(vf[2 * b], p, o0);
        o1 = mfma32(vf[2 * b + 1], p, o1);
      }
      if (g == 1) {
        LDSF4(lmg + (tau * 2 + 0) * KB + lane * 16) = o0;
        LDSF4(lmg + (tau * 2 + 1) * KB + lane * 16) = o1;
        *(float*)(lms + (tau * 64 + lane) * 4) = m;
      }
      if (L == 0) {
#pragma unroll
        for (int f = 0; f < 7; ++f) TOUCH(x[f]);
      } else {
        have = !XBAD;
      }
      BAR();
      if (g == 0) {
        const float mo = *(const float*)(lms + (tau * 64 + lane) * 4);
        const float mt = mx3(m, mo, NEG);
        const float fa = ex2((m - mt) * sc), fb = ex2((mo - mt) * sc);
        const f4 r0 = o0 * fa + LDSF4(lmg + (tau * 2 + 0) * KB + lane * 16) * fb;
        const f4 r1 = o1 * fa + LDSF4(lmg + (tau * 2 + 1) * KB + lane * 16) * fb;
        float dn[4];
        rbcast4(r1[0], dn);
        const float inv = rcp(dn[3]);
        st8c(c.rs, rec_off(L, s) + oa, ns2(pk4(r0 * inv)));
        st8c(c.rs, rec_off(L, s) + ob, ns2(pk4(r1 * inv)));
        if (s + AD < NSEG) {
          st8c(c.rs, rec_off(L, s + AD) + oa, S2);
          st8c(c.rs, rec_off(L, s + AD) + ob, S2);
        }
      }
      STAMP(s, 3);
    }
    slot = (slot == 8) ? 0 : slot + 1;
  }
}

template <int l>
DEVI void role_AO(Ctx& c, int tau, int par, unsigned char* lds) {
  const int lane = c.lane, w = c.wave;
  const int i16 = lane & 15, q = lane >> 4;
  unsigned char* lO = lds;
  unsigned char* lx = lds + 14 * KB;
  unsigned char* lst = lds + 18 * KB;
  v4u W[98];
  {
    const unsigned b0 = WO_OFF + (unsigned)(3 * w) * 28 * KB + lane * 16;
#pragma unroll
    for (int f = 0; f < 84; ++f) W[f] = ld16(c.rs, b0 + f * KB);
    const unsigned b1 = WO_OFF + (unsigned)(12 + (w >> 1)) * 28 * KB + (w & 1) * 7 * KB + lane * 16;
#pragma unroll
    for (int k = 0; k < 7; ++k) { W[84 + k] = ld16(c.rs, b1 + k * KB); W[91 + k] = ld16(c.rs, b1 + (14 + k) * KB); }
#pragma unroll
    for (int f = 0; f < 56; ++f) PIN_A(W[f]);
  }
  const int ux = 12 + (w >> 1), kx = 7 * (w & 1);
  const bool own4 = !(w & 1);
  const unsigned fo = (lane == 0) ? F_XR + (unsigned)((l * 2 + tau) * 2 + par) * 64 + w * 4 : OOB;
  for (int s = par; s < NSEG; s += 2) {
    const unsigned rb = rec_off(l, s);
    STAMP(s, 0);
    {
      const unsigned ob = rb + R_OA + (unsigned)tau * 14 * KB;
      v4u v[4];
      auto ldo = [&]() {
        v[0] = ld16c(c.rs, ob + c.tid * 16); v[1] = ld16c(c.rs, ob + 4096 + c.tid * 16); v[2] = ld16c(c.rs, ob + 8192 + c.tid * 16);
        v[3] = ld16c(c.rs, ob + 12288 + (c.tid & 127) * 16);
      };
      CBAR();
      ldo();
      DRAIN();
      st4c(c.rs, fo, (unsigned)max(s - 1, 0));
      POLL(anysent(mx4(mx4(mx4(mx4(0u, v[0]), v[1]), v[2]), v[3])), ldo(), 1);
      LDS16(lO + c.tid * 16) = v[0];
      LDS16(lO + 4096 + c.tid * 16) = v[1];
      LDS16(lO + 8192 + c.tid * 16) = v[2];
      LDS16(lO + 12288 + (c.tid & 127) * 16) = v[3];
    }
    STAMP(s, 1);
    v4u xr[4];
    auto ldr = [&]() {
#pragma unroll
      for (int p = 0; p < 4; ++p) {
        const int u = (p < 3) ? 3 * w + p : ux;
        if (l == 0) xr[p] = ld16(c.rs, X0_OFF + (unsigned)s * SEGX + (unsigned)(tau * 14 + u) * KB + lane * 16);
        else xr[p] = ld16c(c.rs, rec_off(0, s) + R_X2 + (unsigned)(tau * 14 + u) * KB + lane * 16);
      }
    };
    BAR();
    f4 acc[8] = {Z4, Z4, Z4, Z4, Z4, Z4, Z4, Z4};
    {
      const unsigned char* xp = lO + lane * 16;
      v4u xs[7];
#pragma unroll
      for (int k = 0; k < 7; ++k) xs[k] = LDS16(xp + (kx + k) * KB);
      v4u y0 = LDS16(xp), y1 = LDS16(xp + KB);
      SB();
#pragma unroll
      for (int k = 0; k < 7; ++k) {
        acc[6] = mfma32(W[84 + k], xs[k], acc[6]);
        acc[7] = mfma32(W[91 + k], xs[k], acc[7]);
      }
      SB();
      ldr();
      SB();
#pragma unroll
      for (int ks = 0; ks < 14; ++ks) {
        const v4u x = y0;
        y0 = y1;
        if (ks + 2 < 14) y1 = LDS16(xp + (ks + 2) * KB);
        if (ks == 2 && !own4) { LDSF4(lx + ((w >> 1) * 2 + 0) * KB + lane * 16) = acc[6]; LDSF4(lx + ((w >> 1) * 2 + 1) * KB + lane * 16) = acc[7]; }
#pragma unroll
        for (int pp = 0; pp < 3; ++pp) {
          acc[pp * 2 + 0] = mfma32(W[pp * 28 + ks], x, acc[pp * 2 + 0]);
          acc[pp * 2 + 1] = mfma32(W[pp * 28 + 14 + ks], x, acc[pp * 2 + 1]);
        }
        SB();
      }
    }
    if (l == 1) POLL(anysent(mx4(mx4(mx4(mx4(0u, xr[0]), xr[1]), xr[2]), xr[3])), ldr(), 1);
    BAR();
    STAMP(s, 2);
    v4u xb[4];
    if (own4) {
      acc[6] += LDSF4(lx + ((w >> 1) * 2 + 0) * KB + lane * 16);
      acc[7] += LDSF4(lx + ((w >> 1) * 2 + 1) * KB + lane * 16);
    }
    float sum = 0.f, sq = 0.f;
#pragma unroll
    for (int p = 0; p < 4; ++p) {
      xb[p] = resid8(xr[p], acc[2 * p], acc[2 * p + 1]);
      if (p == 3 && !own4) xb[p] = v4u{0u, 0u, 0u, 0u};
#pragma unroll
      for (int e = 0; e < 4; ++e) { sum = dot2(xb[p][e], 0x3F803F80u, sum); sq = dot2(xb[p][e], xb[p][e], sq); }
    }
    sum = rsum4(sum);
    sq = rsum4(sq);
    if (q == 0) *(f2*)(lst + (w * 16 + i16) * 8) = f2{sum, sq};
    BAR();
    const f2 st = (*(const f2*)(lst + i16 * 8) + *(const f2*)(lst + (16 + i16) * 8)) + (*(const f2*)(lst + (32 + i16) * 8) + *(const f2*)(lst + (48 + i16) * 8));
    const float mean = st[0] * (1.0f / 448.0f);
    const float rstd = rsq(fmaxf(st[1] * (1.0f / 448.0f) - mean * mean, 0.f) + 1e-5f);
    const float mr = mean * rstd;
#pragma unroll
    for (int p = 0; p < 4; ++p)
      if (p < 3 || own4) {
        const int u = (p < 3) ? 3 * w + p : ux;
        const unsigned fo = (unsigned)(tau * 14 + u) * KB + lane * 16;
        v4u o;
#pragma unroll
        for (int e = 0; e < 4; ++e) o[e] = pk2(bflo(xb[p][e]) * rstd - mr, bfhi(xb[p][e]) * rstd - mr);
        st16c(c.rs, rb + R_XN + fo, ns4(o));
      }
#pragma unroll
    for (int p = 0; p < 4; ++p)
      if (p < 3 || own4) {
        const int u = (p < 3) ? 3 * w + p : ux;
        const unsigned fo = (unsigned)(tau * 14 + u) * KB + lane * 16;
        st16c(c.rs, rb + R_XR + fo, xb[p]);
        st16c(c.rs, rec_in(l, s + AD) + R_XN + fo, S4);
      }
    STAMP(s, 3);
  }
  DRAIN();
  st4c(c.rs, fo, (unsigned)NSEG);
}

DEVI unsigned ring_offset(const unsigned char* ws) {
  const unsigned lo = (unsigned)((size_t)ws & (2u * MB - 1u));
  return ((lo + FLAG_BYTES + 2u * MB - 1u) & ~(2u * MB - 1u)) - lo;
}

__global__ void __launch_bounds__(256, 1) mega_kernel(unsigned char* ws, const unsigned char* mem0, __hip_bfloat16* out) {
  extern __shared__ __attribute__((aligned(16))) unsigned char lds[];
  Ctx c;
  c.ws = ws;
  c.rs = __builtin_amdgcn_make_buffer_rsrc(ws, 0, WS_END, 0x00020000);
  c.ring = ring_offset(ws);
  c.tid = threadIdx.x;
  c.lane = threadIdx.x & 63;
  c.wave = __builtin_amdgcn_readfirstlane(threadIdx.x >> 6);
  c.dead = false;
  const int b = blockIdx.x, x = b & 7, r = b >> 3;
  STAMP(127, 0);
  if (x == 0) {
    role_H(c, r >> 1, r & 1, lds);
  } else if (x < 3) {
    if (r < 28) role_CV(c, r, x - 1, lds, mem0);
  } else if (x < 5) {
    if (r & 1) role_GX<1>(c, r >> 1, x - 3, lds); else role_GX<0>(c, r >> 1, x - 3, lds);
  } else if (x == 5) {
    if (r < 14) role_O<0>(c, r / 7, r % 7, lds, out); else if (r < 28) role_O<1>(c, (r - 14) / 7, (r - 14) % 7, lds, out);
  } else if (x == 6) {
    if (r >> 4) role_AH<1>(c, r & 15, lds); else role_AH<0>(c, r & 15, lds);
  } else {
    if (r < 4) role_AO<0>(c, r & 1, r >> 1, lds); else if (r < 8) role_AO<1>(c, r & 1, (r >> 1) & 1, lds);
  }
}

#define PAUX 16
#define PST(off, v) __builtin_amdgcn_raw_buffer_store_b128(v, rs, off, 0, PAUX)
__global__ void __launch_bounds__(512) prologue_kernel(unsigned char* ws, const unsigned short* x0, const unsigned short* wqkv, const unsigned short* wo,
                                                       const unsigned short* mem0, const unsigned short* Wq, const unsigned short* Wk,
                                                       const unsigned short* Wv, const unsigned short* Wo) {
  extern __shared__ __attribute__((aligned(16))) unsigned char lb[];
  const int b = blockIdx.x, tid = threadIdx.x, lane = tid & 63, w = __builtin_amdgcn_readfirstlane(tid >> 6);
  const int i16 = lane & 15, q = lane >> 4;
  const rsrc_t rs = __builtin_amdgcn_make_buffer_rsrc(ws, 0, WS_END, 0x00020000);
  if (b < 224) {
    const int kk = (b >> 3) / 7, cg = (b >> 3) % 7;
    const bool isG = kk < 2;
    const int h = (2 * (b & 7) + kk) & 15;
    const unsigned short* Wa = isG ? Wk : Wv;
    v4u bv[4];
    if (isG) {
#pragma unroll
      for (int k = 0; k < 4; ++k) bv[k] = *(const v4u*)(Wq + ((size_t)(64 * cg + 16 * k + (tid >> 5)) * 16 + h) * 256 + (tid & 31) * 8);
    } else {
#pragma unroll
      for (int k = 0; k < 4; ++k) bv[k] = *(const v4u*)(Wo + ((size_t)h * 256 + 4 * (tid >> 3) + k) * 448 + 64 * cg + 8 * (tid & 7));
    }
    v4u A[4][8];
    if (w < 7) {
#pragma unroll
      for (int mt = 0; mt < 4; ++mt)
#pragma unroll
        for (int k = 0; k < 8; ++k) {
          const int n = 2 * k + (lane >> 5), d = 64 * w + 32 * (mt >> 1) + 8 * (n >> 2) + 4 * (mt & 1) + (n & 3);
          A[mt][k] = *(const v4u*)(Wa + ((size_t)d * 16 + h) * 256 + (lane & 31) * 8);
        }
    }
    if (isG) {
#pragma unroll
      for (int k = 0; k < 4; ++k) LDS16(lb + (16 * k + (tid >> 5)) * 528 + (tid & 31) * 16) = bv[k];
    } else {
      unsigned char* p = lb + 8 * (tid & 7) * 528 + (tid >> 3) * 8;
#pragma unroll
      for (int k = 0; k < 4; ++k) {
        *(v2u*)(p + (2 * k) * 528) = v2u{__builtin_amdgcn_perm(bv[1][k], bv[0][k], 0x05040100u), __builtin_amdgcn_perm(bv[3][k], bv[2][k], 0x05040100u)};
        *(v2u*)(p + (2 * k + 1) * 528) = v2u{__builtin_amdgcn_perm(bv[1][k], bv[0][k], 0x07060302u), __builtin_amdgcn_perm(bv[3][k], bv[2][k], 0x07060302u)};
      }
    }
    BAR();
    if (w < 7) {
      unsigned char* la = lb + (64 + 16 * w) * 528;
      const float sc = isG ? 0.0625f : 1.0f;
#pragma unroll
      for (int kh = 0; kh < 2; ++kh) {
#pragma unroll
        for (int mt = 2 * kh; mt < 2 * kh + 2; ++mt) {
#pragma unroll
          for (int k = 0; k < 8; ++k) LDS16(la + (2 * k + (lane >> 5)) * 528 + (lane & 31) * 16) = A[mt][k];
          CBAR();
#pragma unroll
          for (int es = 0; es < 8; ++es) A[mt][es] = LDS16(la + i16 * 528 + es * 64 + q * 16);
          CBAR();
        }
        f4 a[8] = {Z4, Z4, Z4, Z4, Z4, Z4, Z4, Z4};
#pragma unroll
        for (int es = 0; es < 8; ++es)
#pragma unroll
          for (int nt = 0; nt < 4; ++nt) {
            const int row = isG ? 32 * (nt >> 1) + 8 * (i16 >> 2) + 4 * (nt & 1) + (i16 & 3) : 16 * nt + i16;
            const v4u bf = LDS16(lb + row * 528 + es * 64 + q * 16);
            a[nt * 2 + 0] = mfma32(A[2 * kh][es], bf, a[nt * 2 + 0]);
            a[nt * 2 + 1] = mfma32(A[2 * kh + 1][es], bf, a[nt * 2 + 1]);
          }
#pragma unroll
        for (int nt = 0; nt < 4; ++nt) {
          const unsigned off = isG ? WG_OFF + (unsigned)(((h * 14 + 2 * cg + (nt >> 1)) * 2 + (nt & 1)) * 14 + 2 * w + kh) * KB
                                   : WU_OFF + (unsigned)(((4 * cg + nt) * 16 + h) * 14 + 2 * w + kh) * KB;
          PST(off + lane * 16, pk8(a[nt * 2] * sc, a[nt * 2 + 1] * sc));
        }
        SB();
      }
    }
  }
  if (b >= 224 || w == 7) {
    constexpr int NW = 224 + 32 * 8;
    const int wid = (b < 224) ? b : 224 + (b - 224) * 8 + w;
    unsigned short gv[4][8];
#pragma unroll
    for (int it = 0; it < 4; ++it) {
      const int f = min(wid + it * NW, 1735), ks = f % 14;
      const unsigned short* src;
      unsigned stride;
      bool ok = true;
      if (f < 1344) {
        const int tl = (f / 14) % 6, a = f / 84;
        const int dh = (tl < 4) ? 8 * (i16 >> 2) + 4 * (tl & 1) + (i16 & 3) : 16 * (tl - 4) + i16;
        ok = dh < 28;
        src = wqkv + ((tl < 4) ? (tl >> 1) * 448 : 896) + 28 * a + min(dh, 27);
        stride = 1344;
      } else {
        const int g = f - 1344;
        src = wo + 32 * (g / 28) + 8 * (i16 >> 2) + 4 * ((g / 14) & 1) + (i16 & 3);
        stride = 448;
      }
      src += (size_t)(32 * ks + 8 * q) * stride;
#pragma unroll
      for (int jj = 0; jj < 8; ++jj) gv[it][jj] = src[(size_t)jj * stride];
      if (!ok) {
#pragma unroll
        for (int jj = 0; jj < 8; ++jj) gv[it][jj] = 0;
      }
    }
    v4u xv[4];
#pragma unroll
    for (int it = 0; it < 4; ++it) {
      const int f = min(wid + it * NW, 1791), sg = f / 28, tau = (f / 14) & 1, ks = f % 14;
      xv[it] = *(const v4u*)(x0 + (size_t)(sg * 32 + tok_of(tau, i16)) * 448 + 32 * ks + 8 * q);
    }
    const unsigned gtid = wid * 64 + lane, gstride = NW * 64;
    for (unsigned idx = gtid; idx < FLAG_BYTES / 16; idx += gstride) PST(idx * 16, (v4u{0u, 0u, 0u, 0u}));
    for (unsigned idx = gtid; idx < 2 * AD * REC / 16; idx += gstride) {
      const unsigned l = idx / (AD * REC / 16), r = idx % (AD * REC / 16);
      PST(REC_OFF + l * NSEG * REC + r * 16, S4);
    }
    const unsigned ring = ring_offset(ws);
    for (unsigned idx = gtid; idx < RING_BYTES / 16; idx += gstride) {
      const unsigned po = idx * 16, lo = ((po >> 14) << 13) | (po & 8191u);
      v4u o = S4;
      if (lo < L_MEMG && (lo & (64 * KB - 1)) < 14 * KB) {
        const unsigned r = lo & (64 * KB - 1), ln = (r >> 4) & 63, ks = r >> 10;
        o = ns4(*(const v4u*)(mem0 + (size_t)(ln & 7) * 448 + 32 * ks + 8 * (ln >> 4)));
      } else if (lo >= L_MEMG && lo < L_MEMG + 16 * KB && (lo & 8191u) < 7 * KB) {
        const unsigned r = lo & 8191u, ks = r >> 9, qq = (r >> 7) & 3, row = (r >> 4) & 7;
        o = ns4(*(const v4u*)(mem0 + (size_t)row * 448 + 32 * ks + 8 * qq));
      }
      PST(ring + po, o);
    }
#pragma unroll
    for (int it = 0; it < 4; ++it) {
      const int f = wid + it * NW;
      if (f < 1736) {
        v4u o;
#pragma unroll
        for (int jj = 0; jj < 4; ++jj) o[jj] = (unsigned)gv[it][2 * jj] | ((unsigned)gv[it][2 * jj + 1] << 16);
        PST(WQ_OFF + (unsigned)f * KB + lane * 16, o);
      }
      if (f < 1792) PST(X0_OFF + (unsigned)f * KB + lane * 16, xv[it]);
    }
  }
}

extern "C" void kernel_launch(void* const* d_in, const int* in_sizes, int n_in, void* d_out, int out_size, void* d_ws, size_t ws_size,
                              hipStream_t stream) {
  constexpr int LDS_BYTES = 100 * 1024;
  (void)hipFuncSetAttribute((const void*)mega_kernel, hipFuncAttributeMaxDynamicSharedMemorySize, LDS_BYTES);
  unsigned char* ws = (unsigned char*)d_ws;
  constexpr int PRO_LDS = (64 + 7 * 16) * 528;
  (void)hipFuncSetAttribute((const void*)prologue_kernel, hipFuncAttributeMaxDynamicSharedMemorySize, PRO_LDS);
  prologue_kernel<<<256, 512, PRO_LDS, stream>>>(ws, (const unsigned short*)d_in[0], (const unsigned short*)d_in[1], (const unsigned short*)d_in[2], (const unsigned short*)d_in[4],
                                          (const unsigned short*)d_in[5], (const unsigned short*)d_in[6], (const unsigned short*)d_in[7],
                                          (const unsigned short*)d_in[8]);
  mega_kernel<<<256, 256, LDS_BYTES, stream>>>(ws, (const unsigned char*)d_in[4], (__hip_bfloat16*)d_out);
}
```

```cpp
#include <hip/hip_runtime.h>
#include <hip/hip_bf16.h>

typedef unsigned v4u __attribute__((ext_vector_type(4)));
typedef unsigned v2u __attribute__((ext_vector_type(2)));
typedef float f4 __attribute__((ext_vector_type(4)));
typedef float f2 __attribute__((ext_vector_type(2)));
typedef short s8v __attribute__((ext_vector_type(8)));
typedef __bf16 b2v __attribute__((ext_vector_type(2)));
typedef __amdgpu_buffer_rsrc_t rsrc_t;
#define DEVI __device__ __forceinline__
#define RLX_AGENT __ATOMIC_RELAXED, __HIP_MEMORY_SCOPE_AGENT

#define LAG 6
#define GXS 1
#define PD 7
#define PP 0
#define CPF 72
#define USTREAM 1
constexpr int NSEG = 64, NT = NSEG + LAG, AD = 4;
constexpr unsigned SENT = 0xFFFFFFFFu;
constexpr unsigned SPINCAP = 1u << 20;
constexpr float LOG2E = 1.4426950408889634f;
constexpr float NEG = -3.0e38f;

constexpr unsigned KB = 1024u, MB = 1024u * 1024u;
constexpr unsigned FLAG_BYTES = 8 * KB;
constexpr unsigned F_U = 1024, F_XR = 7168;
constexpr unsigned OOB = 0x80000000u;
constexpr unsigned F_ABORT = 0, F_SIDE = 128;
constexpr unsigned RING_BYTES = 1 * MB;
constexpr unsigned L_MEMH = 0, L_MEMC = 64 * KB, L_MEMG = 128 * KB, L_S = 192 * KB, L_E = 256 * KB, L_XS = 384 * KB, L_PROBE = 504 * KB;
constexpr unsigned WG_OFF = 4 * MB;
constexpr unsigned WG_BYTES = 16 * 14 * 2 * 14 * KB;
constexpr unsigned WU_OFF = WG_OFF + WG_BYTES;
constexpr unsigned WQ_OFF = WU_OFF + WG_BYTES;
constexpr unsigned WO_OFF = WQ_OFF + 16 * 6 * 14 * KB;
constexpr unsigned SEGX = 28 * KB;
constexpr unsigned R_XN = 0, R_XR = SEGX, R_OA = 2 * SEGX, R_X2 = 3 * SEGX, R_ETT = 4 * SEGX, R_ETS = R_ETT + 32 * KB, R_STM = R_ETS + 4 * KB,
                   R_ZB = R_STM + 32 * KB, REC = R_ZB + 112 * KB;
constexpr unsigned X0_OFF = WO_OFF + 392 * KB;
constexpr unsigned REC_OFF = X0_OFF + NSEG * SEGX;
constexpr unsigned SEGG = 448 * KB;
constexpr unsigned U_OFF = REC_OFF + 2 * NSEG * REC;
constexpr unsigned WS_END = U_OFF + 2 * NSEG * SEGG;
static_assert(WS_END < 200u * MB, "workspace");

DEVI f4 mfma32(v4u a, v4u b, f4 c) {
  return __builtin_amdgcn_mfma_f32_16x16x32_bf16(__builtin_bit_cast(s8v, a), __builtin_bit_cast(s8v, b), c, 0, 0, 0);
}
DEVI unsigned pk2(float a, float b) {
  f2 v = {a, b};
  return __builtin_bit_cast(unsigned, __builtin_convertvector(v, b2v));
}
DEVI float bflo(unsigned u) { return __uint_as_float(u << 16); }
DEVI float bfhi(unsigned u) { return __uint_as_float(u & 0xFFFF0000u); }
DEVI float rbf(float a) { return bflo(pk2(a, 0.f)); }
DEVI v4u pk8(f4 a, f4 b) { return v4u{pk2(a[0], a[1]), pk2(a[2], a[3]), pk2(b[0], b[1]), pk2(b[2], b[3])}; }
DEVI v2u pk4(f4 a) { return v2u{pk2(a[0], a[1]), pk2(a[2], a[3])}; }
DEVI v4u resid8(v4u x, f4 a, f4 b) {
  const v4u y = pk8(a, b);
  return v4u{pk2(bflo(x.x) + bflo(y.x), bfhi(x.x) + bfhi(y.x)), pk2(bflo(x.y) + bflo(y.y), bfhi(x.y) + bfhi(y.y)),
             pk2(bflo(x.z) + bflo(y.z), bfhi(x.z) + bfhi(y.z)), pk2(bflo(x.w) + bflo(y.w), bfhi(x.w) + bfhi(y.w))};
}
DEVI unsigned ns(unsigned x) { return min(x, 0xFFFFFFFEu); }
DEVI v4u ns4(v4u x) { return v4u{ns(x.x), ns(x.y), ns(x.z), ns(x.w)}; }
DEVI v2u ns2(v2u x) { return v2u{ns(x.x), ns(x.y)}; }
DEVI v4u f2u(f4 x) { return v4u{__float_as_uint(x[0]), __float_as_uint(x[1]), __float_as_uint(x[2]), __float_as_uint(x[3])}; }
DEVI f4 u2f(v4u x) { return f4{__uint_as_float(x.x), __uint_as_float(x.y), __uint_as_float(x.z), __uint_as_float(x.w)}; }
DEVI unsigned mx4(unsigned m, v4u v) { return max(max(max(max(m, v.x), v.y), v.z), v.w); }
DEVI unsigned mx2(unsigned m, v2u v) { return max(max(m, v.x), v.y); }
DEVI bool anysent(unsigned m) { return __builtin_amdgcn_ballot_w64(m == SENT) != 0ull; }
DEVI float ex2(float x) { return __builtin_amdgcn_exp2f(x); }
DEVI float rcp(float x) { return __builtin_amdgcn_rcpf(x); }
DEVI float rsq(float x) { return __builtin_amdgcn_rsqf(x); }
DEVI float mx3(float a, float b, float c) { return __builtin_fmaxf(__builtin_fmaxf(a, b), c); }
DEVI float mxf4(f4 a, float m) { return mx3(a[2], a[3], mx3(a[0], a[1], m)); }
DEVI float dot2(unsigned a, unsigned b, float c) {
  return __builtin_amdgcn_fdot2_f32_bf16(__builtin_bit_cast(b2v, a), __builtin_bit_cast(b2v, b), c, false);
}
DEVI void swap16(unsigned& a, unsigned& b) { auto r = __builtin_amdgcn_permlane16_swap(a, b, false, false); a = r[0]; b = r[1]; }
DEVI void swap32(unsigned& a, unsigned& b) { auto r = __builtin_amdgcn_permlane32_swap(a, b, false, false); a = r[0]; b = r[1]; }
DEVI float rmax4(float x) {
  unsigned a = __float_as_uint(x), b = a;
  swap16(a, b);
  a = b = __float_as_uint(mx3(__uint_as_float(a), __uint_as_float(b), NEG));
  swap32(a, b);
  return mx3(__uint_as_float(a), __uint_as_float(b), NEG);
}
DEVI float rsum4(float x) {
  unsigned a = __float_as_uint(x), b = a;
  swap16(a, b);
  a = b = __float_as_uint(__uint_as_float(a) + __uint_as_float(b));
  swap32(a, b);
  return __uint_as_float(a) + __uint_as_float(b);
}
DEVI float ror8(float x) { return __uint_as_float(__builtin_amdgcn_update_dpp(0u, __float_as_uint(x), 0x128, 0xF, 0xF, false)); }
DEVI void rbcast4(float x, float* o) {
  unsigned a = __float_as_uint(x), b = a;
  swap16(a, b);
  unsigned a2 = a, b2 = b;
  swap32(a, a2);
  swap32(b, b2);
  o[0] = __uint_as_float(a); o[1] = __uint_as_float(b); o[2] = __uint_as_float(a2); o[3] = __uint_as_float(b2);
}

DEVI v4u ld16(rsrc_t r, unsigned off) { return __builtin_amdgcn_raw_buffer_load_b128(r, off, 0, 0); }
DEVI v4u ld16c(rsrc_t r, unsigned off) { return __builtin_amdgcn_raw_buffer_load_b128(r, off, 0, 16); }
DEVI v2u ld8c(rsrc_t r, unsigned off) { return __builtin_amdgcn_raw_buffer_load_b64(r, off, 0, 16); }
DEVI unsigned ld4c(rsrc_t r, unsigned off) { return __builtin_amdgcn_raw_buffer_load_b32(r, off, 0, 16); }
DEVI void st16c(rsrc_t r, unsigned off, v4u v) { __builtin_amdgcn_raw_buffer_store_b128(v, r, off, 0, 16); }
DEVI void st8c(rsrc_t r, unsigned off, v2u v) { __builtin_amdgcn_raw_buffer_store_b64(v, r, off, 0, 16); }
DEVI void st4c(rsrc_t r, unsigned off, unsigned v) { __builtin_amdgcn_raw_buffer_store_b32(v, r, off, 0, 16); }
#define PIN_A(x) asm volatile("" : "+a"(x))
#define TOUCH(x) asm volatile("" :: "v"(x))
#define PIN_V(x) asm volatile("" : "+v"(x))
#define DRAIN() asm volatile("s_waitcnt vmcnt(0)" ::: "memory")
#define CBAR() asm volatile("" ::: "memory")
#define SB() __builtin_amdgcn_sched_barrier(0)
#define BAR() do { asm volatile("s_waitcnt lgkmcnt(0)" ::: "memory"); __builtin_amdgcn_s_barrier(); asm volatile("" ::: "memory"); } while (0)
#define LDS16(p) (*(v4u*)(p))
#define LDSF4(p) (*(f4*)(p))
#define S4 (v4u{SENT, SENT, SENT, SENT})
#define S2 (v2u{SENT, SENT})
#define Z4 (f4{0.f, 0.f, 0.f, 0.f})

#define STAMP(it, e) do { } while (0)
#define STAMPC(it, e) do { } while (0)
struct Ctx {
  unsigned char* ws;
  rsrc_t rs;
  unsigned ring;
  int tid, lane, wave;
  bool dead;
};
DEVI bool give_up(Ctx& c, unsigned& spins) {
  if (c.dead) return true;
  ++spins;
  bool d = false;
  if (spins > SPINCAP) { __hip_atomic_store((unsigned*)(c.ws + F_ABORT), 1u, RLX_AGENT); d = true; }
  else if ((spins & 255u) == 0u) d = __builtin_amdgcn_readfirstlane(__hip_atomic_load((unsigned*)(c.ws + F_ABORT), RLX_AGENT)) != 0u;
  c.dead = d;
  return d;
}
#define POLL2(FIRST, BADEXPR, RELOAD, SLEEP) do { if (FIRST) { unsigned spins_ = 0; do { if (give_up(c, spins_)) break; if (SLEEP) __builtin_amdgcn_s_sleep(SLEEP); CBAR(); RELOAD; } while (BADEXPR); } } while (0)
#define POLL(BADEXPR, RELOAD, SLEEP) do { unsigned spins_ = 0; while (BADEXPR) { if (give_up(c, spins_)) break; if (SLEEP) __builtin_amdgcn_s_sleep(SLEEP); CBAR(); RELOAD; } } while (0)

template <class LD>
DEVI void poll2(Ctx& c, v4u* r, LD ld) {
  v4u a[4] = {r[0], r[1], r[2], r[3]}, b[4];
  unsigned spins = 0;
  __builtin_amdgcn_s_sleep(PD);
  while (true) {
    CBAR();
    ld(b);
    SB();
    if (!anysent(mx4(mx4(mx4(mx4(0u, a[0]), a[1]), a[2]), a[3])) || give_up(c, spins)) {
#pragma unroll
      for (int k = 0; k < 4; ++k) r[k] = a[k];
      break;
    }
    CBAR();
    ld(a);
    SB();
    if (!anysent(mx4(mx4(mx4(mx4(0u, b[0]), b[1]), b[2]), b[3]))) {
#pragma unroll
      for (int k = 0; k < 4; ++k) r[k] = b[k];
      break;
    }
  }
}

DEVI unsigned rmap(const Ctx& c, unsigned lo, unsigned side) { return c.ring + ((lo >> 13) << 14) + (side << 13) + (lo & 8191u); }
DEVI unsigned rec_off(int l, int s) { return REC_OFF + (unsigned)(l * NSEG + s) * REC; }
DEVI unsigned rec_in(int l, int s) { return (s >= 0 && s < NSEG) ? rec_off(l, s) : OOB; }
DEVI unsigned u_in(int l, int s) { return (s >= 0 && s < NSEG) ? U_OFF + (unsigned)(l * NSEG + s) * SEGG : OOB; }
DEVI int tok_of(int tau, int m) { return 8 * (m >> 2) + 4 * tau + (m & 3); }

DEVI void probe_side(Ctx& c, int k) {
  if (c.wave != 0) return;
  unsigned best[2] = {0xFFFFFFFFu, 0xFFFFFFFFu};
#pragma unroll 1
  for (int it = 0; it < 4; ++it) {
#pragma unroll
    for (int sd = 0; sd < 2; ++sd) {
      DRAIN();
      const unsigned t0 = (unsigned)__builtin_amdgcn_s_memtime();
      if (c.lane < 8) st16c(c.rs, rmap(c, L_PROBE + k * 256 + c.lane * 16, sd), S4);
      DRAIN();
      const unsigned t1 = (unsigned)__builtin_amdgcn_s_memtime();
      best[sd] = min(best[sd], t1 - t0);
    }
  }
  if (c.lane == 0) st4c(c.rs, F_SIDE + k * 64, best[1] < best[0] ? 2u : 1u);
}
DEVI unsigned get_sides(Ctx& c) {
  const unsigned o = F_SIDE + (c.lane & 7) * 64;
  unsigned v = ld4c(c.rs, o);
  POLL(__builtin_amdgcn_ballot_w64(v == 0u) != 0ull, v = ld4c(c.rs, o), 2);
  return (__builtin_popcountll(__builtin_amdgcn_ballot_w64(v == 2u)) > 32) ? 0x10u : 0x0Fu;
}

DEVI void role_H(Ctx& c, int h, int a, unsigned char* lds) {
  const int lane = c.lane, w = c.wave;
  const int i16 = lane & 15, q = lane >> 4;
  unsigned char* lmem = lds;
  unsigned char* lred = lds + 8 * KB;
  unsigned char* ltb = lds + 24 * KB;
  const int odd = w & 1;
  const int ks0 = (w >> 1) * 7 + odd * 4, ksx = (w >> 1) * 7 + 3;
  v4u W[42], Wx[8];
#pragma unroll
  for (int lp = 0; lp < 7; ++lp) {
    const int gp = odd ? 6 - lp : lp;
    const unsigned b0 = WG_OFF + (unsigned)(h * 14 + 7 * a + gp) * 28 * KB + lane * 16;
#pragma unroll
    for (int t = 0; t < 2; ++t)
#pragma unroll
      for (int k = 0; k < 3; ++k) W[(lp * 2 + t) * 3 + k] = ld16(c.rs, b0 + (t * 14 + ks0 + k) * KB);
    if (lp < 4) {
#pragma unroll
      for (int t = 0; t < 2; ++t) {
        Wx[lp * 2 + t] = ld16(c.rs, b0 + (t * 14 + ksx) * KB);
        if (lp == 3 && t != odd) Wx[lp * 2 + t] = v4u{0u, 0u, 0u, 0u};
      }
    }
  }
#pragma unroll
  for (int f = 0; f < 42; ++f) PIN_A(W[f]);
#pragma unroll
  for (int f = 0; f < 8; ++f) PIN_A(Wx[f]);
  const unsigned sides = get_sides(c);
  unsigned mo[4];
#pragma unroll
  for (int k = 0; k < 4; ++k) mo[k] = rmap(c, L_MEMH + (k < 3 ? ks0 + k : ksx) * KB + lane * 16, sides & 1);
  const unsigned so0 = rmap(c, L_S + ((a * 2 + (q & 1)) * 16 + h) * 256 + i16 * 16, 0);
  const unsigned sd0 = ((sides >> 1) & 1) << 13, sd1 = ((sides >> 2) & 1) << 13;
  const bool sval = (q >> 1) == (i16 >> 3);
  const bool stager = (w >> 1) == a;
  const int gp1 = (w < 3) ? w + 4 : 3;
  v4u X[8];
  auto load_x = [&](int t) {
#pragma unroll
    for (int l = 0; l < 2; ++l) {
      const unsigned xb = rec_in(l, t - l * LAG) + R_XN + (unsigned)(7 * a) * KB + lane * 16;
#pragma unroll
      for (int tau = 0; tau < 2; ++tau) {
        X[(l * 2 + tau) * 2 + 0] = ld16c(c.rs, xb + (tau * 14 + w) * KB);
        X[(l * 2 + tau) * 2 + 1] = ld16c(c.rs, xb + (tau * 14 + gp1) * KB);
      }
    }
  };
  CBAR();
  load_x(0);
  for (int t = 0; t < NT; ++t) {
    const unsigned slot = (unsigned)(t & 3) * 32 * KB;
    STAMP(t, 0);
    v4u m[4];
    CBAR();
#pragma unroll
    for (int k = 0; k < 4; ++k) m[k] = ld16c(c.rs, mo[k] + slot);
    POLL(anysent(mx4(mx4(mx4(mx4(0u, m[0]), m[1]), m[2]), m[3])),
         { _Pragma("unroll") for (int k = 0; k < 4; ++k) m[k] = ld16c(c.rs, mo[k] + slot); }, 0);
    STAMP(t, 1);
    if (stager) {
#pragma unroll
      for (int k = 0; k < 4; ++k) LDS16(lmem + ((k < 3 ? ks0 + k : ksx) - 7 * a) * KB + lane * 16) = m[k];
    }
    SB();
    v4u tb[7], mu[7];
    f4 acc[7][2];
    f4 sacc = Z4;
#pragma unroll
    for (int lp = 0; lp < 7; ++lp) {
#define HPK(e) do { if (lp >= 2) { SB(); tb[lp - 2][e] = pk2(acc[lp - 2][(e) >> 1][((e) & 1) * 2], acc[lp - 2][(e) >> 1][((e) & 1) * 2 + 1]); SB(); } } while (0)
      acc[lp][0] = mfma32(W[(lp * 2 + 0) * 3 + 0], m[0], Z4);
      HPK(0);
      acc[lp][1] = mfma32(W[(lp * 2 + 1) * 3 + 0], m[0], Z4);
      HPK(1);
      acc[lp][0] = mfma32(W[(lp * 2 + 0) * 3 + 1], m[1], acc[lp][0]);
      HPK(2);
      acc[lp][1] = mfma32(W[(lp * 2 + 1) * 3 + 1], m[1], acc[lp][1]);
      HPK(3);
      acc[lp][0] = mfma32(W[(lp * 2 + 0) * 3 + 2], m[2], acc[lp][0]);
      if (lp == 6) { SB(); tb[5][0] = pk2(acc[5][0][0], acc[5][0][1]); SB(); }
      acc[lp][1] = mfma32(W[(lp * 2 + 1) * 3 + 2], m[2], acc[lp][1]);
      if (lp == 6) { SB(); tb[5][1] = pk2(acc[5][0][2], acc[5][0][3]); SB(); }
      if (lp < 4) {
        acc[lp][0] = mfma32(Wx[lp * 2 + 0], m[3], acc[lp][0]);
        acc[lp][1] = mfma32(Wx[lp * 2 + 1], m[3], acc[lp][1]);
      }
      if (lp >= 2) sacc = mfma32(tb[lp - 2], mu[lp - 2], sacc);
      SB();
      if (lp == 0) {
        BAR();
#pragma unroll
        for (int k = 0; k < 7; ++k) mu[k] = LDS16(lmem + (odd ? 6 - k : k) * KB + lane * 16);
        SB();
      }
    }
    tb[5][2] = pk2(acc[5][1][0], acc[5][1][1]);
    tb[5][3] = pk2(acc[5][1][2], acc[5][1][3]);
    sacc = mfma32(tb[5], mu[5], sacc);
    SB();
    tb[6] = pk8(acc[6][0], acc[6][1]);
    sacc = mfma32(tb[6], mu[6], sacc);
    if (w != 0) LDSF4(lred + w * KB + lane * 16) = sacc;
    BAR();
    if (w == 0) {
      const f4 s = (sacc + LDSF4(lred + KB + lane * 16)) + (LDSF4(lred + 2 * KB + lane * 16) + LDSF4(lred + 3 * KB + lane * 16));
      if (sval) {
        st16c(c.rs, so0 + slot + ((t & 1) ? sd1 : sd0), ns4(f2u(s)));
        if (t >= 1) st16c(c.rs, so0 + (unsigned)((t - 1) & 3) * 32 * KB + ((t & 1) ? sd0 : sd1), S4);
      }
    }
    STAMP(t, 2);
#pragma unroll
    for (int lp = 0; lp < 7; ++lp) LDS16(ltb + (w * 7 + (odd ? 6 - lp : lp)) * KB + lane * 16) = tb[lp];
    BAR();
    v4u tp[8];
#pragma unroll
    for (int ww = 0; ww < 4; ++ww) {
      tp[ww * 2 + 0] = LDS16(ltb + (ww * 7 + w) * KB + lane * 16);
      tp[ww * 2 + 1] = LDS16(ltb + (ww * 7 + gp1) * KB + lane * 16);
      if (w == 3) tp[ww * 2 + 1] = v4u{0u, 0u, 0u, 0u};
    }
    POLL(anysent(mx4(mx4(mx4(mx4(mx4(mx4(mx4(mx4(0u, X[0]), X[1]), X[2]), X[3]), X[4]), X[5]), X[6]), X[7])), load_x(t), 1);
    SB();
    {
      f4 s[4] = {Z4, Z4, Z4, Z4};
#pragma unroll
      for (int k = 0; k < 8; ++k)
#pragma unroll
        for (int lt = 0; lt < 4; ++lt) s[lt] = mfma32(tp[k], X[lt * 2 + (k & 1)], s[lt]);
#pragma unroll
      for (int lt = 0; lt < 4; ++lt) LDSF4(lred + (lt * 4 + w) * KB + lane * 16) = s[lt];
    }
    BAR();
    {
      const int l = w >> 1, tau = w & 1, s = t - l * LAG;
      const f4 v = (LDSF4(lred + (w * 4 + 0) * KB + lane * 16) + LDSF4(lred + (w * 4 + 1) * KB + lane * 16)) +
                   (LDSF4(lred + (w * 4 + 2) * KB + lane * 16) + LDSF4(lred + (w * 4 + 3) * KB + lane * 16));
      if ((q >> 1) == l) {
        const unsigned o = R_STM + (unsigned)((((a * 2 + (q & 1)) * 2 + tau) * 16 + h) * 256 + i16 * 16);
        st16c(c.rs, rec_in(l, s) + o, ns4(f2u(v)));
        if (s >= 0) st16c(c.rs, rec_in(l, s + AD) + o, S4);
      }
    }
    CBAR();
    load_x(t + 1);
    STAMP(t, 3);
  }
}

DEVI void role_CV(Ctx& c, int j, int p, unsigned char* lds, const unsigned char* mem0) {
  const int lane = c.lane, w = c.wave;
  const int i16 = lane & 15, q = lane >> 4;
  unsigned char* lmem = lds;
  unsigned char* lred = lds + 16 * KB;
  unsigned char* lXb = lds + 20 * KB;
  v4u W[56];
  {
    const unsigned b0 = WU_OFF + (unsigned)(j * 16 + 4 * w) * 14 * KB + lane * 16;
#pragma unroll
    for (int f = 0; f < 56; ++f) W[f] = ld16(c.rs, b0 + f * KB);
#pragma unroll
    for (int f = 0; f < 56; ++f) PIN_A(W[f]);
  }
  const v2u m0 = ns2(*(const v2u*)(mem0 + (size_t)(i16 & 7) * 896 + (j * 16 + 4 * q) * 2));
  const unsigned sides = get_sides(c);
  const unsigned sC = (sides >> (1 + p)) & 1;
  unsigned mo[4], so[4], eo[4];
#pragma unroll
  for (int k = 0; k < 4; ++k) {
    mo[k] = rmap(c, L_MEMC + (k < 3 ? k * 4096 + c.tid * 16 : 12288 + (c.tid & 127) * 16), sC);
    so[k] = rmap(c, L_S + (k * 16 + 4 * w) * 256 + lane * 16, sC);
    eo[k] = rmap(c, L_E + (4 * w + k) * KB + lane * 16, sC);
  }
  const unsigned xo = rmap(c, L_XS + ((4 * w + q) * 2 + (i16 >> 3)) * 128 + (i16 & 7) * 8, sC);
  const unsigned pf = (unsigned)(j >> 1) * KB + (unsigned)((2 * (j & 1) + (q >> 1)) * 16 + i16) * 16 + (q & 1) * 8;
  const unsigned pg = (unsigned)(i16 >> 3) * 8 * KB + (unsigned)(j >> 1) * 512 + (unsigned)(2 * (j & 1) + (q >> 1)) * 128 + (i16 & 7) * 16 + (q & 1) * 8;
  const unsigned pH = rmap(c, L_MEMH + pf, sides & 1), pC = rmap(c, L_MEMC + pf, (sides >> (2 - p)) & 1), pG = rmap(c, L_MEMG + pg, (sides >> (4 - p)) & 1);
  const unsigned pR = (w == 1) ? pH : (w == 2) ? pG : (w == 3) ? pC : OOB;
  const unsigned zo = R_ZB + (unsigned)(j * 4 + w) * KB + lane * 16;
  const unsigned uo = (unsigned)(j * 16 + 4 * w) * KB + lane * 16;
  const unsigned fo = (lane == 0) ? F_U + (unsigned)(p * 28 + j) * 64 + w * 4 : OOB;
#define XBAD anysent(mx4(mx4(mx4(mx4(mx4(mx4(mx4(0u, x[0]), x[1]), x[2]), x[3]), x[4]), x[5]), x[6]))
  v4u U[8], x[7];
  auto load_x = [&](int l, int s) {
    const unsigned xb = rec_in(l, s) + R_XN + c.tid * 16;
#pragma unroll
    for (int f = 0; f < 7; ++f) x[f] = ld16c(c.rs, xb + f * 4096);
  };
  auto stage_x = [&](int l) {
#pragma unroll
    for (int f = 0; f < 7; ++f) LDS16(lXb + l * 28 * KB + f * 4096 + c.tid * 16) = x[f];
  };
  unsigned* lhave = (unsigned*)(lds + 15 * KB);
  bool have = false, allhave = false;
  for (int t = p; t < NT; t += 2) {
    const unsigned slot = (unsigned)(t & 3) * 32 * KB, eslot = (unsigned)(t & 7) * 32 * KB, xslot = (unsigned)(t & 7) * 16 * KB;
    const int s0 = t, s1 = t - LAG;
    STAMP(t, 0);
    if (!allhave) {
      if (!have) {
        CBAR();
        load_x(0, s0);
        POLL(XBAD, load_x(0, s0), 1);
        stage_x(0);
      }
      BAR();
    }
    CBAR();
    load_x(1, s1);
    SB();
    f4 acc[8];
    const unsigned ub0 = u_in(0, s0) + uo, za0 = rec_in(0, s0 + AD) + zo, xb0 = rec_in(0, s0 + 2) + R_XN + c.tid * 16;
    auto side_op = [&](int k) {
      if (k < 4) st16c(c.rs, ub0 + k * KB, U[k]);
      else if (k == 4) st16c(c.rs, za0, S4);
      else if (k < 12) x[k - 5] = ld16c(c.rs, xb0 + (k - 5) * 4096);
    };
#pragma unroll
    for (int l = 0; l < 2; ++l) {
      const int s = l ? s1 : s0;
      const bool act = (s >= 0 && s < NSEG);
#pragma unroll
      for (int k = 0; k < 8; ++k) acc[k] = Z4;
      if (act) {
        const unsigned char* xp = lXb + l * 28 * KB + lane * 16;
        v4u xa = LDS16(xp), xb = LDS16(xp + 14 * KB), ya = LDS16(xp + KB), yb = LDS16(xp + 15 * KB);
        SB();
#pragma unroll
        for (int ks = 0; ks < 14; ++ks) {
          const v4u x0 = xa, x1 = xb;
          xa = ya; xb = yb;
          if (ks + 2 < 14) { ya = LDS16(xp + (ks + 2) * KB); yb = LDS16(xp + (16 + ks) * KB); }
#pragma unroll
          for (int hh = 0; hh < 4; ++hh) {
            acc[hh * 2 + 0] = mfma32(x0, W[hh * 14 + ks], acc[hh * 2 + 0]);
            acc[hh * 2 + 1] = mfma32(x1, W[hh * 14 + ks], acc[hh * 2 + 1]);
            if (l == 1 && hh == 1) { SB(); side_op(ks); SB(); }
          }
          SB();
        }
      }
      else if (l == 1) {
#pragma unroll
        for (int k = 0; k < 12; ++k) side_op(k);
      }
      if (l == 0) {
        const bool bad = XBAD;
#pragma unroll
        for (int f = 0; f < 7; ++f) {
          LDS16(lXb + 28 * KB + f * 4096 + c.tid * 16) = x[f];
          if (f < 4) { U[f] = pk8(acc[f * 2], acc[f * 2 + 1]); PIN_V(U[f]); }
          SB();
        }
        if (bad) {
          POLL2(true, XBAD, load_x(1, s1), 1);
          stage_x(1);
        }
        CBAR();
        BAR();
        SB();
      }
    }
    STAMP(t, 1);
    v4u m[4];
    CBAR();
#pragma unroll
    for (int k = 0; k < 4; ++k) m[k] = ld16c(c.rs, mo[k] + slot);
    SB();
#pragma unroll
    for (int hh = 0; hh < 4; ++hh) { U[4 + hh] = pk8(acc[hh * 2], acc[hh * 2 + 1]); PIN_V(U[4 + hh]); }
    SB();
    have = !XBAD;
    if (lane == 0) lhave[w] = have ? 1u : 0u;
    SB();
    POLL(anysent(mx4(mx4(mx4(mx4(0u, m[0]), m[1]), m[2]), m[3])),
         { _Pragma("unroll") for (int k = 0; k < 4; ++k) m[k] = ld16c(c.rs, mo[k] + slot); }, 0);
#pragma unroll
    for (int k = 0; k < 3; ++k) LDS16(lmem + k * 4096 + c.tid * 16) = m[k];
    LDS16(lmem + 12288 + (c.tid & 127) * 16) = m[3];
    DRAIN();
    st4c(c.rs, fo, (unsigned)min(s0 + 1, NSEG));
    {
      const unsigned ub = u_in(1, s1) + uo;
#pragma unroll
      for (int hh = 0; hh < 4; ++hh) st16c(c.rs, ub + hh * KB, U[4 + hh]);
      st16c(c.rs, (s1 >= 0 ? rec_in(1, s1 + AD) : OOB) + zo, S4);
    }
    STAMP(t, 2);
    BAR();
    {
      const v4u hv = LDS16((unsigned char*)lhave);
      allhave = __builtin_amdgcn_readfirstlane(hv.x & hv.y & hv.z & hv.w) != 0u;
    }
    if (t >= 1) st8c(c.rs, pR + (unsigned)((t - 1) & 3) * 32 * KB, S2);
    v4u zA[2];
    {
      v4u mf[14];
#pragma unroll
      for (int ks = 0; ks < 14; ++ks) mf[ks] = LDS16(lmem + ks * KB + lane * 16);
      SB();
      f4 z[4] = {Z4, Z4, Z4, Z4};
#pragma unroll
      for (int ks = 0; ks < 14; ++ks) {
#pragma unroll
        for (int hh = 0; hh < 4; ++hh) z[hh] = mfma32(mf[ks], W[hh * 14 + ks], z[hh]);
        SB();
      }
      unsigned zl[4], zh[4];
#pragma unroll
      for (int hh = 0; hh < 4; ++hh) { zl[hh] = pk2(z[hh][0], z[hh][1]); zh[hh] = pk2(z[hh][2], z[hh][3]); }
      swap16(zl[0], zl[1]); swap16(zl[2], zl[3]); swap32(zl[0], zl[2]); swap32(zl[1], zl[3]);
      swap16(zh[0], zh[1]); swap16(zh[2], zh[3]); swap32(zh[0], zh[2]); swap32(zh[1], zh[3]);
      zA[0] = v4u{zl[0], zh[0], zl[1], zh[1]};
      zA[1] = v4u{zl[2], zh[2], zl[3], zh[3]};
    }
    STAMP(t, 3);
    if (t == NT - 1) {
      st16c(c.rs, rec_in(1, s1) + zo, ns4(zA[1]));
      DRAIN();
      st4c(c.rs, fo + 16, (unsigned)NSEG);
      STAMP(t, 6);
      break;
    }
    v4u E[4], sv[4];
    v2u xs;
    auto load_e = [&]() {
#pragma unroll
      for (int hh = 0; hh < 4; ++hh) E[hh] = ld16c(c.rs, eo[hh] + eslot);
      xs = ld8c(c.rs, xo + xslot);
    };
    auto load_s = [&]() {
#pragma unroll
      for (int k = 0; k < 4; ++k) sv[k] = ld16c(c.rs, so[k] + slot);
    };
    CBAR();
    load_e();
    SB();
    stage_x(0);
    SB();
    load_s();
    POLL(anysent(mx2(mx4(mx4(mx4(mx4(0u, E[0]), E[1]), E[2]), E[3]), xs)), { load_e(); load_s(); }, 0);
    SB();
    f4 ox[4];
#pragma unroll
    for (int hh = 0; hh < 4; ++hh) {
      const f4 o0 = mfma32(U[hh], E[hh], Z4), o1 = mfma32(U[4 + hh], E[hh], Z4);
      ox[hh] = (i16 < 8) ? o0 : o1;
    }
#pragma unroll
    for (int hh = 0; hh < 4; ++hh) PIN_V(ox[hh]);
    STAMP(t, 4);
    SB();
    POLL(anysent(mx4(mx4(mx4(mx4(0u, sv[0]), sv[1]), sv[2]), sv[3])), load_s(), 0);
    STAMP(t, 5);
    DRAIN();
    st4c(c.rs, fo + 16, (unsigned)max(s1 + 1, 0));
    {
      f4 a = u2f(sv[0]) + u2f(sv[2]), b = u2f(sv[1]) + u2f(sv[3]);
      const float mxx = __uint_as_float(xs.x), lx = __uint_as_float(xs.y);
      const float mm = mxf4(b, mxf4(a, mxx));
      const float ml = mm * LOG2E;
#pragma unroll
      for (int r = 0; r < 4; ++r) { a[r] = ex2(a[r] * LOG2E - ml); b[r] = ex2(b[r] * LOG2E - ml); }
      const float ax = ex2(mxx * LOG2E - ml);
      const float den = ((a[0] + a[1]) + (a[2] + a[3])) + ((b[0] + b[1]) + (b[2] + b[3])) + ax * lx;
      const float inv = rcp(den);
      const v4u P = pk8(a * inv, b * inv);
      const f4 d0 = mfma32(zA[0], P, Z4), d1 = mfma32(zA[1], P, Z4);
      float wx[4];
      rbcast4(ax * inv, wx);
      f4 acc = (ox[0] * wx[0] + ox[1] * wx[1]) + (ox[2] * wx[2] + ox[3] * wx[3]);
      acc += (i16 < 8) ? d0 : d1;
      if (w != 0) LDSF4(lred + w * KB + lane * 16) = acc;
      BAR();
      if (w == 0) {
        acc = (acc + LDSF4(lred + KB + lane * 16)) + (LDSF4(lred + 2 * KB + lane * 16) + LDSF4(lred + 3 * KB + lane * 16));
        v2u o = ns2(pk4(acc));
        if (i16 >= 8 && t + 1 <= LAG) o = m0;
        const unsigned nslot = (unsigned)((t + 1) & 3) * 32 * KB;
        st8c(c.rs, pH + nslot, o);
        st8c(c.rs, pG + nslot, o);
        st8c(c.rs, pC + nslot, o);
      }
      st16c(c.rs, rec_in(0, s0) + zo, ns4(zA[0]));
      st16c(c.rs, rec_in(1, s1) + zo, ns4(zA[1]));
    }
    STAMP(t, 6);
  }
}

template <int L>
DEVI void role_GX(Ctx& c, int h, int p, unsigned char* lds) {
  asm volatile("; layer %0" :: "n"(L) : "memory");
  const int lane = c.lane, w = c.wave;
  const int i16 = lane & 15, q = lane >> 4;
  unsigned char* lX = lds;
  unsigned char* lG = lds + 28 * KB;
  unsigned char* lred = lds + 60 * KB;
  v4u W[98];
  {
    const unsigned b0 = WG_OFF + (unsigned)(h * 14 + 3 * w) * 28 * KB + lane * 16;
#pragma unroll
    for (int f = 0; f < 84; ++f) W[f] = ld16(c.rs, b0 + f * KB);
    const unsigned b1 = WG_OFF + (unsigned)(h * 14 + 12 + (w >> 1)) * 28 * KB + (w & 1) * 7 * KB + lane * 16;
#pragma unroll
    for (int k = 0; k < 7; ++k) { W[84 + k] = ld16(c.rs, b1 + k * KB); W[91 + k] = ld16(c.rs, b1 + (14 + k) * KB); }
#pragma unroll
    for (int f = 0; f < 56; ++f) PIN_A(W[f]);
  }
  const unsigned sides = get_sides(c);
  const unsigned sC = (sides >> (1 + p)) & 1;
  const int kx = 7 * (w & 1);
  unsigned mo[4];
#pragma unroll
  for (int k = 0; k < 4; ++k) {
    const int vu = 4 * w + k, ks = vu < 12 ? vu : 12 + ((vu - 12) >> 1);
    mo[k] = rmap(c, L_MEMG + L * 8 * KB + ks * 512 + q * 128 + (i16 & 7) * 16, (sides >> (3 + p)) & 1);
  }
  const unsigned eo = rmap(c, L_E + h * KB + (q * 16 + L * 8 + (i16 & 7)) * 16 + (i16 >> 3) * 8, sC);
  const unsigned xo = rmap(c, L_XS + (h * 2 + L) * 128 + (i16 & 7) * 8, sC);
  v4u x[7];
  auto load_x = [&](int s) {
    const unsigned xb = rec_in(L, s) + R_XN + c.tid * 16;
#pragma unroll
    for (int f = 0; f < 7; ++f) x[f] = ld16c(c.rs, xb + f * 4096);
  };
  bool have = false;
  for (int t = p; t < NT; t += 2) {
    const int s = t - L * LAG;
    const bool act = (s >= 0 && s < NSEG);
    v4u g[8];
    STAMP(t, 0);
    if (act) {
      if (!have) {
        CBAR();
        load_x(s);
        POLL(anysent(mx4(mx4(mx4(mx4(mx4(mx4(mx4(0u, x[0]), x[1]), x[2]), x[3]), x[4]), x[5]), x[6])), load_x(s), 1);
      }
#pragma unroll
      for (int f = 0; f < 7; ++f) LDS16(lX + f * 4096 + c.tid * 16) = x[f];
      BAR();
      STAMP(t, 1);
#pragma unroll 1
      for (int tau = 0; tau < 2; ++tau) {
        const unsigned char* xp = lX + tau * 14 * KB + lane * 16;
        const unsigned xnext = (tau ? rec_in(L, s + 2) : OOB) + R_XN + c.tid * 16;
        f4 acc[8] = {Z4, Z4, Z4, Z4, Z4, Z4, Z4, Z4};
        v4u xs[7];
#pragma unroll
        for (int k = 0; k < 7; ++k) xs[k] = LDS16(xp + (kx + k) * KB);
        v4u x0 = LDS16(xp), x1 = LDS16(xp + KB);
        SB();
#pragma unroll
        for (int k = 0; k < 7; ++k) {
          acc[6] = mfma32(W[84 + k], xs[k], acc[6]);
          acc[7] = mfma32(W[91 + k], xs[k], acc[7]);
        }
        SB();
        unsigned char* gp = lG + tau * 16 * KB + lane * 16;
#pragma unroll
        for (int ks = 0; ks < 14; ++ks) {
          const v4u xk = x0;
          x0 = x1;
          if (ks + 2 < 14) x1 = LDS16(xp + (ks + 2) * KB);
          if (ks == 2) LDS16(gp + (12 + w) * KB) = pk8(acc[6], acc[7]);
#pragma unroll
          for (int pp = 0; pp < 3; ++pp) {
            acc[pp * 2 + 0] = mfma32(W[pp * 28 + ks], xk, acc[pp * 2 + 0]);
            acc[pp * 2 + 1] = mfma32(W[pp * 28 + 14 + ks], xk, acc[pp * 2 + 1]);
            if (L == 0 && pp == 0 && ks >= 3 && ks < 10) { SB(); x[ks - 3] = ld16c(c.rs, xnext + (ks - 3) * 4096); SB(); }
          }
          SB();
        }
#pragma unroll
        for (int pp = 0; pp < 3; ++pp) LDS16(gp + (3 * w + pp) * KB) = pk8(acc[pp * 2], acc[pp * 2 + 1]);
      }
      BAR();
#pragma unroll
      for (int k = 0; k < 4; ++k) { g[k] = LDS16(lG + (4 * w + k) * KB + lane * 16); g[4 + k] = LDS16(lG + (16 + 4 * w + k) * KB + lane * 16); }
    } else {
#pragma unroll
      for (int k = 0; k < 8; ++k) g[k] = v4u{0u, 0u, 0u, 0u};
    }
    STAMP(t, 2);
    {
      const unsigned slot = (unsigned)(t & 3) * 32 * KB;
      v4u m[4];
      CBAR();
#pragma unroll
      for (int k = 0; k < 4; ++k) m[k] = ld16c(c.rs, mo[k] + slot);
      POLL(anysent(mx4(mx4(mx4(mx4(0u, m[0]), m[1]), m[2]), m[3])),
           { _Pragma("unroll") for (int k = 0; k < 4; ++k) m[k] = ld16c(c.rs, mo[k] + slot); }, 0);
      STAMP(t, 3);
      f4 a0 = Z4, a1 = Z4;
#pragma unroll
      for (int k = 0; k < 4; ++k) { a0 = mfma32(g[k], m[k], a0); a1 = mfma32(g[4 + k], m[k], a1); }
      LDSF4(lred + 4 * KB + w * KB + lane * 16) = (i16 < 8) ? a0 : a1;
      BAR();
      if (w == 0) {
        const unsigned char* rp = lred + 4 * KB + lane * 16;
        f4 v = (LDSF4(rp) + LDSF4(rp + KB)) + (LDSF4(rp + 2 * KB) + LDSF4(rp + 3 * KB));
        float m1 = mxf4(v, NEG);
        m1 = mx3(m1, ror8(m1), NEG);
        m1 = rmax4(m1);
        const float ml = m1 * LOG2E;
#pragma unroll
        for (int r = 0; r < 4; ++r) v[r] = ex2(v[r] * LOG2E - ml);
        const unsigned eslot = (unsigned)(t & 7) * 32 * KB, xslot = (unsigned)(t & 7) * 16 * KB;
        st8c(c.rs, eo + eslot, act ? ns2(pk4(v)) : v2u{0u, 0u});
        float ls = (v[0] + v[1]) + (v[2] + v[3]);
        ls += ror8(ls);
        ls = rsum4(ls);
        if (lane < 8) st8c(c.rs, xo + xslot, act ? ns2(v2u{__float_as_uint(m1), __float_as_uint(ls)}) : v2u{__float_as_uint(-1.0e30f), 0u});
        if (t >= 2) {
          st8c(c.rs, eo + (unsigned)((t - 2) & 7) * 32 * KB, S2);
          if (lane < 8) st8c(c.rs, xo + (unsigned)((t - 2) & 7) * 16 * KB, S2);
        }
      }
      STAMP(t, 4);
    }
    if (act) {
      const int tq = w & 1, kh = w >> 1;
      f4 a0 = Z4, a1 = Z4;
#pragma unroll
      for (int hf = 0; hf < 3; ++hf) {
        v4u ga[3], gb[3], xq[3];
#pragma unroll
        for (int k = 0; k < 3; ++k)
          if (hf * 3 + k < 8) {
            const int vu = kh * 8 + hf * 3 + k, ks = vu < 12 ? vu : 12 + ((vu - 12) >> 1);
            ga[k] = LDS16(lG + vu * KB + lane * 16);
            gb[k] = LDS16(lG + (16 + vu) * KB + lane * 16);
            xq[k] = LDS16(lX + (tq * 14 + ks) * KB + lane * 16);
          }
        SB();
#pragma unroll
        for (int k = 0; k < 3; ++k)
          if (hf * 3 + k < 8) { a0 = mfma32(ga[k], xq[k], a0); a1 = mfma32(gb[k], xq[k], a1); }
        SB();
      }
      if (!kh) { LDSF4(lred + (tq * 2 + 0) * KB + lane * 16) = a0; LDSF4(lred + (tq * 2 + 1) * KB + lane * 16) = a1; }
      BAR();
      if (L == 0) have = !anysent(mx4(mx4(mx4(mx4(mx4(mx4(mx4(0u, x[0]), x[1]), x[2]), x[3]), x[4]), x[5]), x[6]));
      if (kh) {
        a0 += LDSF4(lred + (tq * 2 + 0) * KB + lane * 16);
        a1 += LDSF4(lred + (tq * 2 + 1) * KB + lane * 16);
        const int lim = tok_of(tq, i16) - 8 * q;
#pragma unroll
        for (int r = 0; r < 4; ++r) {
          a0[r] = (r <= lim) ? a0[r] : NEG;
          a1[r] = (4 + r <= lim) ? a1[r] : NEG;
        }
        const float m = rmax4(mxf4(a1, mxf4(a0, NEG)));
        const float ml = m * LOG2E;
        float lsum = 0.f;
#pragma unroll
        for (int r = 0; r < 4; ++r) {
          a0[r] = (r <= lim) ? ex2(a0[r] * LOG2E - ml) : 0.f;
          a1[r] = (4 + r <= lim) ? ex2(a1[r] * LOG2E - ml) : 0.f;
          lsum += a0[r] + a1[r];
        }
        lsum = rsum4(lsum);
        const unsigned o1 = R_ETT + (unsigned)(h * 2 + tq) * KB + lane * 16, o2 = R_ETS + (unsigned)((tq * 16 + h) * 16 + i16) * 8;
        st16c(c.rs, rec_off(L, s) + o1, ns4(pk8(a0, a1)));
        if (q == 0) st8c(c.rs, rec_off(L, s) + o2, ns2(v2u{__float_as_uint(m), __float_as_uint(lsum)}));
        st16c(c.rs, rec_in(L, s + AD) + o1, S4);
        if (q == 0) st8c(c.rs, rec_in(L, s + AD) + o2, S2);
      }
    }
    STAMP(t, 5);
  }
}

template <int L>
DEVI void role_O(Ctx& c, int tau, int cg, unsigned char* lds, __hip_bfloat16* out) {
  const int lane = c.lane, w = c.wave;
  const int i16 = lane & 15, q = lane >> 4;
  const int ct = 4 * cg + w;
  const unsigned po = (unsigned)(tau * 14 + (ct >> 1)) * KB + (unsigned)((2 * (ct & 1) + (q >> 1)) * 16 + i16) * 16 + (q & 1) * 8;
  v4u U[16], e[4], sv[4], z[4];
  v2u es, xr;
  auto load_u = [&](int s) {
    const unsigned ub = u_in(L, s) + (unsigned)(4 * cg * 16 + 4 * w) * KB + lane * 16;
#pragma unroll
    for (int k = 0; k < 4; ++k)
#pragma unroll
      for (int hh = 0; hh < 4; ++hh) U[k * 4 + hh] = ld16c(c.rs, ub + (k * 16 + hh) * KB);
    xr = ld8c(c.rs, rec_in(L, s) + R_XR + po);
  };
  auto load_e = [&](int s) {
    const unsigned rb = rec_off(L, s);
#pragma unroll
    for (int hh = 0; hh < 4; ++hh) e[hh] = ld16c(c.rs, rb + R_ETT + (unsigned)((4 * w + hh) * 2 + tau) * KB + lane * 16);
    es = ld8c(c.rs, rb + R_ETS + (unsigned)((tau * 16 + 4 * w) * 16 + lane) * 8);
  };
  auto load_s = [&](int s) {
    const unsigned rb = rec_off(L, s);
#pragma unroll
    for (int k = 0; k < 4; ++k) sv[k] = ld16c(c.rs, rb + R_STM + (unsigned)((k * 2 + tau) * 16 + 4 * w) * 256 + lane * 16);
#pragma unroll
    for (int k = 0; k < 4; ++k) z[k] = ld16c(c.rs, rb + R_ZB + (unsigned)((4 * cg + k) * 4 + w) * KB + lane * 16);
  };
  const int fpar = (lane >> 2) & 1;
  const unsigned fo = (lane & 8) == 0 ? F_U + (unsigned)(fpar * 28 + 4 * cg + (lane & 3)) * 64 + L * 16 + w * 4
                                      : F_XR + (unsigned)((L * 2 + tau) * 2 + fpar) * 64 + (lane & 3) * 4;
  int fl = 0;
  for (int s = 0; s < NSEG; ++s) {
    unsigned char* lred = lds + (s & 1) * 16 * KB;
    STAMP(s, 0);
    {
      const int need = (fpar != ((((lane & 8) == 0 ? s + L * LAG : s)) & 1)) ? 0 : s + 1;
      POLL(__builtin_amdgcn_ballot_w64(fl < need) != 0ull, fl = (int)ld4c(c.rs, fo), 1);
      CBAR();
      load_u(s);
    }
    STAMP(s, 1);
    CBAR();
    load_e(s);
    load_s(s);
    fl = (int)ld4c(c.rs, fo);
    POLL(anysent(mx4(mx4(mx4(mx4(mx4(mx4(mx4(mx4(mx2(mx4(mx4(mx4(mx4(0u, e[0]), e[1]), e[2]), e[3]), es), sv[0]), sv[1]), sv[2]), sv[3]), z[0]), z[1]), z[2]), z[3])),
         { load_e(s); load_s(s); fl = (int)ld4c(c.rs, fo); }, 1);
    TOUCH(fl);
    SB();
    f4 ott[16];
#pragma unroll
    for (int k = 0; k < 4; ++k)
#pragma unroll
      for (int hh = 0; hh < 4; ++hh) ott[k * 4 + hh] = mfma32(U[k * 4 + hh], e[hh], Z4);
    STAMP(s, 2);
    {
      f4 a = u2f(sv[0]) + u2f(sv[2]), b = u2f(sv[1]) + u2f(sv[3]);
      const float mt = __uint_as_float(es.x), lt = __uint_as_float(es.y);
      const float mm = mxf4(b, mxf4(a, mt));
      const float ml = mm * LOG2E;
#pragma unroll
      for (int r = 0; r < 4; ++r) { a[r] = ex2(a[r] * LOG2E - ml); b[r] = ex2(b[r] * LOG2E - ml); }
      const float at = ex2(mt * LOG2E - ml);
      const float den = ((a[0] + a[1]) + (a[2] + a[3])) + ((b[0] + b[1]) + (b[2] + b[3])) + at * lt;
      const float inv = rcp(den);
      const v4u P = pk8(a * inv, b * inv);
      float wt[4];
      rbcast4(at * inv, wt);
#pragma unroll
      for (int k = 0; k < 4; ++k) {
        f4 acc = mfma32(z[k], P, Z4);
        acc += (ott[k * 4 + 0] * wt[0] + ott[k * 4 + 1] * wt[1]) + (ott[k * 4 + 2] * wt[2] + ott[k * 4 + 3] * wt[3]);
        LDSF4(lred + (k * 4 + w) * KB + lane * 16) = acc;
      }
    }
    BAR();
    {
      const unsigned char* rp = lred + w * 4 * KB + lane * 16;
      const f4 acc = (LDSF4(rp) + LDSF4(rp + KB)) + (LDSF4(rp + 2 * KB) + LDSF4(rp + 3 * KB));
      const v2u y = pk4(acc);
      const v2u o = v2u{pk2(bflo(xr.x) + bflo(y.x), bfhi(xr.x) + bfhi(y.x)), pk2(bflo(xr.y) + bflo(y.y), bfhi(xr.y) + bfhi(y.y))};
      if (L == 0) {
        st8c(c.rs, rec_off(0, s) + R_X2 + po, ns2(o));
        st8c(c.rs, rec_in(0, s + AD) + R_X2 + po, S2);
      } else {
        *(v2u*)((unsigned char*)out + (size_t)(s * 32 + tok_of(tau, i16)) * 896 + (ct * 16 + 4 * q) * 2) = o;
      }
    }
    STAMP(s, 3);
  }
}

template <int L>
DEVI void role_AH(Ctx& c, int a, unsigned char* lds) {
  asm volatile("; layer %0" :: "n"(L) : "memory");
  const int lane = c.lane, w = c.wave;
  const int i16 = lane & 15, q = lane >> 4;
  unsigned char* lX = lds;
  unsigned char* lK = lds + 28 * KB;
  unsigned char* lV = lds + 46 * KB;
  unsigned char* lQ = lds + 64 * KB;
  unsigned char* lst = lds + 66 * KB;
  unsigned char* lmg = lds + 67 * KB;
  unsigned char* lms = lds + 71 * KB;
  const int tau = w & 1, g = w >> 1;
  v4u x[7];
  auto load_x = [&](int s) {
    const unsigned xb = (L == 0 ? X0_OFF + (unsigned)s * SEGX : rec_off(0, s) + R_X2) + (unsigned)(tau * 14 + g * 7) * KB + lane * 16;
#pragma unroll
    for (int f = 0; f < 7; ++f) x[f] = (L == 0) ? ld16(c.rs, xb + f * KB) : ld16c(c.rs, xb + f * KB);
  };
  if (L == 0) load_x(0);
  v4u W[42];
#pragma unroll
  for (int tl = 0; tl < 3; ++tl) {
    const unsigned b0 = WQ_OFF + (unsigned)(a * 6 + (tl < 2 ? 2 * g + tl : 4 + g)) * 14 * KB + lane * 16;
#pragma unroll
    for (int k = 0; k < 14; ++k) W[tl * 14 + k] = ld16(c.rs, b0 + (unsigned)(k < 7 ? 7 * g + k : 7 * (1 - g) + k - 7) * KB);
  }
  for (int o = c.tid * 16; o < 36 * (int)KB; o += 4096) LDS16(lK + o) = v4u{0u, 0u, (o < 18 * (int)KB && q == 3) ? 0xFF7Fu : 0u, 0u};
  f4 cs0 = Z4, cs1 = Z4, cs2 = Z4;
  {
    const v4u one = {0x3F803F80u, 0x3F803F80u, 0x3F803F80u, 0x3F803F80u};
#pragma unroll
    for (int k = 0; k < 14; ++k) { cs0 = mfma32(W[k], one, cs0); cs1 = mfma32(W[14 + k], one, cs1); cs2 = mfma32(one, W[28 + k], cs2); }
  }
#pragma unroll
  for (int f = 0; f < 42; ++f) PIN_A(W[f]);
  const float csv = cs2[0];
  const float vone = (g == 1 && i16 == 12) ? 1.0f : 0.0f;
  const float qone = (g == 0 && q == 3) ? 1.0f : 0.0f;
  const float sc = 0.18898223650461362f * LOG2E;
  const int lim = tok_of(tau, i16) - 8 * q;
  v4u dm;
#pragma unroll
  for (int e = 0; e < 4; ++e) dm[e] = ((2 * e <= lim) ? 0xFFFFu : 0u) | ((2 * e + 1 <= lim) ? 0xFFFF0000u : 0u);
  const int c0 = 28 * a + 4 * q, c1 = c0 + 16;
  const unsigned oa = R_OA + (unsigned)tau * 14 * KB + (c0 >> 5) * KB + (((c0 >> 3) & 3) * 16 + i16) * 16 + (c0 & 4) * 2;
  const unsigned ob = (q < 3) ? R_OA + (unsigned)tau * 14 * KB + (c1 >> 5) * KB + (((c1 >> 3) & 3) * 16 + i16) * 16 + (c1 & 4) * 2 : OOB;
  CBAR();
  if (L == 0) {
#pragma unroll
    for (int f = 0; f < 7; ++f) TOUCH(x[f]);
  }
  int slot = 0;
  bool have = false;
  for (int s = 0; s < NSEG; ++s) {
    STAMP(s, 0);
    if (L == 1 && !have) {
      CBAR();
      load_x(s);
      POLL(XBAD, load_x(s), 1);
    }
    STAMP(s, 1);
#pragma unroll
    for (int f = 0; f < 7; ++f) LDS16(lX + (tau * 14 + g * 7 + f) * KB + lane * 16) = x[f];
    f4 a0 = Z4, a1 = Z4, a2 = Z4;
    {
      float sum = 0.f, sq = 0.f;
#pragma unroll
      for (int f = 0; f < 7; ++f) {
        a0 = mfma32(W[f], x[f], a0);
        a1 = mfma32(W[14 + f], x[f], a1);
        a2 = mfma32(x[f], W[28 + f], a2);
#pragma unroll
        for (int e = 0; e < 4; ++e) { sum = dot2(x[f][e], 0x3F803F80u, sum); sq = dot2(x[f][e], x[f][e], sq); }
        SB();
      }
      sum = rsum4(sum);
      sq = rsum4(sq);
      if (q == 0) *(f2*)(lst + ((g * 2 + tau) * 16 + i16) * 8) = f2{sum, sq};
    }
    BAR();
    {
      v4u y[7];
#pragma unroll
      for (int f = 0; f < 7; ++f) y[f] = LDS16(lX + (tau * 14 + (1 - g) * 7 + f) * KB + lane * 16);
      const f2 sa = *(const f2*)(lst + (tau * 16 + i16) * 8), sb = *(const f2*)(lst + ((2 + tau) * 16 + i16) * 8);
      const f4 t0 = LDSF4(lst + (tau * 16 + 4 * q) * 8), t1 = LDSF4(lst + (tau * 16 + 4 * q) * 8 + 16);
      const f4 t2 = LDSF4(lst + ((2 + tau) * 16 + 4 * q) * 8), t3 = LDSF4(lst + ((2 + tau) * 16 + 4 * q) * 8 + 16);
      CBAR();
      if (L == 0) load_x(min(s + 1, NSEG - 1));
      SB();
#pragma unroll
      for (int f = 0; f < 7; ++f) {
        a0 = mfma32(W[7 + f], y[f], a0);
        a1 = mfma32(W[21 + f], y[f], a1);
        a2 = mfma32(y[f], W[35 + f], a2);
      }
      const float mean = (sa[0] + sb[0]) * (1.0f / 448.0f);
      const float rstd = rsq(fmaxf((sa[1] + sb[1]) * (1.0f / 448.0f) - mean * mean, 0.f) + 1e-5f);
      const float mr = mean * rstd;
      const f4 sm = f4{t0[0] + t2[0], t0[2] + t2[2], t1[0] + t3[0], t1[2] + t3[2]} * (1.0f / 448.0f);
      const f4 s2 = f4{t0[1] + t2[1], t0[3] + t2[3], t1[1] + t3[1], t1[3] + t3[3]} * (1.0f / 448.0f);
      f4 rs;
#pragma unroll
      for (int r = 0; r < 4; ++r) rs[r] = rsq(fmaxf(s2[r] - sm[r] * sm[r], 0.f) + 1e-5f);
      a0 = a0 * rstd - cs0 * mr;
      a1 = a1 * rstd - cs1 * mr;
      a1[0] += qone;
      LDS16((g == 0 ? lQ + tau * KB : lK + (slot * 2 + tau) * KB) + lane * 16) = pk8(a0, a1);
      a2 = (a2 - sm * csv) * rs + vone;
      *(v2u*)(lV + (slot * 2 + g) * KB + lane * 16 + tau * 8) = pk4(a2);
    }
    BAR();
    STAMP(s, 2);
    {
      const v4u qf = LDS16(lQ + tau * KB + lane * 16);
      int sl[5];
      v4u kf[10];
#pragma unroll
      for (int b = 0; b < 5; ++b) {
        const int d = (b < 4) ? 4 * g + b : 8;
        int k = slot - d;
        k += (k < 0) ? 9 : 0;
        sl[b] = k;
        if (b < 4 || g == 0) { kf[2 * b] = LDS16(lK + (k * 2) * KB + lane * 16); kf[2 * b + 1] = LDS16(lK + (k * 2 + 1) * KB + lane * 16); }
      }
      SB();
      f4 scv[4][2];
#pragma unroll
      for (int b = 0; b < 4; ++b) {
        scv[b][0] = mfma32(kf[2 * b], qf, Z4);
        scv[b][1] = mfma32(kf[2 * b + 1], qf, Z4);
      }
      v4u vf[10];
      f4 e0 = Z4, e1 = Z4;
      if (g == 0) { e0 = mfma32(kf[8], qf, Z4); e1 = mfma32(kf[9], qf, Z4); }
      SB();
#pragma unroll
      for (int b = 0; b < 5; ++b)
        if (b < 4 || g == 0) { vf[2 * b] = LDS16(lV + (sl[b] * 2) * KB + lane * 16); vf[2 * b + 1] = LDS16(lV + (sl[b] * 2 + 1) * KB + lane * 16); }
      if (L == 1) { CBAR(); load_x(min(s + 1, NSEG - 1)); }
      SB();
#pragma unroll
      for (int b = 0; b < 4; ++b) { PIN_V(scv[b][0]); PIN_V(scv[b][1]); }
      if (g == 0) {
        PIN_V(e0); PIN_V(e1);
#pragma unroll
        for (int r = 0; r < 4; ++r) {
          scv[0][0][r] = (r <= lim) ? scv[0][0][r] : e0[r];
          scv[0][1][r] = (4 + r <= lim) ? scv[0][1][r] : e1[r];
        }
      }
      float m = -1.0e30f;
#pragma unroll
      for (int b = 0; b < 4; ++b) m = mxf4(scv[b][1], mxf4(scv[b][0], m));
      m = rmax4(m);
      const float ml = m * sc;
      f4 o0 = Z4, o1 = Z4;
#pragma unroll
      for (int bb = 0; bb < 4; ++bb) {
        const int b = 3 - bb;
#pragma unroll
        for (int r = 0; r < 4; ++r) {
          scv[b][0][r] = ex2(scv[b][0][r] * sc - ml);
          scv[b][1][r] = ex2(scv[b][1][r] * sc - ml);
        }
        v4u p = pk8(scv[b][0], scv[b][1]);
        if (b == 0 && g == 0) {
          const v4u p8 = p & ~dm;
          p = p & dm;
          o0 = mfma32(vf[8], p8, o0);
          o1 = mfma32(vf[9], p8, o1);
        }
        o0 = mfma32(vf[2 * b], p, o0);
        o1 = mfma32(vf[2 * b + 1], p, o1);
      }
      if (g == 1) {
        LDSF4(lmg + (tau * 2 + 0) * KB + lane * 16) = o0;
        LDSF4(lmg + (tau * 2 + 1) * KB + lane * 16) = o1;
        *(float*)(lms + (tau * 64 + lane) * 4) = m;
      }
      if (L == 0) {
#pragma unroll
        for (int f = 0; f < 7; ++f) TOUCH(x[f]);
      } else {
        have = !XBAD;
      }
      BAR();
      if (g == 0) {
        const float mo = *(const float*)(lms + (tau * 64 + lane) * 4);
        const float mt = mx3(m, mo, NEG);
        const float fa = ex2((m - mt) * sc), fb = ex2((mo - mt) * sc);
        const f4 r0 = o0 * fa + LDSF4(lmg + (tau * 2 + 0) * KB + lane * 16) * fb;
        const f4 r1 = o1 * fa + LDSF4(lmg + (tau * 2 + 1) * KB + lane * 16) * fb;
        float dn[4];
        rbcast4(r1[0], dn);
        const float inv = rcp(dn[3]);
        st8c(c.rs, rec_off(L, s) + oa, ns2(pk4(r0 * inv)));
        st8c(c.rs, rec_off(L, s) + ob, ns2(pk4(r1 * inv)));
        if (s + AD < NSEG) {
          st8c(c.rs, rec_off(L, s + AD) + oa, S2);
          st8c(c.rs, rec_off(L, s + AD) + ob, S2);
        }
      }
      STAMP(s, 3);
    }
    slot = (slot == 8) ? 0 : slot + 1;
  }
}

template <int l>
DEVI void role_AO(Ctx& c, int tau, int par, unsigned char* lds) {
  asm volatile("; layer %0" :: "n"(l) : "memory");
  const int lane = c.lane, w = c.wave;
  const int i16 = lane & 15, q = lane >> 4;
  unsigned char* lO = lds;
  unsigned char* lx = lds + 14 * KB;
  unsigned char* lst = lds + 18 * KB;
  v4u W[98];
  {
    const unsigned b0 = WO_OFF + (unsigned)(3 * w) * 28 * KB + lane * 16;
#pragma unroll
    for (int f = 0; f < 84; ++f) W[f] = ld16(c.rs, b0 + f * KB);
    const unsigned b1 = WO_OFF + (unsigned)(12 + (w >> 1)) * 28 * KB + (w & 1) * 7 * KB + lane * 16;
#pragma unroll
    for (int k = 0; k < 7; ++k) { W[84 + k] = ld16(c.rs, b1 + k * KB); W[91 + k] = ld16(c.rs, b1 + (14 + k) * KB); }
#pragma unroll
    for (int f = 0; f < 56; ++f) PIN_A(W[f]);
  }
  const int ux = 12 + (w >> 1), kx = 7 * (w & 1);
  const bool own4 = !(w & 1);
  const unsigned fo = (lane == 0) ? F_XR + (unsigned)((l * 2 + tau) * 2 + par) * 64 + w * 4 : OOB;
  for (int s = par; s < NSEG; s += 2) {
    const unsigned rb = rec_off(l, s);
    STAMP(s, 0);
    {
      const unsigned ob = rb + R_OA + (unsigned)tau * 14 * KB;
      v4u v[4];
      auto ldo = [&]() {
        v[0] = ld16c(c.rs, ob + c.tid * 16); v[1] = ld16c(c.rs, ob + 4096 + c.tid * 16); v[2] = ld16c(c.rs, ob + 8192 + c.tid * 16);
        v[3] = ld16c(c.rs, ob + 12288 + (c.tid & 127) * 16);
      };
      CBAR();
      ldo();
      DRAIN();
      st4c(c.rs, fo, (unsigned)max(s - 1, 0));
      POLL(anysent(mx4(mx4(mx4(mx4(0u, v[0]), v[1]), v[2]), v[3])), ldo(), 1);
      LDS16(lO + c.tid * 16) = v[0];
      LDS16(lO + 4096 + c.tid * 16) = v[1];
      LDS16(lO + 8192 + c.tid * 16) = v[2];
      LDS16(lO + 12288 + (c.tid & 127) * 16) = v[3];
    }
    STAMP(s, 1);
    v4u xr[4];
    auto ldr = [&]() {
#pragma unroll
      for (int p = 0; p < 4; ++p) {
        const int u = (p < 3) ? 3 * w + p : ux;
        if (l == 0) xr[p] = ld16(c.rs, X0_OFF + (unsigned)s * SEGX + (unsigned)(tau * 14 + u) * KB + lane * 16);
        else xr[p] = ld16c(c.rs, rec_off(0, s) + R_X2 + (unsigned)(tau * 14 + u) * KB + lane * 16);
      }
    };
    BAR();
    f4 acc[8] = {Z4, Z4, Z4, Z4, Z4, Z4, Z4, Z4};
    {
      const unsigned char* xp = lO + lane * 16;
      v4u xs[7];
#pragma unroll
      for (int k = 0; k < 7; ++k) xs[k] = LDS16(xp + (kx + k) * KB);
      v4u y0 = LDS16(xp), y1 = LDS16(xp + KB);
      SB();
#pragma unroll
      for (int k = 0; k < 7; ++k) {
        acc[6] = mfma32(W[84 + k], xs[k], acc[6]);
        acc[7] = mfma32(W[91 + k], xs[k], acc[7]);
      }
      SB();
      ldr();
      SB();
#pragma unroll
      for (int ks = 0; ks < 14; ++ks) {
        const v4u x = y0;
        y0 = y1;
        if (ks + 2 < 14) y1 = LDS16(xp + (ks + 2) * KB);
        if (ks == 2 && !own4) { LDSF4(lx + ((w >> 1) * 2 + 0) * KB + lane * 16) = acc[6]; LDSF4(lx + ((w >> 1) * 2 + 1) * KB + lane * 16) = acc[7]; }
#pragma unroll
        for (int pp = 0; pp < 3; ++pp) {
          acc[pp * 2 + 0] = mfma32(W[pp * 28 + ks], x, acc[pp * 2 + 0]);
          acc[pp * 2 + 1] = mfma32(W[pp * 28 + 14 + ks], x, acc[pp * 2 + 1]);
        }
        SB();
      }
    }
    if (l == 1) POLL(anysent(mx4(mx4(mx4(mx4(0u, xr[0]), xr[1]), xr[2]), xr[3])), ldr(), 1);
    BAR();
    STAMP(s, 2);
    v4u xb[4];
    if (own4) {
      acc[6] += LDSF4(lx + ((w >> 1) * 2 + 0) * KB + lane * 16);
      acc[7] += LDSF4(lx + ((w >> 1) * 2 + 1) * KB + lane * 16);
    }
    float sum = 0.f, sq = 0.f;
#pragma unroll
    for (int p = 0; p < 4; ++p) {
      xb[p] = resid8(xr[p], acc[2 * p], acc[2 * p + 1]);
      if (p == 3 && !own4) xb[p] = v4u{0u, 0u, 0u, 0u};
#pragma unroll
      for (int e = 0; e < 4; ++e) { sum = dot2(xb[p][e], 0x3F803F80u, sum); sq = dot2(xb[p][e], xb[p][e], sq); }
    }
    sum = rsum4(sum);
    sq = rsum4(sq);
    if (q == 0) *(f2*)(lst + (w * 16 + i16) * 8) = f2{sum, sq};
    BAR();
    const f2 st = (*(const f2*)(lst + i16 * 8) + *(const f2*)(lst + (16 + i16) * 8)) + (*(const f2*)(lst + (32 + i16) * 8) + *(const f2*)(lst + (48 + i16) * 8));
    const float mean = st[0] * (1.0f / 448.0f);
    const float rstd = rsq(fmaxf(st[1] * (1.0f / 448.0f) - mean * mean, 0.f) + 1e-5f);
    const float mr = mean * rstd;
#pragma unroll
    for (int p = 0; p < 4; ++p)
      if (p < 3 || own4) {
        const int u = (p < 3) ? 3 * w + p : ux;
        const unsigned fo = (unsigned)(tau * 14 + u) * KB + lane * 16;
        v4u o;
#pragma unroll
        for (int e = 0; e < 4; ++e) o[e] = pk2(bflo(xb[p][e]) * rstd - mr, bfhi(xb[p][e]) * rstd - mr);
        st16c(c.rs, rb + R_XN + fo, ns4(o));
      }
#pragma unroll
    for (int p = 0; p < 4; ++p)
      if (p < 3 || own4) {
        const int u = (p < 3) ? 3 * w + p : ux;
        const unsigned fo = (unsigned)(tau * 14 + u) * KB + lane * 16;
        st16c(c.rs, rb + R_XR + fo, xb[p]);
        st16c(c.rs, rec_in(l, s + AD) + R_XN + fo, S4);
      }
    STAMP(s, 3);
  }
  DRAIN();
  st4c(c.rs, fo, (unsigned)NSEG);
}

DEVI unsigned ring_offset(const unsigned char* ws) {
  const unsigned lo = (unsigned)((size_t)ws & (2u * MB - 1u));
  return ((lo + FLAG_BYTES + 2u * MB - 1u) & ~(2u * MB - 1u)) - lo;
}

__global__ void __launch_bounds__(256, 1) mega_kernel(unsigned char* ws, const unsigned char* mem0, __hip_bfloat16* out) {
  extern __shared__ __attribute__((aligned(16))) unsigned char lds[];
  Ctx c;
  c.ws = ws;
  c.rs = __builtin_amdgcn_make_buffer_rsrc(ws, 0, WS_END, 0x00020000);
  c.ring = ring_offset(ws);
  c.tid = threadIdx.x;
  c.lane = threadIdx.x & 63;
  c.wave = __builtin_amdgcn_readfirstlane(threadIdx.x >> 6);
  c.dead = false;
  const int b = blockIdx.x, x = b & 7, r = b >> 3;
  STAMP(127, 0);
  {
    const unsigned char* pc = (const unsigned char*)(__builtin_amdgcn_s_getpc() & ~(unsigned long long)255);
    const unsigned o = (unsigned)(r * 256 + threadIdx.x) * 16;
    if (o < CPF * KB) { const v4u v = *(const v4u*)(pc + o); TOUCH(v); }
  }
  if (x == 0) {
    role_H(c, r >> 1, r & 1, lds);
  } else if (x < 3) {
    if (r < 28) role_CV(c, r, x - 1, lds, mem0);
  } else if (x < 5) {
    if (r & 1) role_GX<1>(c, r >> 1, x - 3, lds); else role_GX<0>(c, r >> 1, x - 3, lds);
  } else if (x == 5) {
    if (r < 14) role_O<0>(c, r / 7, r % 7, lds, out); else if (r < 28) role_O<1>(c, (r - 14) / 7, (r - 14) % 7, lds, out);
  } else if (x == 6) {
    if (r >> 4) role_AH<1>(c, r & 15, lds); else role_AH<0>(c, r & 15, lds);
  } else {
    if (r < 4) role_AO<0>(c, r & 1, r >> 1, lds); else if (r < 8) role_AO<1>(c, r & 1, (r >> 1) & 1, lds); else if (r < 16) probe_side(c, r - 8);
  }
}

#define PAUX 16
#define PST(off, v) __builtin_amdgcn_raw_buffer_store_b128(v, rs, off, 0, PAUX)
__global__ void __launch_bounds__(512) prologue_kernel(unsigned char* ws, const unsigned short* x0, const unsigned short* wqkv, const unsigned short* wo,
                                                       const unsigned short* mem0, const unsigned short* Wq, const unsigned short* Wk,
                                                       const unsigned short* Wv, const unsigned short* Wo) {
  extern __shared__ __attribute__((aligned(16))) unsigned char lb[];
  const int b = blockIdx.x, tid = threadIdx.x, lane = tid & 63, w = __builtin_amdgcn_readfirstlane(tid >> 6);
  const int i16 = lane & 15, q = lane >> 4;
  const rsrc_t rs = __builtin_amdgcn_make_buffer_rsrc(ws, 0, WS_END, 0x00020000);
  if (b < 224) {
    const int kk = (b >> 3) / 7, cg = (b >> 3) % 7;
    const bool isG = kk < 2;
    const int h = (2 * (b & 7) + kk) & 15;
    const unsigned short* Wa = isG ? Wk : Wv;
    v4u bv[4];
    if (isG) {
#pragma unroll
      for (int k = 0; k < 4; ++k) bv[k] = *(const v4u*)(Wq + ((size_t)(64 * cg + 16 * k + (tid >> 5)) * 16 + h) * 256 + (tid & 31) * 8);
    } else {
#pragma unroll
      for (int k = 0; k < 4; ++k) bv[k] = *(const v4u*)(Wo + ((size_t)h * 256 + 4 * (tid >> 3) + k) * 448 + 64 * cg + 8 * (tid & 7));
    }
    v4u A[4][8];
    if (w < 7) {
#pragma unroll
      for (int mt = 0; mt < 4; ++mt)
#pragma unroll
        for (int k = 0; k < 8; ++k) {
          const int n = 2 * k + (lane >> 5), d = 64 * w + 32 * (mt >> 1) + 8 * (n >> 2) + 4 * (mt & 1) + (n & 3);
          A[mt][k] = *(const v4u*)(Wa + ((size_t)d * 16 + h) * 256 + (lane & 31) * 8);
        }
    }
    if (isG) {
#pragma unroll
      for (int k = 0; k < 4; ++k) LDS16(lb + (16 * k + (tid >> 5)) * 528 + (tid & 31) * 16) = bv[k];
    } else {
      unsigned char* p = lb + 8 * (tid & 7) * 528 + (tid >> 3) * 8;
#pragma unroll
      for (int k = 0; k < 4; ++k) {
        *(v2u*)(p + (2 * k) * 528) = v2u{__builtin_amdgcn_perm(bv[1][k], bv[0][k], 0x05040100u), __builtin_amdgcn_perm(bv[3][k], bv[2][k], 0x05040100u)};
        *(v2u*)(p + (2 * k + 1) * 528) = v2u{__builtin_amdgcn_perm(bv[1][k], bv[0][k], 0x07060302u), __builtin_amdgcn_perm(bv[3][k], bv[2][k], 0x07060302u)};
      }
    }
    BAR();
    if (w < 7) {
      unsigned char* la = lb + (64 + 16 * w) * 528;
      const float sc = isG ? 0.0625f : 1.0f;
#pragma unroll
      for (int kh = 0; kh < 2; ++kh) {
#pragma unroll
        for (int mt = 2 * kh; mt < 2 * kh + 2; ++mt) {
#pragma unroll
          for (int k = 0; k < 8; ++k) LDS16(la + (2 * k + (lane >> 5)) * 528 + (lane & 31) * 16) = A[mt][k];
          CBAR();
#pragma unroll
          for (int es = 0; es < 8; ++es) A[mt][es] = LDS16(la + i16 * 528 + es * 64 + q * 16);
          CBAR();
        }
        f4 a[8] = {Z4, Z4, Z4, Z4, Z4, Z4, Z4, Z4};
#pragma unroll
        for (int es = 0; es < 8; ++es)
#pragma unroll
          for (int nt = 0; nt < 4; ++nt) {
            const int row = isG ? 32 * (nt >> 1) + 8 * (i16 >> 2) + 4 * (nt & 1) + (i16 & 3) : 16 * nt + i16;
            const v4u bf = LDS16(lb + row * 528 + es * 64 + q * 16);
            a[nt * 2 + 0] = mfma32(A[2 * kh][es], bf, a[nt * 2 + 0]);
            a[nt * 2 + 1] = mfma32(A[2 * kh + 1][es], bf, a[nt * 2 + 1]);
          }
#pragma unroll
        for (int nt = 0; nt < 4; ++nt) {
          const unsigned off = isG ? WG_OFF + (unsigned)(((h * 14 + 2 * cg + (nt >> 1)) * 2 + (nt & 1)) * 14 + 2 * w + kh) * KB
                                   : WU_OFF + (unsigned)(((4 * cg + nt) * 16 + h) * 14 + 2 * w + kh) * KB;
          PST(off + lane * 16, pk8(a[nt * 2] * sc, a[nt * 2 + 1] * sc));
        }
        SB();
      }
    }
  }
  if (b >= 224 || w == 7) {
    constexpr int NW = 224 + 32 * 8;
    const int wid = (b < 224) ? b : 224 + (b - 224) * 8 + w;
    unsigned short gv[4][8];
#pragma unroll
    for (int it = 0; it < 4; ++it) {
      const int f = min(wid + it * NW, 1735), ks = f % 14;
      const unsigned short* src;
      unsigned stride;
      bool ok = true;
      if (f < 1344) {
        const int tl = (f / 14) % 6, a = f / 84;
        const int dh = (tl < 4) ? 8 * (i16 >> 2) + 4 * (tl & 1) + (i16 & 3) : 16 * (tl - 4) + i16;
        ok = dh < 28;
        src = wqkv + ((tl < 4) ? (tl >> 1) * 448 : 896) + 28 * a + min(dh, 27);
        stride = 1344;
      } else {
        const int g = f - 1344;
        src = wo + 32 * (g / 28) + 8 * (i16 >> 2) + 4 * ((g / 14) & 1) + (i16 & 3);
        stride = 448;
      }
      src += (size_t)(32 * ks + 8 * q) * stride;
#pragma unroll
      for (int jj = 0; jj < 8; ++jj) gv[it][jj] = src[(size_t)jj * stride];
      if (!ok) {
#pragma unroll
        for (int jj = 0; jj < 8; ++jj) gv[it][jj] = 0;
      }
    }
    v4u xv[4];
#pragma unroll
    for (int it = 0; it < 4; ++it) {
      const int f = min(wid + it * NW, 1791), sg = f / 28, tau = (f / 14) & 1, ks = f % 14;
      xv[it] = *(const v4u*)(x0 + (size_t)(sg * 32 + tok_of(tau, i16)) * 448 + 32 * ks + 8 * q);
    }
    const unsigned gtid = wid * 64 + lane, gstride = NW * 64;
    for (unsigned idx = gtid; idx < FLAG_BYTES / 16; idx += gstride) PST(idx * 16, (v4u{0u, 0u, 0u, 0u}));
    for (unsigned idx = gtid; idx < 2 * AD * REC / 16; idx += gstride) {
      const unsigned l = idx / (AD * REC / 16), r = idx % (AD * REC / 16);
      PST(REC_OFF + l * NSEG * REC + r * 16, S4);
    }
    const unsigned ring = ring_offset(ws);
    for (unsigned idx = gtid; idx < RING_BYTES / 16; idx += gstride) {
      const unsigned po = idx * 16, lo = ((po >> 14) << 13) | (po & 8191u);
      v4u o = S4;
      if (lo < L_MEMG && (lo & (64 * KB - 1)) < 14 * KB) {
        const unsigned r = lo & (64 * KB - 1), ln = (r >> 4) & 63, ks = r >> 10;
        o = ns4(*(const v4u*)(mem0 + (size_t)(ln & 7) * 448 + 32 * ks + 8 * (ln >> 4)));
      } else if (lo >= L_MEMG && lo < L_MEMG + 16 * KB && (lo & 8191u) < 7 * KB) {
        const unsigned r = lo & 8191u, ks = r >> 9, qq = (r >> 7) & 3, row = (r >> 4) & 7;
        o = ns4(*(const v4u*)(mem0 + (size_t)row * 448 + 32 * ks + 8 * qq));
      }
      PST(ring + po, o);
    }
#pragma unroll
    for (int it = 0; it < 4; ++it) {
      const int f = wid + it * NW;
      if (f < 1736) {
        v4u o;
#pragma unroll
        for (int jj = 0; jj < 4; ++jj) o[jj] = (unsigned)gv[it][2 * jj] | ((unsigned)gv[it][2 * jj + 1] << 16);
        PST(WQ_OFF + (unsigned)f * KB + lane * 16, o);
      }
      if (f < 1792) PST(X0_OFF + (unsigned)f * KB + lane * 16, xv[it]);
    }
  }
}

extern "C" void kernel_launch(void* const* d_in, const int* in_sizes, int n_in, void* d_out, int out_size, void* d_ws, size_t ws_size,
                              hipStream_t stream) {
  constexpr int LDS_BYTES = 100 * 1024;
  (void)hipFuncSetAttribute((const void*)mega_kernel, hipFuncAttributeMaxDynamicSharedMemorySize, LDS_BYTES);
  unsigned char* ws = (unsigned char*)d_ws;
  constexpr int PRO_LDS = (64 + 7 * 16) * 528;
  (void)hipFuncSetAttribute((const void*)prologue_kernel, hipFuncAttributeMaxDynamicSharedMemorySize, PRO_LDS);
  prologue_kernel<<<256, 512, PRO_LDS, stream>>>(ws, (const unsigned short*)d_in[0], (const unsigned short*)d_in[1], (const unsigned short*)d_in[2], (const unsigned short*)d_in[4],
                                          (const unsigned short*)d_in[5], (const unsigned short*)d_in[6], (const unsigned short*)d_in[7],
                                          (const unsigned short*)d_in[8]);
  mega_kernel<<<256, 256, LDS_BYTES, stream>>>(ws, (const unsigned char*)d_in[4], (__hip_bfloat16*)d_out);
}
```

```cpp
#include <hip/hip_runtime.h>
#include <hip/hip_bf16.h>

typedef unsigned v4u __attribute__((ext_vector_type(4)));
typedef unsigned v2u __attribute__((ext_vector_type(2)));
typedef float f4 __attribute__((ext_vector_type(4)));
typedef float f2 __attribute__((ext_vector_type(2)));
typedef short s8v __attribute__((ext_vector_type(8)));
typedef __bf16 b2v __attribute__((ext_vector_type(2)));
typedef __amdgpu_buffer_rsrc_t rsrc_t;
#define DEVI __device__ __forceinline__
#define RLX_AGENT __ATOMIC_RELAXED, __HIP_MEMORY_SCOPE_AGENT

#define LAG 6
#define GXS 1
#define PD 7
#define PP 0
#define CPF 71
#define D1 4
#define D2 2
#define NAP 110
#define DX 8
#define PX 6
#define PO 4
#define PZ 1
#define USTREAM 1
constexpr int NSEG = 64, NT = NSEG + LAG, AD = 4, NPH = 2;
constexpr unsigned SENT = 0xFFFFFFFFu;
constexpr unsigned SPINCAP = 1u << 20;
constexpr float LOG2E = 1.4426950408889634f;
constexpr float NEG = -3.0e38f;

constexpr unsigned KB = 1024u, MB = 1024u * 1024u;
constexpr unsigned FLAG_BYTES = 8 * KB;
constexpr unsigned F_U = 1024, F_XR = 7168;
constexpr unsigned OOB = 0x80000000u;
constexpr unsigned F_ABORT = 0, F_SIDE = 128;
constexpr unsigned RING_BYTES = 1 * MB;
constexpr unsigned L_MEMH = 0, L_MEMC = 64 * KB, L_MEMG = 128 * KB, L_S = 192 * KB, L_E = 256 * KB, L_XS = 384 * KB, L_PROBE = 504 * KB;
constexpr unsigned WG_OFF = 4 * MB;
constexpr unsigned WG_BYTES = 16 * 14 * 2 * 14 * KB;
constexpr unsigned WU_OFF = WG_OFF + WG_BYTES;
constexpr unsigned WQ_OFF = WU_OFF + WG_BYTES;
constexpr unsigned WO_OFF = WQ_OFF + 16 * 6 * 14 * KB;
constexpr unsigned SEGX = 28 * KB;
constexpr unsigned R_XN = 0, R_XR = SEGX, R_OA = 2 * SEGX, R_X2 = 3 * SEGX, R_ETT = 4 * SEGX, R_ETS = R_ETT + 32 * KB, R_STM = R_ETS + 4 * KB,
                   R_ZB = R_STM + 32 * KB, REC = R_ZB + 112 * KB;
constexpr unsigned X0_OFF = WO_OFF + 392 * KB;
constexpr unsigned REC_OFF = X0_OFF + NSEG * SEGX;
constexpr unsigned SEGG = 448 * KB;
constexpr unsigned U_OFF = REC_OFF + 2 * NSEG * REC;
constexpr unsigned WS_END = U_OFF + 2 * NSEG * SEGG;
static_assert(WS_END < 200u * MB, "workspace");

DEVI f4 mfma32(v4u a, v4u b, f4 c) {
  return __builtin_amdgcn_mfma_f32_16x16x32_bf16(__builtin_bit_cast(s8v, a), __builtin_bit_cast(s8v, b), c, 0, 0, 0);
}
DEVI unsigned pk2(float a, float b) {
  f2 v = {a, b};
  return __builtin_bit_cast(unsigned, __builtin_convertvector(v, b2v));
}
DEVI float bflo(unsigned u) { return __uint_as_float(u << 16); }
DEVI float bfhi(unsigned u) { return __uint_as_float(u & 0xFFFF0000u); }
DEVI float rbf(float a) { return bflo(pk2(a, 0.f)); }
DEVI v4u pk8(f4 a, f4 b) { return v4u{pk2(a[0], a[1]), pk2(a[2], a[3]), pk2(b[0], b[1]), pk2(b[2], b[3])}; }
DEVI v2u pk4(f4 a) { return v2u{pk2(a[0], a[1]), pk2(a[2], a[3])}; }
DEVI v4u resid8(v4u x, f4 a, f4 b) {
  const v4u y = pk8(a, b);
  return v4u{pk2(bflo(x.x) + bflo(y.x), bfhi(x.x) + bfhi(y.x)), pk2(bflo(x.y) + bflo(y.y), bfhi(x.y) + bfhi(y.y)),
             pk2(bflo(x.z) + bflo(y.z), bfhi(x.z) + bfhi(y.z)), pk2(bflo(x.w) + bflo(y.w), bfhi(x.w) + bfhi(y.w))};
}
DEVI unsigned ns(unsigned x) { return min(x, 0xFFFFFFFEu); }
DEVI v4u ns4(v4u x) { return v4u{ns(x.x), ns(x.y), ns(x.z), ns(x.w)}; }
DEVI v2u ns2(v2u x) { return v2u{ns(x.x), ns(x.y)}; }
DEVI v4u f2u(f4 x) { return v4u{__float_as_uint(x[0]), __float_as_uint(x[1]), __float_as_uint(x[2]), __float_as_uint(x[3])}; }
DEVI f4 u2f(v4u x) { return f4{__uint_as_float(x.x), __uint_as_float(x.y), __uint_as_float(x.z), __uint_as_float(x.w)}; }
DEVI unsigned mx4(unsigned m, v4u v) { return max(max(max(max(m, v.x), v.y), v.z), v.w); }
DEVI unsigned mx2(unsigned m, v2u v) { return max(max(m, v.x), v.y); }
DEVI bool anysent(unsigned m) { return __builtin_amdgcn_ballot_w64(m == SENT) != 0ull; }
DEVI float ex2(float x) { return __builtin_amdgcn_exp2f(x); }
DEVI float rcp(float x) { return __builtin_amdgcn_rcpf(x); }
DEVI float rsq(float x) { return __builtin_amdgcn_rsqf(x); }
DEVI float mx3(float a, float b, float c) { return __builtin_fmaxf(__builtin_fmaxf(a, b), c); }
DEVI float mxf4(f4 a, float m) { return mx3(a[2], a[3], mx3(a[0], a[1], m)); }
DEVI float dot2(unsigned a, unsigned b, float c) {
  return __builtin_amdgcn_fdot2_f32_bf16(__builtin_bit_cast(b2v, a), __builtin_bit_cast(b2v, b), c, false);
}
DEVI void swap16(unsigned& a, unsigned& b) { auto r = __builtin_amdgcn_permlane16_swap(a, b, false, false); a = r[0]; b = r[1]; }
DEVI void swap32(unsigned& a, unsigned& b) { auto r = __builtin_amdgcn_permlane32_swap(a, b, false, false); a = r[0]; b = r[1]; }
DEVI float rmax4(float x) {
  unsigned a = __float_as_uint(x), b = a;
  swap16(a, b);
  a = b = __float_as_uint(mx3(__uint_as_float(a), __uint_as_float(b), NEG));
  swap32(a, b);
  return mx3(__uint_as_float(a), __uint_as_float(b), NEG);
}
DEVI float rsum4(float x) {
  unsigned a = __float_as_uint(x), b = a;
  swap16(a, b);
  a = b = __float_as_uint(__uint_as_float(a) + __uint_as_float(b));
  swap32(a, b);
  return __uint_as_float(a) + __uint_as_float(b);
}
DEVI float ror8(float x) { return __uint_as_float(__builtin_amdgcn_update_dpp(0u, __float_as_uint(x), 0x128, 0xF, 0xF, false)); }
DEVI void rbcast4(float x, float* o) {
  unsigned a = __float_as_uint(x), b = a;
  swap16(a, b);
  unsigned a2 = a, b2 = b;
  swap32(a, a2);
  swap32(b, b2);
  o[0] = __uint_as_float(a); o[1] = __uint_as_float(b); o[2] = __uint_as_float(a2); o[3] = __uint_as_float(b2);
}

DEVI v4u ld16(rsrc_t r, unsigned off) { return __builtin_amdgcn_raw_buffer_load_b128(r, off, 0, 0); }
DEVI v4u ld16c(rsrc_t r, unsigned off) { return __builtin_amdgcn_raw_buffer_load_b128(r, off, 0, 16); }
DEVI v2u ld8c(rsrc_t r, unsigned off) { return __builtin_amdgcn_raw_buffer_load_b64(r, off, 0, 16); }
DEVI unsigned ld4c(rsrc_t r, unsigned off) { return __builtin_amdgcn_raw_buffer_load_b32(r, off, 0, 16); }
DEVI void st16c(rsrc_t r, unsigned off, v4u v) { __builtin_amdgcn_raw_buffer_store_b128(v, r, off, 0, 16); }
DEVI void st8c(rsrc_t r, unsigned off, v2u v) { __builtin_amdgcn_raw_buffer_store_b64(v, r, off, 0, 16); }
DEVI void st4c(rsrc_t r, unsigned off, unsigned v) { __builtin_amdgcn_raw_buffer_store_b32(v, r, off, 0, 16); }
#define PIN_A(x) asm volatile("" : "+a"(x))
#define TOUCH(x) asm volatile("" :: "v"(x))
#define PIN_V(x) asm volatile("" : "+v"(x))
#define DRAIN() asm volatile("s_waitcnt vmcnt(0)" ::: "memory")
#define CBAR() asm volatile("" ::: "memory")
#define SB() __builtin_amdgcn_sched_barrier(0)
#define BAR() do { asm volatile("s_waitcnt lgkmcnt(0)" ::: "memory"); __builtin_amdgcn_s_barrier(); asm volatile("" ::: "memory"); } while (0)
#define LDS16(p) (*(v4u*)(p))
#define LDSF4(p) (*(f4*)(p))
#define S4 (v4u{SENT, SENT, SENT, SENT})
#define S2 (v2u{SENT, SENT})
#define Z4 (f4{0.f, 0.f, 0.f, 0.f})

#define STAMP(it, e) do { } while (0)
#define PMASK(m) do { } while (0)
#define STAMPC(it, e) do { } while (0)
struct Ctx {
  unsigned char* ws;
  rsrc_t rs;
  unsigned ring;
  int tid, lane, wave;
  bool dead;
};
DEVI bool give_up(Ctx& c, unsigned& spins) {
  if (c.dead) return true;
  ++spins;
  bool d = false;
  if (spins > SPINCAP) { __hip_atomic_store((unsigned*)(c.ws + F_ABORT), 1u, RLX_AGENT); d = true; }
  else if ((spins & 255u) == 0u) d = __builtin_amdgcn_readfirstlane(__hip_atomic_load((unsigned*)(c.ws + F_ABORT), RLX_AGENT)) != 0u;
  c.dead = d;
  return d;
}
#define POLL2(FIRST, BADEXPR, RELOAD, SLEEP) do { if (FIRST) { unsigned spins_ = 0; do { if (give_up(c, spins_)) break; if (SLEEP) __builtin_amdgcn_s_sleep(SLEEP); CBAR(); RELOAD; } while (BADEXPR); } } while (0)
#define POLL(BADEXPR, RELOAD, SLEEP) do { unsigned spins_ = 0; while (BADEXPR) { if (give_up(c, spins_)) break; if (SLEEP) __builtin_amdgcn_s_sleep(SLEEP); CBAR(); RELOAD; } } while (0)

template <class LD>
DEVI void poll2(Ctx& c, v4u* r, LD ld) {
  v4u a[4] = {r[0], r[1], r[2], r[3]}, b[4];
  unsigned spins = 0;
  __builtin_amdgcn_s_sleep(PD);
  while (true) {
    CBAR();
    ld(b);
    SB();
    if (!anysent(mx4(mx4(mx4(mx4(0u, a[0]), a[1]), a[2]), a[3])) || give_up(c, spins)) {
#pragma unroll
      for (int k = 0; k < 4; ++k) r[k] = a[k];
      break;
    }
    CBAR();
    ld(a);
    SB();
    if (!anysent(mx4(mx4(mx4(mx4(0u, b[0]), b[1]), b[2]), b[3]))) {
#pragma unroll
      for (int k = 0; k < 4; ++k) r[k] = b[k];
      break;
    }
  }
}

DEVI unsigned rmap(const Ctx& c, unsigned lo, unsigned side) { return c.ring + ((lo >> 13) << 14) + (side << 13) + (lo & 8191u); }
DEVI unsigned rec_off(int l, int s) { return REC_OFF + (unsigned)(l * NSEG + s) * REC; }
DEVI unsigned rec_in(int l, int s) { return (s >= 0 && s < NSEG) ? rec_off(l, s) : OOB; }
DEVI unsigned u_in(int l, int s) { return (s >= 0 && s < NSEG) ? U_OFF + (unsigned)(l * NSEG + s) * SEGG : OOB; }
DEVI int tok_of(int tau, int m) { return 8 * (m >> 2) + 4 * tau + (m & 3); }

DEVI void probe_side(Ctx& c, int k) {
  if (c.wave != 0) return;
  unsigned best[2] = {0xFFFFFFFFu, 0xFFFFFFFFu};
#pragma unroll 1
  for (int it = 0; it < 4; ++it) {
#pragma unroll
    for (int sd = 0; sd < 2; ++sd) {
      DRAIN();
      const unsigned t0 = (unsigned)__builtin_amdgcn_s_memtime();
      if (c.lane < 8) st16c(c.rs, rmap(c, L_PROBE + k * 256 + c.lane * 16, sd), S4);
      DRAIN();
      const unsigned t1 = (unsigned)__builtin_amdgcn_s_memtime();
      best[sd] = min(best[sd], t1 - t0);
    }
  }
  if (c.lane == 0) st4c(c.rs, F_SIDE + k * 64, best[1] < best[0] ? 2u : 1u);
}
DEVI unsigned get_sides(Ctx& c) {
  const unsigned o = F_SIDE + (c.lane & 7) * 64;
  unsigned v = ld4c(c.rs, o);
  POLL(__builtin_amdgcn_ballot_w64(v == 0u) != 0ull, v = ld4c(c.rs, o), 2);
  return (__builtin_popcountll(__builtin_amdgcn_ballot_w64(v == 2u)) > 32) ? 0u : 1u;
}

DEVI void role_H(Ctx& c, int h, int a, unsigned char* lds) {
  PMASK(1);
  const int lane = c.lane, w = c.wave;
  const int i16 = lane & 15, q = lane >> 4;
  unsigned char* lmem = lds;
  unsigned char* lred = lds + 8 * KB;
  unsigned char* ltb = lds + 24 * KB;
  const int odd = w & 1;
  const int ks0 = (w >> 1) * 7 + odd * 4, ksx = (w >> 1) * 7 + 3;
  v4u W[42], Wx[8];
#pragma unroll
  for (int lp = 0; lp < 7; ++lp) {
    const int gp = odd ? 6 - lp : lp;
    const unsigned b0 = WG_OFF + (unsigned)(h * 14 + 7 * a + gp) * 28 * KB + lane * 16;
#pragma unroll
    for (int t = 0; t < 2; ++t)
#pragma unroll
      for (int k = 0; k < 3; ++k) W[(lp * 2 + t) * 3 + k] = ld16(c.rs, b0 + (t * 14 + ks0 + k) * KB);
    if (lp < 4) {
#pragma unroll
      for (int t = 0; t < 2; ++t) {
        Wx[lp * 2 + t] = ld16(c.rs, b0 + (t * 14 + ksx) * KB);
        if (lp == 3 && t != odd) Wx[lp * 2 + t] = v4u{0u, 0u, 0u, 0u};
      }
    }
  }
#pragma unroll
  for (int f = 0; f < 42; ++f) PIN_A(W[f]);
#pragma unroll
  for (int f = 0; f < 8; ++f) PIN_A(Wx[f]);
  const unsigned sides = get_sides(c);
  unsigned mo[4];
#pragma unroll
  for (int k = 0; k < 4; ++k) mo[k] = rmap(c, L_MEMH + (k < 3 ? ks0 + k : ksx) * KB + lane * 16, sides);
  const unsigned so0 = rmap(c, L_S + ((a * 2 + (q & 1)) * 16 + h) * 256 + i16 * 16, sides);
  const bool sval = (q >> 1) == (i16 >> 3);
  const bool stager = (w >> 1) == a;
  const int gp1 = (w < 3) ? w + 4 : 3;
  v4u X[8];
  auto load_x = [&](int t) {
#pragma unroll
    for (int l = 0; l < 2; ++l) {
      const unsigned xb = rec_in(l, t - l * LAG) + R_XN + (unsigned)(7 * a) * KB + lane * 16;
#pragma unroll
      for (int tau = 0; tau < 2; ++tau) {
        X[(l * 2 + tau) * 2 + 0] = ld16c(c.rs, xb + (tau * 14 + w) * KB);
        X[(l * 2 + tau) * 2 + 1] = ld16c(c.rs, xb + (tau * 14 + gp1) * KB);
      }
    }
  };
  CBAR();
  load_x(0);
  for (int t = 0; t < NT; ++t) {
    const unsigned slot = (unsigned)(t & 3) * 32 * KB;
    STAMP(t, 0);
    v4u m[4];
    CBAR();
#pragma unroll
    for (int k = 0; k < 4; ++k) m[k] = ld16c(c.rs, mo[k] + slot);
    POLL(anysent(mx4(mx4(mx4(mx4(0u, m[0]), m[1]), m[2]), m[3])),
         { _Pragma("unroll") for (int k = 0; k < 4; ++k) m[k] = ld16c(c.rs, mo[k] + slot); }, 0);
    STAMP(t, 1);
    if (stager) {
#pragma unroll
      for (int k = 0; k < 4; ++k) LDS16(lmem + ((k < 3 ? ks0 + k : ksx) - 7 * a) * KB + lane * 16) = m[k];
    }
    SB();
    v4u tb[7], mu[7];
    f4 acc[7][2];
    f4 sacc = Z4;
#pragma unroll
    for (int lp = 0; lp < 7; ++lp) {
#define HPK(e) do { if (lp >= 2) { SB(); tb[lp - 2][e] = pk2(acc[lp - 2][(e) >> 1][((e) & 1) * 2], acc[lp - 2][(e) >> 1][((e) & 1) * 2 + 1]); SB(); } } while (0)
      acc[lp][0] = mfma32(W[(lp * 2 + 0) * 3 + 0], m[0], Z4);
      HPK(0);
      acc[lp][1] = mfma32(W[(lp * 2 + 1) * 3 + 0], m[0], Z4);
      HPK(1);
      acc[lp][0] = mfma32(W[(lp * 2 + 0) * 3 + 1], m[1], acc[lp][0]);
      HPK(2);
      acc[lp][1] = mfma32(W[(lp * 2 + 1) * 3 + 1], m[1], acc[lp][1]);
      HPK(3);
      acc[lp][0] = mfma32(W[(lp * 2 + 0) * 3 + 2], m[2], acc[lp][0]);
      if (lp == 6) { SB(); tb[5][0] = pk2(acc[5][0][0], acc[5][0][1]); SB(); }
      acc[lp][1] = mfma32(W[(lp * 2 + 1) * 3 + 2], m[2], acc[lp][1]);
      if (lp == 6) { SB(); tb[5][1] = pk2(acc[5][0][2], acc[5][0][3]); SB(); }
      if (lp < 4) {
        acc[lp][0] = mfma32(Wx[lp * 2 + 0], m[3], acc[lp][0]);
        acc[lp][1] = mfma32(Wx[lp * 2 + 1], m[3], acc[lp][1]);
      }
      if (lp >= 2) sacc = mfma32(tb[lp - 2], mu[lp - 2], sacc);
      SB();
      if (lp == 0) {
        BAR();
#pragma unroll
        for (int k = 0; k < 7; ++k) mu[k] = LDS16(lmem + (odd ? 6 - k : k) * KB + lane * 16);
        SB();
      }
    }
    tb[5][2] = pk2(acc[5][1][0], acc[5][1][1]);
    tb[5][3] = pk2(acc[5][1][2], acc[5][1][3]);
    sacc = mfma32(tb[5], mu[5], sacc);
    SB();
    tb[6] = pk8(acc[6][0], acc[6][1]);
    sacc = mfma32(tb[6], mu[6], sacc);
    if (w != 0) LDSF4(lred + w * KB + lane * 16) = sacc;
    BAR();
    if (w == 0) {
      const f4 s = (sacc + LDSF4(lred + KB + lane * 16)) + (LDSF4(lred + 2 * KB + lane * 16) + LDSF4(lred + 3 * KB + lane * 16));
      if (sval) {
        st16c(c.rs, so0 + slot, ns4(f2u(s)));
        if (t >= 1) st16c(c.rs, so0 + (unsigned)((t - 1) & 3) * 32 * KB, S4);
      }
    }
    STAMP(t, 2);
#pragma unroll
    for (int lp = 0; lp < 7; ++lp) LDS16(ltb + (w * 7 + (odd ? 6 - lp : lp)) * KB + lane * 16) = tb[lp];
    BAR();
    v4u tp[8];
#pragma unroll
    for (int ww = 0; ww < 4; ++ww) {
      tp[ww * 2 + 0] = LDS16(ltb + (ww * 7 + w) * KB + lane * 16);
      tp[ww * 2 + 1] = LDS16(ltb + (ww * 7 + gp1) * KB + lane * 16);
      if (w == 3) tp[ww * 2 + 1] = v4u{0u, 0u, 0u, 0u};
    }
    POLL(anysent(mx4(mx4(mx4(mx4(mx4(mx4(mx4(mx4(0u, X[0]), X[1]), X[2]), X[3]), X[4]), X[5]), X[6]), X[7])), load_x(t), 1);
    SB();
    {
      f4 s[4] = {Z4, Z4, Z4, Z4};
#pragma unroll
      for (int k = 0; k < 8; ++k)
#pragma unroll
        for (int lt = 0; lt < 4; ++lt) s[lt] = mfma32(tp[k], X[lt * 2 + (k & 1)], s[lt]);
#pragma unroll
      for (int lt = 0; lt < 4; ++lt) LDSF4(lred + (lt * 4 + w) * KB + lane * 16) = s[lt];
    }
    BAR();
    {
      const int l = w >> 1, tau = w & 1, s = t - l * LAG;
      const f4 v = (LDSF4(lred + (w * 4 + 0) * KB + lane * 16) + LDSF4(lred + (w * 4 + 1) * KB + lane * 16)) +
                   (LDSF4(lred + (w * 4 + 2) * KB + lane * 16) + LDSF4(lred + (w * 4 + 3) * KB + lane * 16));
      if ((q >> 1) == l) {
        const unsigned o = R_STM + (unsigned)((((a * 2 + (q & 1)) * 2 + tau) * 16 + h) * 256 + i16 * 16);
        st16c(c.rs, rec_in(l, s) + o, ns4(f2u(v)));
        if (s >= 0) st16c(c.rs, rec_in(l, s + AD) + o, S4);
      }
    }
    CBAR();
    load_x(t + 1);
    STAMP(t, 3);
  }
}

DEVI void role_CV(Ctx& c, int j, int p, unsigned char* lds, const unsigned char* mem0) {
  PMASK(67);
  const int lane = c.lane, w = c.wave;
  const int i16 = lane & 15, q = lane >> 4;
  unsigned char* lmem = lds;
  unsigned char* lred = lds + 16 * KB;
  unsigned char* lXb = lds + 20 * KB;
  v4u W[56];
  {
    const unsigned b0 = WU_OFF + (unsigned)(j * 16 + 4 * w) * 14 * KB + lane * 16;
#pragma unroll
    for (int f = 0; f < 56; ++f) W[f] = ld16(c.rs, b0 + f * KB);
#pragma unroll
    for (int f = 0; f < 56; ++f) PIN_A(W[f]);
  }
  const v2u m0 = ns2(*(const v2u*)(mem0 + (size_t)(i16 & 7) * 896 + (j * 16 + 4 * q) * 2));
  const unsigned sides = get_sides(c);
  const unsigned sC = sides;
  unsigned mo[4], so[4], eo[4];
#pragma unroll
  for (int k = 0; k < 4; ++k) {
    mo[k] = rmap(c, L_MEMC + (k < 3 ? k * 4096 + c.tid * 16 : 12288 + (c.tid & 127) * 16), sC);
    so[k] = rmap(c, L_S + (k * 16 + 4 * w) * 256 + lane * 16, sC);
    eo[k] = rmap(c, L_E + (4 * w + k) * KB + lane * 16, sC);
  }
  const unsigned xo = rmap(c, L_XS + ((4 * w + q) * 2 + (i16 >> 3)) * 128 + (i16 & 7) * 8, sC);
  const unsigned pf = (unsigned)(j >> 1) * KB + (unsigned)((2 * (j & 1) + (q >> 1)) * 16 + i16) * 16 + (q & 1) * 8;
  const unsigned pg = (unsigned)(i16 >> 3) * 8 * KB + (unsigned)(j >> 1) * 512 + (unsigned)(2 * (j & 1) + (q >> 1)) * 128 + (i16 & 7) * 16 + (q & 1) * 8;
  const unsigned pH = rmap(c, L_MEMH + pf, sides), pC = rmap(c, L_MEMC + pf, sides), pG = rmap(c, L_MEMG + pg, sides ^ (unsigned)(p ^ 1));
  const unsigned pR = (w == 1) ? pH : (w == 2) ? pG : (w == 3) ? pC : OOB;
  const unsigned zo = R_ZB + (unsigned)(j * 4 + w) * KB + lane * 16;
  const unsigned uo = (unsigned)(j * 16 + 4 * w) * KB + lane * 16;
  const unsigned fo = (lane == 0) ? F_U + (unsigned)(p * 28 + j) * 64 + w * 4 : OOB;
#define XBAD anysent(mx4(mx4(mx4(mx4(mx4(mx4(mx4(0u, x[0]), x[1]), x[2]), x[3]), x[4]), x[5]), x[6]))
  v4u U[8], x[7];
  auto load_x = [&](int l, int s) {
    const unsigned xb = rec_in(l, s) + R_XN + c.tid * 16;
#pragma unroll
    for (int f = 0; f < 7; ++f) x[f] = ld16c(c.rs, xb + f * 4096);
  };
  auto stage_x = [&](int l) {
#pragma unroll
    for (int f = 0; f < 7; ++f) LDS16(lXb + l * 28 * KB + f * 4096 + c.tid * 16) = x[f];
  };
  unsigned* lhave = (unsigned*)(lds + 15 * KB);
  bool have = false, allhave = false;
  for (int t = p; t < NT; t += 2) {
    const unsigned slot = (unsigned)(t & 3) * 32 * KB, eslot = (unsigned)(t & 7) * 32 * KB, xslot = (unsigned)(t & 7) * 16 * KB;
    const int s0 = t, s1 = t - LAG;
    STAMP(t, 0);
    if (!allhave) {
      if (!have) {
        CBAR();
        load_x(0, s0);
        POLL(XBAD, load_x(0, s0), 1);
        stage_x(0);
      }
      BAR();
    }
    CBAR();
    load_x(1, s1);
    SB();
    f4 acc[8];
    const unsigned ub0 = u_in(0, s0) + uo, za0 = rec_in(0, s0 + AD) + zo, xb0 = rec_in(0, s0 + 2) + R_XN + c.tid * 16;
    auto side_op = [&](int k) {
      if (k < 4) st16c(c.rs, ub0 + k * KB, U[k]);
      else if (k == 4) st16c(c.rs, za0, S4);
      else if (k < 12) x[k - 5] = ld16c(c.rs, xb0 + (k - 5) * 4096);
    };
#pragma unroll
    for (int l = 0; l < 2; ++l) {
      const int s = l ? s1 : s0;
      const bool act = (s >= 0 && s < NSEG);
#pragma unroll
      for (int k = 0; k < 8; ++k) acc[k] = Z4;
      if (act) {
        const unsigned char* xp = lXb + l * 28 * KB + lane * 16;
        v4u xa = LDS16(xp), xb = LDS16(xp + 14 * KB), ya = LDS16(xp + KB), yb = LDS16(xp + 15 * KB);
        SB();
#pragma unroll
        for (int ks = 0; ks < 14; ++ks) {
          const v4u x0 = xa, x1 = xb;
          xa = ya; xb = yb;
          if (ks + 2 < 14) { ya = LDS16(xp + (ks + 2) * KB); yb = LDS16(xp + (16 + ks) * KB); }
#pragma unroll
          for (int hh = 0; hh < 4; ++hh) {
            acc[hh * 2 + 0] = mfma32(x0, W[hh * 14 + ks], acc[hh * 2 + 0]);
            acc[hh * 2 + 1] = mfma32(x1, W[hh * 14 + ks], acc[hh * 2 + 1]);
            if (l == 1 && hh == 1) { SB(); side_op(ks); SB(); }
          }
          SB();
        }
      }
      else if (l == 1) {
#pragma unroll
        for (int k = 0; k < 12; ++k) side_op(k);
      }
      if (l == 0) {
        const bool bad = XBAD;
#pragma unroll
        for (int f = 0; f < 7; ++f) {
          LDS16(lXb + 28 * KB + f * 4096 + c.tid * 16) = x[f];
          if (f < 4) { U[f] = pk8(acc[f * 2], acc[f * 2 + 1]); PIN_V(U[f]); }
          SB();
        }
        if (bad) {
          POLL2(true, XBAD, load_x(1, s1), 1);
          stage_x(1);
        }
        CBAR();
        BAR();
        SB();
      }
    }
    STAMP(t, 1);
    v4u m[4];
    CBAR();
#pragma unroll
    for (int k = 0; k < 4; ++k) m[k] = ld16c(c.rs, mo[k] + slot);
    SB();
#pragma unroll
    for (int hh = 0; hh < 4; ++hh) { U[4 + hh] = pk8(acc[hh * 2], acc[hh * 2 + 1]); PIN_V(U[4 + hh]); }
    SB();
    have = !XBAD;
    if (lane == 0) lhave[w] = have ? 1u : 0u;
    SB();
    POLL(anysent(mx4(mx4(mx4(mx4(0u, m[0]), m[1]), m[2]), m[3])),
         { _Pragma("unroll") for (int k = 0; k < 4; ++k) m[k] = ld16c(c.rs, mo[k] + slot); }, 0);
#pragma unroll
    for (int k = 0; k < 3; ++k) LDS16(lmem + k * 4096 + c.tid * 16) = m[k];
    LDS16(lmem + 12288 + (c.tid & 127) * 16) = m[3];
    DRAIN();
    st4c(c.rs, fo, (unsigned)min(s0 + 1, NSEG));
    {
      const unsigned ub = u_in(1, s1) + uo;
#pragma unroll
      for (int hh = 0; hh < 4; ++hh) st16c(c.rs, ub + hh * KB, U[4 + hh]);
      st16c(c.rs, (s1 >= 0 ? rec_in(1, s1 + AD) : OOB) + zo, S4);
    }
    STAMP(t, 2);
    BAR();
    {
      const v4u hv = LDS16((unsigned char*)lhave);
      allhave = __builtin_amdgcn_readfirstlane(hv.x & hv.y & hv.z & hv.w) != 0u;
    }
    if (t >= 1) st8c(c.rs, pR + (unsigned)((t - 1) & 3) * 32 * KB, S2);
    v4u zA[2];
    {
      v4u mf[14];
#pragma unroll
      for (int ks = 0; ks < 14; ++ks) mf[ks] = LDS16(lmem + ks * KB + lane * 16);
      SB();
      f4 z[4] = {Z4, Z4, Z4, Z4};
#pragma unroll
      for (int ks = 0; ks < 14; ++ks) {
#pragma unroll
        for (int hh = 0; hh < 4; ++hh) z[hh] = mfma32(mf[ks], W[hh * 14 + ks], z[hh]);
        SB();
      }
      unsigned zl[4], zh[4];
#pragma unroll
      for (int hh = 0; hh < 4; ++hh) { zl[hh] = pk2(z[hh][0], z[hh][1]); zh[hh] = pk2(z[hh][2], z[hh][3]); }
      swap16(zl[0], zl[1]); swap16(zl[2], zl[3]); swap32(zl[0], zl[2]); swap32(zl[1], zl[3]);
      swap16(zh[0], zh[1]); swap16(zh[2], zh[3]); swap32(zh[0], zh[2]); swap32(zh[1], zh[3]);
      zA[0] = v4u{zl[0], zh[0], zl[1], zh[1]};
      zA[1] = v4u{zl[2], zh[2], zl[3], zh[3]};
    }
    STAMP(t, 3);
    if (t == NT - 1) {
      CBAR();
      st16c(c.rs, rec_in(1, s1) + zo, ns4(zA[1]));
      asm volatile("s_waitcnt vmcnt(1)" ::: "memory");
      st4c(c.rs, fo + 16, (unsigned)NSEG);
      STAMP(t, 6);
      break;
    }
    v4u E[4], sv[4];
    v2u xs;
    auto load_e = [&]() {
#pragma unroll
      for (int hh = 0; hh < 4; ++hh) E[hh] = ld16c(c.rs, eo[hh] + eslot);
      xs = ld8c(c.rs, xo + xslot);
    };
    auto load_s = [&]() {
#pragma unroll
      for (int k = 0; k < 4; ++k) sv[k] = ld16c(c.rs, so[k] + slot);
    };
    CBAR();
    load_e();
    SB();
    stage_x(0);
    SB();
    load_s();
    POLL(anysent(mx2(mx4(mx4(mx4(mx4(0u, E[0]), E[1]), E[2]), E[3]), xs)), { load_e(); load_s(); }, 0);
    SB();
    f4 ox[4];
#pragma unroll
    for (int hh = 0; hh < 4; ++hh) {
      const f4 o0 = mfma32(U[hh], E[hh], Z4), o1 = mfma32(U[4 + hh], E[hh], Z4);
      ox[hh] = (i16 < 8) ? o0 : o1;
    }
#pragma unroll
    for (int hh = 0; hh < 4; ++hh) PIN_V(ox[hh]);
    STAMP(t, 4);
    SB();
    POLL(anysent(mx4(mx4(mx4(mx4(0u, sv[0]), sv[1]), sv[2]), sv[3])), load_s(), 0);
    STAMP(t, 5);
    DRAIN();
    st4c(c.rs, fo + 16, (unsigned)max(s1 + 1, 0));
    {
      f4 a = u2f(sv[0]) + u2f(sv[2]), b = u2f(sv[1]) + u2f(sv[3]);
      const float mxx = __uint_as_float(xs.x), lx = __uint_as_float(xs.y);
      const float mm = mxf4(b, mxf4(a, mxx));
      const float ml = mm * LOG2E;
#pragma unroll
      for (int r = 0; r < 4; ++r) { a[r] = ex2(a[r] * LOG2E - ml); b[r] = ex2(b[r] * LOG2E - ml); }
      const float ax = ex2(mxx * LOG2E - ml);
      const float den = ((a[0] + a[1]) + (a[2] + a[3])) + ((b[0] + b[1]) + (b[2] + b[3])) + ax * lx;
      const float inv = rcp(den);
      const v4u P = pk8(a * inv, b * inv);
      const f4 d0 = mfma32(zA[0], P, Z4), d1 = mfma32(zA[1], P, Z4);
      float wx[4];
      rbcast4(ax * inv, wx);
      f4 acc = (ox[0] * wx[0] + ox[1] * wx[1]) + (ox[2] * wx[2] + ox[3] * wx[3]);
      acc += (i16 < 8) ? d0 : d1;
      if (w != 0) LDSF4(lred + w * KB + lane * 16) = acc;
      BAR();
      if (w == 0) {
        acc = (acc + LDSF4(lred + KB + lane * 16)) + (LDSF4(lred + 2 * KB + lane * 16) + LDSF4(lred + 3 * KB + lane * 16));
        v2u o = ns2(pk4(acc));
        if (i16 >= 8 && t + 1 <= LAG) o = m0;
        const unsigned nslot = (unsigned)((t + 1) & 3) * 32 * KB;
        st8c(c.rs, pH + nslot, o);
        st8c(c.rs, pG + nslot, o);
        st8c(c.rs, pC + nslot, o);
      }
      STAMP(t, 6);
      st16c(c.rs, rec_in(0, s0) + zo, ns4(zA[0]));
      st16c(c.rs, rec_in(1, s1) + zo, ns4(zA[1]));
    }
  }
}

template <int L>
DEVI void role_GX(Ctx& c, int h, int p, unsigned char* lds) {
  PMASK(5);
  asm volatile("; layer %2" : "+v"(c.tid), "+v"(c.lane) : "n"(L) : "memory");
  const int lane = c.lane, w = c.wave;
  const int i16 = lane & 15, q = lane >> 4;
  unsigned char* lX = lds;
  unsigned char* lG = lds + 28 * KB;
  unsigned char* lred = lds + 60 * KB;
  v4u W[98];
  {
    const unsigned b0 = WG_OFF + (unsigned)(h * 14 + 3 * w) * 28 * KB + lane * 16;
#pragma unroll
    for (int f = 0; f < 84; ++f) W[f] = ld16(c.rs, b0 + f * KB);
    const unsigned b1 = WG_OFF + (unsigned)(h * 14 + 12 + (w >> 1)) * 28 * KB + (w & 1) * 7 * KB + lane * 16;
#pragma unroll
    for (int k = 0; k < 7; ++k) { W[84 + k] = ld16(c.rs, b1 + k * KB); W[91 + k] = ld16(c.rs, b1 + (14 + k) * KB); }
#pragma unroll
    for (int f = 0; f < 56; ++f) PIN_A(W[f]);
  }
  const unsigned sides = get_sides(c);
  const unsigned sC = sides;
  const int kx = 7 * (w & 1);
  unsigned mo[4];
#pragma unroll
  for (int k = 0; k < 4; ++k) {
    const int vu = 4 * w + k, ks = vu < 12 ? vu : 12 + ((vu - 12) >> 1);
    mo[k] = rmap(c, L_MEMG + L * 8 * KB + ks * 512 + q * 128 + (i16 & 7) * 16, sides ^ (unsigned)p);
  }
  const unsigned eo = rmap(c, L_E + h * KB + (q * 16 + L * 8 + (i16 & 7)) * 16 + (i16 >> 3) * 8, sC);
  const unsigned xo = rmap(c, L_XS + (h * 2 + L) * 128 + (i16 & 7) * 8, sC);
  v4u x[7];
  auto load_x = [&](int s) {
    const unsigned xb = rec_in(L, s) + R_XN + c.tid * 16;
#pragma unroll
    for (int f = 0; f < 7; ++f) x[f] = ld16c(c.rs, xb + f * 4096);
  };
  bool have = false;
  for (int t = p; t < NT; t += 2) {
    const int s = t - L * LAG;
    const bool act = (s >= 0 && s < NSEG);
    v4u g[8];
    STAMP(t, 0);
    if (act) {
      if (!have) {
        CBAR();
        load_x(s);
        POLL(anysent(mx4(mx4(mx4(mx4(mx4(mx4(mx4(0u, x[0]), x[1]), x[2]), x[3]), x[4]), x[5]), x[6])), load_x(s), 1);
      }
#pragma unroll
      for (int f = 0; f < 7; ++f) LDS16(lX + f * 4096 + c.tid * 16) = x[f];
      BAR();
      STAMP(t, 1);
#pragma unroll 1
      for (int tau = 0; tau < 2; ++tau) {
        const unsigned char* xp = lX + tau * 14 * KB + lane * 16;
        const unsigned xnext = (tau ? rec_in(L, s + 2) : OOB) + R_XN + c.tid * 16;
        f4 acc[8] = {Z4, Z4, Z4, Z4, Z4, Z4, Z4, Z4};
        v4u xs[7];
#pragma unroll
        for (int k = 0; k < 7; ++k) xs[k] = LDS16(xp + (kx + k) * KB);
        v4u x0 = LDS16(xp), x1 = LDS16(xp + KB);
        SB();
#pragma unroll
        for (int k = 0; k < 7; ++k) {
          acc[6] = mfma32(W[84 + k], xs[k], acc[6]);
          acc[7] = mfma32(W[91 + k], xs[k], acc[7]);
        }
        SB();
        unsigned char* gp = lG + tau * 16 * KB + lane * 16;
#pragma unroll
        for (int ks = 0; ks < 14; ++ks) {
          const v4u xk = x0;
          x0 = x1;
          if (ks + 2 < 14) x1 = LDS16(xp + (ks + 2) * KB);
          if (ks == 2) LDS16(gp + (12 + w) * KB) = pk8(acc[6], acc[7]);
#pragma unroll
          for (int pp = 0; pp < 3; ++pp) {
            acc[pp * 2 + 0] = mfma32(W[pp * 28 + ks], xk, acc[pp * 2 + 0]);
            acc[pp * 2 + 1] = mfma32(W[pp * 28 + 14 + ks], xk, acc[pp * 2 + 1]);
            if (L == 0 && pp == 0 && ks >= 3 && ks < 10) { SB(); x[ks - 3] = ld16c(c.rs, xnext + (ks - 3) * 4096); SB(); }
          }
          SB();
        }
#pragma unroll
        for (int pp = 0; pp < 3; ++pp) LDS16(gp + (3 * w + pp) * KB) = pk8(acc[pp * 2], acc[pp * 2 + 1]);
      }
      BAR();
#pragma unroll
      for (int k = 0; k < 4; ++k) { g[k] = LDS16(lG + (4 * w + k) * KB + lane * 16); g[4 + k] = LDS16(lG + (16 + 4 * w + k) * KB + lane * 16); }
    } else {
#pragma unroll
      for (int k = 0; k < 8; ++k) g[k] = v4u{0u, 0u, 0u, 0u};
    }
    STAMP(t, 2);
    {
      const unsigned slot = (unsigned)(t & 3) * 32 * KB;
      v4u m[4];
      CBAR();
#pragma unroll
      for (int k = 0; k < 4; ++k) m[k] = ld16c(c.rs, mo[k] + slot);
      POLL(anysent(mx4(mx4(mx4(mx4(0u, m[0]), m[1]), m[2]), m[3])),
           { _Pragma("unroll") for (int k = 0; k < 4; ++k) m[k] = ld16c(c.rs, mo[k] + slot); }, 0);
      STAMP(t, 3);
      f4 a0 = Z4, a1 = Z4;
#pragma unroll
      for (int k = 0; k < 4; ++k) { a0 = mfma32(g[k], m[k], a0); a1 = mfma32(g[4 + k], m[k], a1); }
      LDSF4(lred + 4 * KB + w * KB + lane * 16) = (i16 < 8) ? a0 : a1;
      BAR();
      if (w == 0) {
        const unsigned char* rp = lred + 4 * KB + lane * 16;
        f4 v = (LDSF4(rp) + LDSF4(rp + KB)) + (LDSF4(rp + 2 * KB) + LDSF4(rp + 3 * KB));
        float m1 = mxf4(v, NEG);
        m1 = mx3(m1, ror8(m1), NEG);
        m1 = rmax4(m1);
        const float ml = m1 * LOG2E;
#pragma unroll
        for (int r = 0; r < 4; ++r) v[r] = ex2(v[r] * LOG2E - ml);
        const unsigned eslot = (unsigned)(t & 7) * 32 * KB, xslot = (unsigned)(t & 7) * 16 * KB;
        st8c(c.rs, eo + eslot, act ? ns2(pk4(v)) : v2u{0u, 0u});
        float ls = (v[0] + v[1]) + (v[2] + v[3]);
        ls += ror8(ls);
        ls = rsum4(ls);
        if (lane < 8) st8c(c.rs, xo + xslot, act ? ns2(v2u{__float_as_uint(m1), __float_as_uint(ls)}) : v2u{__float_as_uint(-1.0e30f), 0u});
        if (t >= 2) {
          st8c(c.rs, eo + (unsigned)((t - 2) & 7) * 32 * KB, S2);
          if (lane < 8) st8c(c.rs, xo + (unsigned)((t - 2) & 7) * 16 * KB, S2);
        }
      }
      STAMP(t, 4);
    }
    if (act) {
      const int tq = w & 1, kh = w >> 1;
      f4 a0 = Z4, a1 = Z4;
#pragma unroll
      for (int hf = 0; hf < 3; ++hf) {
        v4u ga[3], gb[3], xq[3];
#pragma unroll
        for (int k = 0; k < 3; ++k)
          if (hf * 3 + k < 8) {
            const int vu = kh * 8 + hf * 3 + k, ks = vu < 12 ? vu : 12 + ((vu - 12) >> 1);
            ga[k] = LDS16(lG + vu * KB + lane * 16);
            gb[k] = LDS16(lG + (16 + vu) * KB + lane * 16);
            xq[k] = LDS16(lX + (tq * 14 + ks) * KB + lane * 16);
          }
        SB();
#pragma unroll
        for (int k = 0; k < 3; ++k)
          if (hf * 3 + k < 8) { a0 = mfma32(ga[k], xq[k], a0); a1 = mfma32(gb[k], xq[k], a1); }
        SB();
      }
      if (!kh) { LDSF4(lred + (tq * 2 + 0) * KB + lane * 16) = a0; LDSF4(lred + (tq * 2 + 1) * KB + lane * 16) = a1; }
      BAR();
      if (L == 0) have = !anysent(mx4(mx4(mx4(mx4(mx4(mx4(mx4(0u, x[0]), x[1]), x[2]), x[3]), x[4]), x[5]), x[6]));
      if (kh) {
        a0 += LDSF4(lred + (tq * 2 + 0) * KB + lane * 16);
        a1 += LDSF4(lred + (tq * 2 + 1) * KB + lane * 16);
        const int lim = tok_of(tq, i16) - 8 * q;
#pragma unroll
        for (int r = 0; r < 4; ++r) {
          a0[r] = (r <= lim) ? a0[r] : NEG;
          a1[r] = (4 + r <= lim) ? a1[r] : NEG;
        }
        const float m = rmax4(mxf4(a1, mxf4(a0, NEG)));
        const float ml = m * LOG2E;
        float lsum = 0.f;
#pragma unroll
        for (int r = 0; r < 4; ++r) {
          a0[r] = (r <= lim) ? ex2(a0[r] * LOG2E - ml) : 0.f;
          a1[r] = (4 + r <= lim) ? ex2(a1[r] * LOG2E - ml) : 0.f;
          lsum += a0[r] + a1[r];
        }
        lsum = rsum4(lsum);
        const unsigned o1 = R_ETT + (unsigned)(h * 2 + tq) * KB + lane * 16, o2 = R_ETS + (unsigned)((tq * 16 + h) * 16 + i16) * 8;
        st16c(c.rs, rec_off(L, s) + o1, ns4(pk8(a0, a1)));
        if (q == 0) st8c(c.rs, rec_off(L, s) + o2, ns2(v2u{__float_as_uint(m), __float_as_uint(lsum)}));
        st16c(c.rs, rec_in(L, s + AD) + o1, S4);
        if (q == 0) st8c(c.rs, rec_in(L, s + AD) + o2, S2);
      }
    }
    STAMP(t, 5);
  }
}

template <int L>
DEVI void role_O(Ctx& c, int tau, int cg, unsigned char* lds, __hip_bfloat16* out) {
  PMASK(8);
  const int lane = c.lane, w = c.wave;
  const int i16 = lane & 15, q = lane >> 4;
  const int ct = 4 * cg + w;
  const unsigned po = (unsigned)(tau * 14 + (ct >> 1)) * KB + (unsigned)((2 * (ct & 1) + (q >> 1)) * 16 + i16) * 16 + (q & 1) * 8;
  v4u U[16], e[4], sv[4], z[4];
  v2u es, xr;
  auto load_u = [&](int s) {
    const unsigned ub = u_in(L, s) + (unsigned)(4 * cg * 16 + 4 * w) * KB + lane * 16;
#pragma unroll
    for (int k = 0; k < 4; ++k)
#pragma unroll
      for (int hh = 0; hh < 4; ++hh) U[k * 4 + hh] = ld16c(c.rs, ub + (k * 16 + hh) * KB);
    xr = ld8c(c.rs, rec_in(L, s) + R_XR + po);
  };
  auto load_e = [&](int s) {
    const unsigned rb = rec_off(L, s);
#pragma unroll
    for (int hh = 0; hh < 4; ++hh) e[hh] = ld16c(c.rs, rb + R_ETT + (unsigned)((4 * w + hh) * 2 + tau) * KB + lane * 16);
    es = ld8c(c.rs, rb + R_ETS + (unsigned)((tau * 16 + 4 * w) * 16 + lane) * 8);
  };
  auto load_z = [&](int s) {
#pragma unroll
    for (int k = 0; k < 4; ++k) z[k] = ld16c(c.rs, rec_off(L, s) + R_ZB + (unsigned)((4 * cg + k) * 4 + w) * KB + lane * 16);
  };
  auto load_s = [&](int s) {
    const unsigned rb = rec_off(L, s);
#pragma unroll
    for (int k = 0; k < 4; ++k) sv[k] = ld16c(c.rs, rb + R_STM + (unsigned)((k * 2 + tau) * 16 + 4 * w) * 256 + lane * 16);
    load_z(s);
  };
  const int fph = (lane >> 2) & 3;
  const unsigned fo = (lane < 4 * NPH) ? F_U + (unsigned)(fph * 28 + 4 * cg + (lane & 3)) * 64 + L * 16 + w * 4
                      : (lane >= 16 && lane < 24) ? F_XR + (unsigned)((L * 2 + tau) * 2 + fph) * 64 + (lane & 3) * 4 : OOB;
  int fl = 0;
  for (int s = 0; s < NSEG; ++s) {
    unsigned char* lred = lds + (s & 1) * 16 * KB;
    STAMP(s, 0);
    {
      const int need = (lane < 24 && fph == (lane < 16 ? (s + L * LAG) % NPH : (s & 1))) ? s + 1 : 0;
      POLL(__builtin_amdgcn_ballot_w64(fl < need) != 0ull, fl = (int)ld4c(c.rs, fo), 1);
      CBAR();
      load_u(s);
    }
    STAMP(s, 1);
    CBAR();
    load_e(s);
    load_s(s);
    fl = (int)ld4c(c.rs, fo);
    POLL(anysent(mx4(mx4(mx4(mx4(mx2(mx4(mx4(mx4(mx4(0u, e[0]), e[1]), e[2]), e[3]), es), sv[0]), sv[1]), sv[2]), sv[3])),
         { load_e(s); load_s(s); fl = (int)ld4c(c.rs, fo); }, PO);
    SB();
    {
      f4 a = u2f(sv[0]) + u2f(sv[2]), b = u2f(sv[1]) + u2f(sv[3]);
      const float mt = __uint_as_float(es.x), lt = __uint_as_float(es.y);
      const float mm = mxf4(b, mxf4(a, mt));
      const float ml = mm * LOG2E;
#pragma unroll
      for (int r = 0; r < 4; ++r) { a[r] = ex2(a[r] * LOG2E - ml); b[r] = ex2(b[r] * LOG2E - ml); }
      const float at = ex2(mt * LOG2E - ml);
      const float den = ((a[0] + a[1]) + (a[2] + a[3])) + ((b[0] + b[1]) + (b[2] + b[3])) + at * lt;
      const float inv = rcp(den);
      const v4u P = pk8(a * inv, b * inv);
      float wt[4];
      rbcast4(at * inv, wt);
      f4 acc[4];
#pragma unroll
      for (int hh = 0; hh < 4; ++hh) {
        v4u en;
#pragma unroll
        for (int r = 0; r < 4; ++r) en[r] = pk2(bflo(e[hh][r]) * wt[hh], bfhi(e[hh][r]) * wt[hh]);
#pragma unroll
        for (int k = 0; k < 4; ++k) acc[k] = mfma32(U[k * 4 + hh], en, hh ? acc[k] : Z4);
      }
      SB();
      POLL(anysent(mx4(mx4(mx4(mx4(0u, z[0]), z[1]), z[2]), z[3])), { load_z(s); fl = (int)ld4c(c.rs, fo); }, PZ);
      TOUCH(fl);
      STAMP(s, 2);
#pragma unroll
      for (int k = 0; k < 4; ++k) acc[k] = mfma32(z[k], P, acc[k]);
#pragma unroll
      for (int k = 0; k < 4; ++k) LDSF4(lred + (k * 4 + w) * KB + lane * 16) = acc[k];
    }
    BAR();
    {
      const unsigned char* rp = lred + w * 4 * KB + lane * 16;
      const f4 acc = (LDSF4(rp) + LDSF4(rp + KB)) + (LDSF4(rp + 2 * KB) + LDSF4(rp + 3 * KB));
      const v2u y = pk4(acc);
      const v2u o = v2u{pk2(bflo(xr.x) + bflo(y.x), bfhi(xr.x) + bfhi(y.x)), pk2(bflo(xr.y) + bflo(y.y), bfhi(xr.y) + bfhi(y.y))};
      if (L == 0) {
        st8c(c.rs, rec_off(0, s) + R_X2 + po, ns2(o));
        st8c(c.rs, rec_in(0, s + AD) + R_X2 + po, S2);
      } else {
        *(v2u*)((unsigned char*)out + (size_t)(s * 32 + tok_of(tau, i16)) * 896 + (ct * 16 + 4 * q) * 2) = o;
      }
    }
    STAMP(s, 3);
  }
}

template <int L>
DEVI void role_AH(Ctx& c, int a, unsigned char* lds) {
  PMASK(9);
  asm volatile("; layer %2" : "+v"(c.tid), "+v"(c.lane) : "n"(L) : "memory");
  const int lane = c.lane, w = c.wave;
  const int i16 = lane & 15, q = lane >> 4;
  unsigned char* lK = lds;
  unsigned char* lV = lds + 18 * KB;
  unsigned char* lQ = lds + 36 * KB;
  unsigned char* lmg = lds + 38 * KB;
  unsigned char* lms = lds + 42 * KB;
  const int tau = w & 1, g = w >> 1;
  v4u x[14];
  auto load_x = [&](int s) {
    const unsigned xb = (L == 0 ? X0_OFF + (unsigned)s * SEGX : rec_off(0, s) + R_X2) + (unsigned)(tau * 14) * KB + lane * 16;
#pragma unroll
    for (int f = 0; f < 14; ++f) x[f] = (L == 0) ? ld16(c.rs, xb + f * KB) : ld16c(c.rs, xb + f * KB);
  };
  auto xbad = [&]() {
    unsigned m = 0u;
#pragma unroll
    for (int f = 0; f < 14; ++f) m = mx4(m, x[f]);
    return anysent(m);
  };
  load_x(0);
  v4u W[42];
#pragma unroll
  for (int tl = 0; tl < 3; ++tl) {
    const unsigned b0 = WQ_OFF + (unsigned)(a * 6 + (tl < 2 ? 2 * g + tl : 4 + g)) * 14 * KB + lane * 16;
#pragma unroll
    for (int k = 0; k < 14; ++k) W[tl * 14 + k] = ld16(c.rs, b0 + k * KB);
  }
  for (int o = c.tid * 16; o < 36 * (int)KB; o += 4096) LDS16(lK + o) = v4u{0u, 0u, (o < 18 * (int)KB && q == 3) ? 0xFF7Fu : 0u, 0u};
  f4 cs0 = Z4, cs1 = Z4, cs2 = Z4;
  {
    const v4u one = {0x3F803F80u, 0x3F803F80u, 0x3F803F80u, 0x3F803F80u};
#pragma unroll
    for (int k = 0; k < 14; ++k) { cs0 = mfma32(W[k], one, cs0); cs1 = mfma32(W[14 + k], one, cs1); cs2 = mfma32(one, W[28 + k], cs2); }
  }
#pragma unroll
  for (int f = 0; f < 42; ++f) PIN_A(W[f]);
  const float csv = cs2[0];
  const float vone = (g == 1 && i16 == 12) ? 1.0f : 0.0f;
  const float qone = (g == 0 && q == 3) ? 1.0f : 0.0f;
  const float sc = 0.18898223650461362f * LOG2E;
  const int lim = tok_of(tau, i16) - 8 * q;
  v4u dm;
#pragma unroll
  for (int e = 0; e < 4; ++e) dm[e] = ((2 * e <= lim) ? 0xFFFFu : 0u) | ((2 * e + 1 <= lim) ? 0xFFFF0000u : 0u);
  const int c0 = 28 * a + 4 * q, c1 = c0 + 16;
  const unsigned oa = (g == 0) ? R_OA + (unsigned)tau * 14 * KB + (c0 >> 5) * KB + (((c0 >> 3) & 3) * 16 + i16) * 16 + (c0 & 4) * 2 : OOB;
  const unsigned ob = (g == 0 && q < 3) ? R_OA + (unsigned)tau * 14 * KB + (c1 >> 5) * KB + (((c1 >> 3) & 3) * 16 + i16) * 16 + (c1 & 4) * 2 : OOB;
  CBAR();
#pragma unroll
  for (int f = 0; f < 14; ++f) TOUCH(x[f]);
  BAR();
  int slot = 0;
  for (int s = 0; s < NSEG; ++s) {
    STAMP(s, 0);
    v4u kf[10], vf[10];
    int sl[4];
#pragma unroll
    for (int b = 0; b < 4; ++b) {
      int k = slot - ((b == 0 && g == 0) ? 8 : 4 * g + b);
      k += (k < 0) ? 9 : 0;
      sl[b] = k;
      kf[2 * b] = LDS16(lK + (k * 2) * KB + lane * 16); kf[2 * b + 1] = LDS16(lK + (k * 2 + 1) * KB + lane * 16);
    }
    if (L == 1) POLL(xbad(), load_x(s), 2);
    STAMP(s, 1);
    SB();
    {
      f4 a0 = Z4, a1 = Z4, a2 = Z4, gm = Z4;
#pragma unroll
      for (int f = 0; f < 14; ++f) {
        gm = mfma32(x[f], x[f], gm);
        a1 = mfma32(W[14 + f], x[f], a1);
        SB();
      }
      float mean, rstd, mr;
      f4 rs, ms;
      float sum, sq;
      f4 su, sv;
#pragma unroll
      for (int f = 0; f < 14; ++f) {
        a0 = mfma32(W[f], x[f], a0);
        a2 = mfma32(x[f], W[28 + f], a2);
        if (f == 1) {
          const float d = (i16 & 2) ? ((i16 & 1) ? gm[3] : gm[2]) : ((i16 & 1) ? gm[1] : gm[0]);
          const int di = __float_as_int(d), si = __float_as_int(a1[1]);
          sq = __int_as_float(__builtin_amdgcn_ds_bpermute(((i16 >> 2) * 16 + i16) * 4, di));
          sum = __int_as_float(__builtin_amdgcn_ds_bpermute((48 + i16) * 4, si));
#pragma unroll
          for (int r = 0; r < 4; ++r) {
            sv[r] = __int_as_float(__builtin_amdgcn_ds_bpermute((20 * q + r) * 4, di));
            su[r] = __int_as_float(__builtin_amdgcn_ds_bpermute((48 + 4 * q + r) * 4, si));
          }
        }
        if (f == 7) {
          mean = sum * (1.0f / 448.0f);
          rstd = rsq(fmaxf(sq * (1.0f / 448.0f) - mean * mean, 0.f) + 1e-5f);
          mr = mean * rstd;
        }
        if (f >= 8 && f < 12) {
          const int r = f - 8;
          const float mn = su[r] * (1.0f / 448.0f);
          rs[r] = rsq(fmaxf(sv[r] * (1.0f / 448.0f) - mn * mn, 0.f) + 1e-5f);
          ms[r] = mn * rs[r];
        }
        SB();
      }
      a0 = a0 * rstd - cs0 * mr;
      a1 = a1 * rstd - cs1 * mr;
      a1[0] += qone;
      a1[1] = (q == 3) ? 0.f : a1[1];
      LDS16((g == 0 ? lQ + tau * KB : lK + (slot * 2 + tau) * KB) + lane * 16) = pk8(a0, a1);
      a2 = a2 * rs - ms * csv + vone;
      *(v2u*)(lV + (slot * 2 + g) * KB + lane * 16 + tau * 8) = pk4(a2);
    }
    BAR();
    STAMP(s, 2);
    {
      const v4u qf = LDS16(lQ + tau * KB + lane * 16);
      if (g == 0) { kf[8] = LDS16(lK + (slot * 2) * KB + lane * 16); kf[9] = LDS16(lK + (slot * 2 + 1) * KB + lane * 16); }
      SB();
      f4 scv[4][2];
#pragma unroll
      for (int b = 0; b < 4; ++b) {
        scv[b][0] = mfma32(kf[2 * b], qf, Z4);
        scv[b][1] = mfma32(kf[2 * b + 1], qf, Z4);
      }
      f4 e0 = Z4, e1 = Z4;
      if (g == 0) { e0 = mfma32(kf[8], qf, Z4); e1 = mfma32(kf[9], qf, Z4); }
      SB();
#pragma unroll
      for (int b = 0; b < 4; ++b) { vf[2 * b] = LDS16(lV + (sl[b] * 2) * KB + lane * 16); vf[2 * b + 1] = LDS16(lV + (sl[b] * 2 + 1) * KB + lane * 16); }
      if (g == 0) { vf[8] = LDS16(lV + (slot * 2) * KB + lane * 16); vf[9] = LDS16(lV + (slot * 2 + 1) * KB + lane * 16); }
      SB();
#pragma unroll
      for (int b = 0; b < 4; ++b) { PIN_V(scv[b][0]); PIN_V(scv[b][1]); }
      if (g == 0) {
        PIN_V(e0); PIN_V(e1);
#pragma unroll
        for (int r = 0; r < 4; ++r) {
          scv[0][0][r] = (r <= lim) ? e0[r] : scv[0][0][r];
          scv[0][1][r] = (4 + r <= lim) ? e1[r] : scv[0][1][r];
        }
      }
      float m = -1.0e30f;
#pragma unroll
      for (int b = 0; b < 4; ++b) m = mxf4(scv[b][1], mxf4(scv[b][0], m));
      m = rmax4(m);
      const float ml = m * sc;
      f4 o0 = Z4, o1 = Z4;
      const unsigned xn = (L == 0 ? X0_OFF + (unsigned)min(s + 1, NSEG - 1) * SEGX : rec_off(0, min(s + 1, NSEG - 1)) + R_X2) + (unsigned)(tau * 14) * KB + lane * 16;
#pragma unroll
      for (int bb = 0; bb < 4; ++bb) {
        const int b = 3 - bb;
#pragma unroll
        for (int r = 0; r < 4; ++r) {
          scv[b][0][r] = ex2(scv[b][0][r] * sc - ml);
          scv[b][1][r] = ex2(scv[b][1][r] * sc - ml);
          if (bb * 4 + r < 14) {
            SB();
            x[bb * 4 + r] = (L == 0) ? ld16(c.rs, xn + (bb * 4 + r) * KB) : ld16c(c.rs, xn + (bb * 4 + r) * KB);
            SB();
          }
        }
        v4u p = pk8(scv[b][0], scv[b][1]);
        if (b == 0 && g == 0) {
          const v4u pd = p & dm;
          p = p & ~dm;
          o0 = mfma32(vf[8], pd, o0);
          o1 = mfma32(vf[9], pd, o1);
        }
        o0 = mfma32(vf[2 * b], p, o0);
        o1 = mfma32(vf[2 * b + 1], p, o1);
      }
      if (g == 1) {
        LDSF4(lmg + (tau * 2 + 0) * KB + lane * 16) = o0;
        LDSF4(lmg + (tau * 2 + 1) * KB + lane * 16) = o1;
        *(float*)(lms + (tau * 64 + lane) * 4) = m;
      }
      BAR();
      v2u y0 = S2, y1 = S2;
      if (g == 0) {
        const float mo = *(const float*)(lms + (tau * 64 + lane) * 4);
        const float mt = mx3(m, mo, NEG);
        const float fa = ex2((m - mt) * sc), fb = ex2((mo - mt) * sc);
        const f4 r0 = o0 * fa + LDSF4(lmg + (tau * 2 + 0) * KB + lane * 16) * fb;
        const f4 r1 = o1 * fa + LDSF4(lmg + (tau * 2 + 1) * KB + lane * 16) * fb;
        float dn[4];
        rbcast4(r1[0], dn);
        const float inv = rcp(dn[3]);
        y0 = ns2(pk4(r0 * inv));
        y1 = ns2(pk4(r1 * inv));
      }
      st8c(c.rs, rec_off(L, s) + oa, y0);
      st8c(c.rs, rec_off(L, s) + ob, y1);
      st8c(c.rs, (s + AD < NSEG) ? rec_off(L, s + AD) + oa : OOB, S2);
      st8c(c.rs, (s + AD < NSEG) ? rec_off(L, s + AD) + ob : OOB, S2);
      STAMP(s, 3);
    }
    slot = (slot == 8) ? 0 : slot + 1;
  }
}

template <int l>
DEVI void role_AO(Ctx& c, int tau, int par, unsigned char* lds) {
  PMASK(9);
  asm volatile("; layer %2" : "+v"(c.tid), "+v"(c.lane) : "n"(l) : "memory");
  const int lane = c.lane, w = c.wave;
  const int i16 = lane & 15, q = lane >> 4;
  unsigned char* lO = lds;
  unsigned char* lx = lds + 14 * KB;
  unsigned char* lst = lds + 18 * KB;
  v4u W[98];
  {
    const unsigned b0 = WO_OFF + (unsigned)(3 * w) * 28 * KB + lane * 16;
#pragma unroll
    for (int f = 0; f < 84; ++f) W[f] = ld16(c.rs, b0 + f * KB);
    const unsigned b1 = WO_OFF + (unsigned)(12 + (w >> 1)) * 28 * KB + (w & 1) * 7 * KB + lane * 16;
#pragma unroll
    for (int k = 0; k < 7; ++k) { W[84 + k] = ld16(c.rs, b1 + k * KB); W[91 + k] = ld16(c.rs, b1 + (14 + k) * KB); }
#pragma unroll
    for (int f = 0; f < 56; ++f) PIN_A(W[f]);
  }
  const int ux = 12 + (w >> 1), kx = 7 * (w & 1);
  const bool own4 = !(w & 1);
  const unsigned fo = (lane == 0) ? F_XR + (unsigned)((l * 2 + tau) * 2 + par) * 64 + w * 4 : OOB;
  for (int s = par; s < NSEG; s += 2) {
    const unsigned rb = rec_off(l, s);
    STAMP(s, 0);
    {
      const unsigned ob = rb + R_OA + (unsigned)tau * 14 * KB;
      v4u v[4];
      auto ldo = [&]() {
        v[0] = ld16c(c.rs, ob + c.tid * 16); v[1] = ld16c(c.rs, ob + 4096 + c.tid * 16); v[2] = ld16c(c.rs, ob + 8192 + c.tid * 16);
        v[3] = ld16c(c.rs, ob + 12288 + (c.tid & 127) * 16);
      };
      CBAR();
      ldo();
      DRAIN();
      st4c(c.rs, fo, (unsigned)max(s - 1, 0));
      POLL(anysent(mx4(mx4(mx4(mx4(0u, v[0]), v[1]), v[2]), v[3])), ldo(), 1);
      LDS16(lO + c.tid * 16) = v[0];
      LDS16(lO + 4096 + c.tid * 16) = v[1];
      LDS16(lO + 8192 + c.tid * 16) = v[2];
      LDS16(lO + 12288 + (c.tid & 127) * 16) = v[3];
    }
    STAMP(s, 1);
    v4u xr[4];
    auto ldr = [&]() {
#pragma unroll
      for (int p = 0; p < 4; ++p) {
        const int u = (p < 3) ? 3 * w + p : ux;
        if (l == 0) xr[p] = ld16(c.rs, X0_OFF + (unsigned)s * SEGX + (unsigned)(tau * 14 + u) * KB + lane * 16);
        else xr[p] = ld16c(c.rs, rec_off(0, s) + R_X2 + (unsigned)(tau * 14 + u) * KB + lane * 16);
      }
    };
    BAR();
    f4 acc[8] = {Z4, Z4, Z4, Z4, Z4, Z4, Z4, Z4};
    {
      const unsigned char* xp = lO + lane * 16;
      v4u xs[7];
#pragma unroll
      for (int k = 0; k < 7; ++k) xs[k] = LDS16(xp + (kx + k) * KB);
      v4u y0 = LDS16(xp), y1 = LDS16(xp + KB);
      SB();
#pragma unroll
      for (int k = 0; k < 7; ++k) {
        acc[6] = mfma32(W[84 + k], xs[k], acc[6]);
        acc[7] = mfma32(W[91 + k], xs[k], acc[7]);
      }
      SB();
      ldr();
      SB();
#pragma unroll
      for (int ks = 0; ks < 14; ++ks) {
        const v4u x = y0;
        y0 = y1;
        if (ks + 2 < 14) y1 = LDS16(xp + (ks + 2) * KB);
        if (ks == 2 && !own4) { LDSF4(lx + ((w >> 1) * 2 + 0) * KB + lane * 16) = acc[6]; LDSF4(lx + ((w >> 1) * 2 + 1) * KB + lane * 16) = acc[7]; }
#pragma unroll
        for (int pp = 0; pp < 3; ++pp) {
          acc[pp * 2 + 0] = mfma32(W[pp * 28 + ks], x, acc[pp * 2 + 0]);
          acc[pp * 2 + 1] = mfma32(W[pp * 28 + 14 + ks], x, acc[pp * 2 + 1]);
        }
        SB();
      }
    }
    if (l == 1) POLL(anysent(mx4(mx4(mx4(mx4(0u, xr[0]), xr[1]), xr[2]), xr[3])), ldr(), 1);
    BAR();
    STAMP(s, 2);
    v4u xb[4];
    if (own4) {
      acc[6] += LDSF4(lx + ((w >> 1) * 2 + 0) * KB + lane * 16);
      acc[7] += LDSF4(lx + ((w >> 1) * 2 + 1) * KB + lane * 16);
    }
    float sum = 0.f, sq = 0.f;
#pragma unroll
    for (int p = 0; p < 4; ++p) {
      xb[p] = resid8(xr[p], acc[2 * p], acc[2 * p + 1]);
      if (p == 3 && !own4) xb[p] = v4u{0u, 0u, 0u, 0u};
#pragma unroll
      for (int e = 0; e < 4; ++e) { sum = dot2(xb[p][e], 0x3F803F80u, sum); sq = dot2(xb[p][e], xb[p][e], sq); }
    }
    sum = rsum4(sum);
    sq = rsum4(sq);
    if (q == 0) *(f2*)(lst + (w * 16 + i16) * 8) = f2{sum, sq};
    BAR();
    const f2 st = (*(const f2*)(lst + i16 * 8) + *(const f2*)(lst + (16 + i16) * 8)) + (*(const f2*)(lst + (32 + i16) * 8) + *(const f2*)(lst + (48 + i16) * 8));
    const float mean = st[0] * (1.0f / 448.0f);
    const float rstd = rsq(fmaxf(st[1] * (1.0f / 448.0f) - mean * mean, 0.f) + 1e-5f);
    const float mr = mean * rstd;
#pragma unroll
    for (int p = 0; p < 4; ++p)
      if (p < 3 || own4) {
        const int u = (p < 3) ? 3 * w + p : ux;
        const unsigned fo = (unsigned)(tau * 14 + u) * KB + lane * 16;
        v4u o;
#pragma unroll
        for (int e = 0; e < 4; ++e) o[e] = pk2(bflo(xb[p][e]) * rstd - mr, bfhi(xb[p][e]) * rstd - mr);
        st16c(c.rs, rb + R_XN + fo, ns4(o));
      }
#pragma unroll
    for (int p = 0; p < 4; ++p)
      if (p < 3 || own4) {
        const int u = (p < 3) ? 3 * w + p : ux;
        const unsigned fo = (unsigned)(tau * 14 + u) * KB + lane * 16;
        st16c(c.rs, rb + R_XR + fo, xb[p]);
        st16c(c.rs, rec_in(l, s + AD) + R_XN + fo, S4);
      }
    STAMP(s, 3);
  }
  DRAIN();
  st4c(c.rs, fo, (unsigned)NSEG);
}

DEVI unsigned ring_offset(const unsigned char* ws) {
  const unsigned lo = (unsigned)((size_t)ws & (2u * MB - 1u));
  return ((lo + FLAG_BYTES + 2u * MB - 1u) & ~(2u * MB - 1u)) - lo;
}

__global__ void __launch_bounds__(256, 1) mega_kernel(unsigned char* ws, const unsigned char* mem0, __hip_bfloat16* out) {
  extern __shared__ __attribute__((aligned(16))) unsigned char lds[];
  Ctx c;
  c.ws = ws;
  c.rs = __builtin_amdgcn_make_buffer_rsrc(ws, 0, WS_END, 0x00020000);
  c.ring = ring_offset(ws);
  c.tid = threadIdx.x;
  c.lane = threadIdx.x & 63;
  c.wave = __builtin_amdgcn_readfirstlane(threadIdx.x >> 6);
  c.dead = false;
  const int b = blockIdx.x, x = b & 7, r = b >> 3;
  PMASK(1);
  STAMP(127, 0);
  {
    const unsigned char* pc = (const unsigned char*)(__builtin_amdgcn_s_getpc() & ~(unsigned long long)255);
    const unsigned o = (unsigned)(r * 256 + threadIdx.x) * 16;
    if (o < CPF * KB) { const v4u v = *(const v4u*)(pc + o); TOUCH(v); }
  }
  if (x == 0) {
    role_H(c, r >> 1, r & 1, lds);
  } else if (x < 3) {
    if (r < 28) role_CV(c, r, x - 1, lds, mem0);
  } else if (x < 5) {
    if (r & 1) role_GX<1>(c, r >> 1, x - 3, lds); else role_GX<0>(c, r >> 1, x - 3, lds);
  } else if (x == 5) {
    if (r < 14) role_O<0>(c, r / 7, r % 7, lds, out); else if (r < 28) role_O<1>(c, (r - 14) / 7, (r - 14) % 7, lds, out);
  } else if (x == 6) {
    if (r >> 4) role_AH<1>(c, r & 15, lds); else role_AH<0>(c, r & 15, lds);
  } else {
    if (r < 4) role_AO<0>(c, r & 1, r >> 1, lds); else if (r < 8) role_AO<1>(c, r & 1, (r >> 1) & 1, lds); else if (r < 16) probe_side(c, r - 8);
  }
}

#define PAUX 16
#define PST(off, v) __builtin_amdgcn_raw_buffer_store_b128(v, rs, off, 0, PAUX)
__global__ void __launch_bounds__(512) prologue_kernel(unsigned char* ws, const unsigned short* x0, const unsigned short* wqkv, const unsigned short* wo,
                                                       const unsigned short* mem0, const unsigned short* Wq, const unsigned short* Wk,
                                                       const unsigned short* Wv, const unsigned short* Wo) {
  extern __shared__ __attribute__((aligned(16))) unsigned char lb[];
  const int b = blockIdx.x, tid = threadIdx.x, lane = tid & 63, w = __builtin_amdgcn_readfirstlane(tid >> 6);
  const int i16 = lane & 15, q = lane >> 4;
  const rsrc_t rs = __builtin_amdgcn_make_buffer_rsrc(ws, 0, WS_END, 0x00020000);
  const bool prod = b < 224;
  const int kk = (b >> 3) / 7, cg = (b >> 3) % 7;
  const bool isG = kk < 2;
  const int h = (2 * (b & 7) + kk) & 15;
  auto stage_b = [&](const v4u* bv) {
    if (isG) {
#pragma unroll
      for (int k = 0; k < 4; ++k) LDS16(lb + (16 * k + (tid >> 5)) * 528 + (tid & 31) * 16) = bv[k];
    } else {
      unsigned char* p = lb + 8 * (tid & 7) * 528 + (tid >> 3) * 8;
#pragma unroll
      for (int k = 0; k < 4; ++k) {
        *(v2u*)(p + (2 * k) * 528) = v2u{__builtin_amdgcn_perm(bv[1][k], bv[0][k], 0x05040100u), __builtin_amdgcn_perm(bv[3][k], bv[2][k], 0x05040100u)};
        *(v2u*)(p + (2 * k + 1) * 528) = v2u{__builtin_amdgcn_perm(bv[1][k], bv[0][k], 0x07060302u), __builtin_amdgcn_perm(bv[3][k], bv[2][k], 0x07060302u)};
      }
    }
    BAR();
  };
  v4u bv[4];
  if (prod) {
    const unsigned short* bp = isG ? Wq + ((size_t)(64 * cg + (tid >> 5)) * 16 + h) * 256 + (tid & 31) * 8
                                   : Wo + ((size_t)h * 256 + 4 * (tid >> 3)) * 448 + 64 * cg + 8 * (tid & 7);
    const unsigned bs = isG ? 16 * 4096 : 448;
#pragma unroll
    for (int k = 0; k < 4; ++k) bv[k] = *(const v4u*)(bp + (size_t)k * bs);
  }
  if (prod && w < 7) {
    const unsigned short* Wa = isG ? Wk : Wv;
    v4u A[4][8];
#pragma unroll
    for (int mt = 0; mt < 4; ++mt)
#pragma unroll
      for (int k = 0; k < 8; ++k) {
        const int n = 2 * k + (lane >> 5), d = 64 * w + 32 * (mt >> 1) + 8 * (n >> 2) + 4 * (mt & 1) + (n & 3);
        A[mt][k] = *(const v4u*)(Wa + ((size_t)d * 16 + h) * 256 + (lane & 31) * 8);
      }
    stage_b(bv);
    unsigned char* la = lb + (64 + 16 * w) * 528;
    const float sc = isG ? 0.0625f : 1.0f;
#pragma unroll
    for (int mt = 0; mt < 4; ++mt) {
#pragma unroll
      for (int k = 0; k < 8; ++k) LDS16(la + (2 * k + (lane >> 5)) * 528 + (lane & 31) * 16) = A[mt][k];
      CBAR();
#pragma unroll
      for (int es = 0; es < 8; ++es) A[mt][es] = LDS16(la + i16 * 528 + es * 64 + q * 16);
      CBAR();
    }
#pragma unroll
    for (int nt = 0; nt < 4; ++nt) {
      const int row = isG ? 32 * (nt >> 1) + 8 * (i16 >> 2) + 4 * (nt & 1) + (i16 & 3) : 16 * nt + i16;
      f4 a[4] = {Z4, Z4, Z4, Z4};
#pragma unroll
      for (int es = 0; es < 8; ++es) {
        const v4u bf = LDS16(lb + row * 528 + es * 64 + q * 16);
#pragma unroll
        for (int mt = 0; mt < 4; ++mt) a[mt] = mfma32(A[mt][es], bf, a[mt]);
      }
      const unsigned off = isG ? WG_OFF + (unsigned)(((h * 14 + 2 * cg + (nt >> 1)) * 2 + (nt & 1)) * 14 + 2 * w) * KB
                               : WU_OFF + (unsigned)(((4 * cg + nt) * 16 + h) * 14 + 2 * w) * KB;
      PST(off + lane * 16, pk8(a[0] * sc, a[1] * sc));
      PST(off + KB + lane * 16, pk8(a[2] * sc, a[3] * sc));
    }
  } else {
    constexpr int NW = 224 + 32 * 8;
    const int wid = prod ? b : 224 + (b - 224) * 8 + w;
    unsigned short gv[4][8];
    v4u xv[4];
#pragma unroll
    for (int it = 0; it < 4; ++it) {
      const int f = min(wid + it * NW, 1735), ks = f % 14;
      const unsigned short* src;
      unsigned stride;
      bool ok = true;
      unsigned short pad = 0;
      if (f < 1344) {
        const int tl = (f / 14) % 6, a = f / 84;
        const int dh = (tl < 4) ? 8 * (i16 >> 2) + 4 * (tl & 1) + (i16 & 3) : 16 * (tl - 4) + i16;
        ok = dh < 28;
        pad = (tl < 4 && dh == 29) ? 0x3F80 : 0;
        src = wqkv + ((tl < 4) ? (tl >> 1) * 448 : 896) + 28 * a + min(dh, 27);
        stride = 1344;
      } else {
        const int g = f - 1344;
        src = wo + 32 * (g / 28) + 8 * (i16 >> 2) + 4 * ((g / 14) & 1) + (i16 & 3);
        stride = 448;
      }
      src += (size_t)(32 * ks + 8 * q) * stride;
#pragma unroll
      for (int jj = 0; jj < 8; ++jj) gv[it][jj] = src[(size_t)jj * stride];
      if (!ok) {
#pragma unroll
        for (int jj = 0; jj < 8; ++jj) gv[it][jj] = pad;
      }
    }
#pragma unroll
    for (int it = 0; it < 4; ++it) {
      const int f = min(wid + it * NW, 1791), sg = f / 28, tau = (f / 14) & 1, ks = f % 14;
      xv[it] = *(const v4u*)(x0 + (size_t)(sg * 32 + tok_of(tau, i16)) * 448 + 32 * ks + 8 * q);
    }
    SB();
    if (prod) stage_b(bv);
    const unsigned gtid = wid * 64 + lane, gstride = NW * 64;
    for (unsigned idx = gtid; idx < FLAG_BYTES / 16; idx += gstride) PST(idx * 16, (v4u{0u, 0u, 0u, 0u}));
    for (unsigned idx = gtid; idx < 2 * AD * REC / 16; idx += gstride) {
      const unsigned l = idx / (AD * REC / 16), r = idx % (AD * REC / 16);
      PST(REC_OFF + l * NSEG * REC + r * 16, S4);
    }
#pragma unroll
    for (int it = 0; it < 4; ++it) {
      const int f = wid + it * NW;
      if (f < 1736) {
        v4u o;
#pragma unroll
        for (int jj = 0; jj < 4; ++jj) o[jj] = (unsigned)gv[it][2 * jj] | ((unsigned)gv[it][2 * jj + 1] << 16);
        PST(WQ_OFF + (unsigned)f * KB + lane * 16, o);
      }
      if (f < 1792) PST(X0_OFF + (unsigned)f * KB + lane * 16, xv[it]);
    }
    const unsigned ring = ring_offset(ws);
    for (unsigned idx = gstride - 1 - gtid; idx < RING_BYTES / 16; idx += gstride) {
      const unsigned po = idx * 16, lo = ((po >> 14) << 13) | (po & 8191u);
      v4u o = S4;
      if (lo < L_MEMG && (lo & (64 * KB - 1)) < 14 * KB) {
        const unsigned r = lo & (64 * KB - 1), ln = (r >> 4) & 63, ks = r >> 10;
        o = ns4(*(const v4u*)(mem0 + (size_t)(ln & 7) * 448 + 32 * ks + 8 * (ln >> 4)));
      } else if (lo >= L_MEMG && lo < L_MEMG + 16 * KB && (lo & 8191u) < 7 * KB) {
        const unsigned r = lo & 8191u, ks = r >> 9, qq = (r >> 7) & 3, row = (r >> 4) & 7;
        o = ns4(*(const v4u*)(mem0 + (size_t)row * 448 + 32 * ks + 8 * qq));
      }
      PST(ring + po, o);
    }
  }
}

extern "C" void kernel_launch(void* const* d_in, const int* in_sizes, int n_in, void* d_out, int out_size, void* d_ws, size_t ws_size,
                              hipStream_t stream) {
  constexpr int LDS_BYTES = 100 * 1024;
  (void)hipFuncSetAttribute((const void*)mega_kernel, hipFuncAttributeMaxDynamicSharedMemorySize, LDS_BYTES);
  unsigned char* ws = (unsigned char*)d_ws;
  constexpr int PRO_LDS = (64 + 7 * 16) * 528;
  (void)hipFuncSetAttribute((const void*)prologue_kernel, hipFuncAttributeMaxDynamicSharedMemorySize, PRO_LDS);
  prologue_kernel<<<256, 512, PRO_LDS, stream>>>(ws, (const unsigned short*)d_in[0], (const unsigned short*)d_in[1], (const unsigned short*)d_in[2], (const unsigned short*)d_in[4],
                                          (const unsigned short*)d_in[5], (const unsigned short*)d_in[6], (const unsigned short*)d_in[7],
                                          (const unsigned short*)d_in[8]);
  mega_kernel<<<256, 256, LDS_BYTES, stream>>>(ws, (const unsigned char*)d_in[4], (__hip_bfloat16*)d_out);
}
```

```cpp
#include <hip/hip_runtime.h>
#include <hip/hip_bf16.h>

typedef unsigned v4u __attribute__((ext_vector_type(4)));
typedef unsigned v2u __attribute__((ext_vector_type(2)));
typedef float f4 __attribute__((ext_vector_type(4)));
typedef float f2 __attribute__((ext_vector_type(2)));
typedef short s8v __attribute__((ext_vector_type(8)));
typedef __bf16 b2v __attribute__((ext_vector_type(2)));
typedef __amdgpu_buffer_rsrc_t rsrc_t;
#define DEVI __device__ __forceinline__
#define RLX_AGENT __ATOMIC_RELAXED, __HIP_MEMORY_SCOPE_AGENT

#define LAG 6
#define GXS 1
#define PD 7
#define PP 0
#define CPF 71
#define D1 4
#define D2 2
#define NAP 110
#define DX 8
#define PX 6
#define PO 4
#define PZ 1
#define E2 2
#define USTREAM 1
constexpr int NSEG = 64, NT = NSEG + LAG, AD = 4, NPH = 2;
constexpr unsigned SENT = 0xFFFFFFFFu;
constexpr unsigned SPINCAP = 1u << 20;
constexpr float LOG2E = 1.4426950408889634f;
constexpr float NEG = -3.0e38f;

constexpr unsigned KB = 1024u, MB = 1024u * 1024u;
constexpr unsigned FLAG_BYTES = 8 * KB;
constexpr unsigned F_U = 1024, F_XR = 7168;
constexpr unsigned OOB = 0x80000000u;
constexpr unsigned F_ABORT = 0, F_SIDE = 128;
constexpr unsigned RING_BYTES = 1 * MB;
constexpr unsigned L_MEMH = 0, L_MEMC = 64 * KB, L_MEMG = 128 * KB, L_S = 192 * KB, L_E = 256 * KB, L_XS = 384 * KB, L_PROBE = 504 * KB;
constexpr unsigned WG_OFF = 4 * MB;
constexpr unsigned WG_BYTES = 16 * 14 * 2 * 14 * KB;
constexpr unsigned WU_OFF = WG_OFF + WG_BYTES;
constexpr unsigned WQ_OFF = WU_OFF + WG_BYTES;
constexpr unsigned WO_OFF = WQ_OFF + 16 * 6 * 14 * KB;
constexpr unsigned SEGX = 28 * KB;
constexpr unsigned R_XN = 0, R_XR = SEGX, R_OA = 2 * SEGX, R_X2 = 3 * SEGX, R_ETT = 4 * SEGX, R_ETS = R_ETT + 32 * KB, R_STM = R_ETS + 4 * KB,
                   R_ZB = R_STM + 32 * KB, REC = R_ZB + 112 * KB;
constexpr unsigned X0_OFF = WO_OFF + 392 * KB;
constexpr unsigned REC_OFF = X0_OFF + NSEG * SEGX;
constexpr unsigned SEGG = 448 * KB;
constexpr unsigned U_OFF = REC_OFF + 2 * NSEG * REC;
constexpr unsigned WS_END = U_OFF + 2 * NSEG * SEGG;
static_assert(WS_END < 200u * MB, "workspace");

DEVI f4 mfma32(v4u a, v4u b, f4 c) {
  return __builtin_amdgcn_mfma_f32_16x16x32_bf16(__builtin_bit_cast(s8v, a), __builtin_bit_cast(s8v, b), c, 0, 0, 0);
}
DEVI unsigned pk2(float a, float b) {
  f2 v = {a, b};
  return __builtin_bit_cast(unsigned, __builtin_convertvector(v, b2v));
}
DEVI float bflo(unsigned u) { return __uint_as_float(u << 16); }
DEVI float bfhi(unsigned u) { return __uint_as_float(u & 0xFFFF0000u); }
DEVI float rbf(float a) { return bflo(pk2(a, 0.f)); }
DEVI v4u pk8(f4 a, f4 b) { return v4u{pk2(a[0], a[1]), pk2(a[2], a[3]), pk2(b[0], b[1]), pk2(b[2], b[3])}; }
DEVI v2u pk4(f4 a) { return v2u{pk2(a[0], a[1]), pk2(a[2], a[3])}; }
DEVI v4u resid8(v4u x, f4 a, f4 b) {
  const v4u y = pk8(a, b);
  return v4u{pk2(bflo(x.x) + bflo(y.x), bfhi(x.x) + bfhi(y.x)), pk2(bflo(x.y) + bflo(y.y), bfhi(x.y) + bfhi(y.y)),
             pk2(bflo(x.z) + bflo(y.z), bfhi(x.z) + bfhi(y.z)), pk2(bflo(x.w) + bflo(y.w), bfhi(x.w) + bfhi(y.w))};
}
DEVI unsigned ns(unsigned x) { return min(x, 0xFFFFFFFEu); }
DEVI v4u ns4(v4u x) { return v4u{ns(x.x), ns(x.y), ns(x.z), ns(x.w)}; }
DEVI v2u ns2(v2u x) { return v2u{ns(x.x), ns(x.y)}; }
DEVI v4u f2u(f4 x) { return v4u{__float_as_uint(x[0]), __float_as_uint(x[1]), __float_as_uint(x[2]), __float_as_uint(x[3])}; }
DEVI f4 u2f(v4u x) { return f4{__uint_as_float(x.x), __uint_as_float(x.y), __uint_as_float(x.z), __uint_as_float(x.w)}; }
DEVI unsigned mx4(unsigned m, v4u v) { return max(max(max(max(m, v.x), v.y), v.z), v.w); }
DEVI unsigned mx2(unsigned m, v2u v) { return max(max(m, v.x), v.y); }
DEVI bool anysent(unsigned m) { return __builtin_amdgcn_ballot_w64(m == SENT) != 0ull; }
DEVI float ex2(float x) { return __builtin_amdgcn_exp2f(x); }
DEVI float rcp(float x) { return __builtin_amdgcn_rcpf(x); }
DEVI float rsq(float x) { return __builtin_amdgcn_rsqf(x); }
DEVI float mx3(float a, float b, float c) { return __builtin_fmaxf(__builtin_fmaxf(a, b), c); }
DEVI float mxf4(f4 a, float m) { return mx3(a[2], a[3], mx3(a[0], a[1], m)); }
DEVI float dot2(unsigned a, unsigned b, float c) {
  return __builtin_amdgcn_fdot2_f32_bf16(__builtin_bit_cast(b2v, a), __builtin_bit_cast(b2v, b), c, false);
}
DEVI void swap16(unsigned& a, unsigned& b) { auto r = __builtin_amdgcn_permlane16_swap(a, b, false, false); a = r[0]; b = r[1]; }
DEVI void swap32(unsigned& a, unsigned& b) { auto r = __builtin_amdgcn_permlane32_swap(a, b, false, false); a = r[0]; b = r[1]; }
DEVI float rmax4(float x) {
  unsigned a = __float_as_uint(x), b = a;
  swap16(a, b);
  a = b = __float_as_uint(mx3(__uint_as_float(a), __uint_as_float(b), NEG));
  swap32(a, b);
  return mx3(__uint_as_float(a), __uint_as_float(b), NEG);
}
DEVI float rsum4(float x) {
  unsigned a = __float_as_uint(x), b = a;
  swap16(a, b);
  a = b = __float_as_uint(__uint_as_float(a) + __uint_as_float(b));
  swap32(a, b);
  return __uint_as_float(a) + __uint_as_float(b);
}
DEVI float ror8(float x) { return __uint_as_float(__builtin_amdgcn_update_dpp(0u, __float_as_uint(x), 0x128, 0xF, 0xF, false)); }
DEVI void rbcast4(float x, float* o) {
  unsigned a = __float_as_uint(x), b = a;
  swap16(a, b);
  unsigned a2 = a, b2 = b;
  swap32(a, a2);
  swap32(b, b2);
  o[0] = __uint_as_float(a); o[1] = __uint_as_float(b); o[2] = __uint_as_float(a2); o[3] = __uint_as_float(b2);
}

DEVI v4u ld16(rsrc_t r, unsigned off) { return __builtin_amdgcn_raw_buffer_load_b128(r, off, 0, 0); }
DEVI v4u ld16c(rsrc_t r, unsigned off) { return __builtin_amdgcn_raw_buffer_load_b128(r, off, 0, 16); }
DEVI v2u ld8c(rsrc_t r, unsigned off) { return __builtin_amdgcn_raw_buffer_load_b64(r, off, 0, 16); }
DEVI unsigned ld4c(rsrc_t r, unsigned off) { return __builtin_amdgcn_raw_buffer_load_b32(r, off, 0, 16); }
DEVI void st16c(rsrc_t r, unsigned off, v4u v) { __builtin_amdgcn_raw_buffer_store_b128(v, r, off, 0, 16); }
DEVI void st8c(rsrc_t r, unsigned off, v2u v) { __builtin_amdgcn_raw_buffer_store_b64(v, r, off, 0, 16); }
DEVI void st4c(rsrc_t r, unsigned off, unsigned v) { __builtin_amdgcn_raw_buffer_store_b32(v, r, off, 0, 16); }
#define PIN_A(x) asm volatile("" : "+a"(x))
#define TOUCH(x) asm volatile("" :: "v"(x))
#define PIN_V(x) asm volatile("" : "+v"(x))
#define DRAIN() asm volatile("s_waitcnt vmcnt(0)" ::: "memory")
#define CBAR() asm volatile("" ::: "memory")
#define SB() __builtin_amdgcn_sched_barrier(0)
#define BAR() do { asm volatile("s_waitcnt lgkmcnt(0)" ::: "memory"); __builtin_amdgcn_s_barrier(); asm volatile("" ::: "memory"); } while (0)
#define LDS16(p) (*(v4u*)(p))
#define LDSF4(p) (*(f4*)(p))
#define S4 (v4u{SENT, SENT, SENT, SENT})
#define S2 (v2u{SENT, SENT})
#define Z4 (f4{0.f, 0.f, 0.f, 0.f})

#define STAMP(it, e) do { } while (0)
#define PMASK(m) do { } while (0)
#define STAMPC(it, e) do { } while (0)
struct Ctx {
  unsigned char* ws;
  rsrc_t rs;
  unsigned ring;
  int tid, lane, wave;
  bool dead;
};
DEVI bool give_up(Ctx& c, unsigned& spins) {
  if (c.dead) return true;
  ++spins;
  bool d = false;
  if (spins > SPINCAP) { __hip_atomic_store((unsigned*)(c.ws + F_ABORT), 1u, RLX_AGENT); d = true; }
  else if ((spins & 255u) == 0u) d = __builtin_amdgcn_readfirstlane(__hip_atomic_load((unsigned*)(c.ws + F_ABORT), RLX_AGENT)) != 0u;
  c.dead = d;
  return d;
}
#define POLL2(FIRST, BADEXPR, RELOAD, SLEEP) do { if (FIRST) { unsigned spins_ = 0; do { if (give_up(c, spins_)) break; if (SLEEP) __builtin_amdgcn_s_sleep(SLEEP); CBAR(); RELOAD; } while (BADEXPR); } } while (0)
#define POLL(BADEXPR, RELOAD, SLEEP) do { unsigned spins_ = 0; while (BADEXPR) { if (give_up(c, spins_)) break; if (SLEEP) __builtin_amdgcn_s_sleep(SLEEP); CBAR(); RELOAD; } } while (0)

template <class LD>
DEVI void poll2(Ctx& c, v4u* r, LD ld) {
  v4u a[4] = {r[0], r[1], r[2], r[3]}, b[4];
  unsigned spins = 0;
  __builtin_amdgcn_s_sleep(PD);
  while (true) {
    CBAR();
    ld(b);
    SB();
    if (!anysent(mx4(mx4(mx4(mx4(0u, a[0]), a[1]), a[2]), a[3])) || give_up(c, spins)) {
#pragma unroll
      for (int k = 0; k < 4; ++k) r[k] = a[k];
      break;
    }
    CBAR();
    ld(a);
    SB();
    if (!anysent(mx4(mx4(mx4(mx4(0u, b[0]), b[1]), b[2]), b[3]))) {
#pragma unroll
      for (int k = 0; k < 4; ++k) r[k] = b[k];
      break;
    }
  }
}

DEVI unsigned rmap(const Ctx& c, unsigned lo, unsigned side) { return c.ring + ((lo >> 13) << 14) + (side << 13) + (lo & 8191u); }
DEVI unsigned rec_off(int l, int s) { return REC_OFF + (unsigned)(l * NSEG + s) * REC; }
DEVI unsigned rec_in(int l, int s) { return (s >= 0 && s < NSEG) ? rec_off(l, s) : OOB; }
DEVI unsigned u_in(int l, int s) { return (s >= 0 && s < NSEG) ? U_OFF + (unsigned)(l * NSEG + s) * SEGG : OOB; }
DEVI int tok_of(int tau, int m) { return 8 * (m >> 2) + 4 * tau + (m & 3); }

DEVI void probe_side(Ctx& c, int k) {
  if (c.wave != 0) return;
  unsigned best[2] = {0xFFFFFFFFu, 0xFFFFFFFFu};
#pragma unroll 1
  for (int it = 0; it < 4; ++it) {
#pragma unroll
    for (int sd = 0; sd < 2; ++sd) {
      DRAIN();
      const unsigned t0 = (unsigned)__builtin_amdgcn_s_memtime();
      if (c.lane < 8) st16c(c.rs, rmap(c, L_PROBE + k * 256 + c.lane * 16, sd), S4);
      DRAIN();
      const unsigned t1 = (unsigned)__builtin_amdgcn_s_memtime();
      best[sd] = min(best[sd], t1 - t0);
    }
  }
  if (c.lane == 0) st4c(c.rs, F_SIDE + k * 64, best[1] < best[0] ? 2u : 1u);
}
DEVI void touch_pages(Ctx& c, int k) {
  unsigned acc = 0u;
#pragma unroll 1
  for (unsigned o = REC_OFF + (unsigned)(k * 256 + c.tid) * 64 * KB; o < WS_END; o += 1024u * 64 * KB) acc |= ld4c(c.rs, o);
  TOUCH(acc);
}
DEVI unsigned get_sides(Ctx& c) {
  const unsigned o = F_SIDE + (c.lane & 7) * 64;
  unsigned v = ld4c(c.rs, o);
  POLL(__builtin_amdgcn_ballot_w64(v == 0u) != 0ull, v = ld4c(c.rs, o), 2);
  return (__builtin_popcountll(__builtin_amdgcn_ballot_w64(v == 2u)) > 32) ? 0u : 1u;
}

DEVI void role_H(Ctx& c, int h, int a, unsigned char* lds) {
  PMASK(1);
  const int lane = c.lane, w = c.wave;
  const int i16 = lane & 15, q = lane >> 4;
  unsigned char* lmem = lds;
  unsigned char* lred = lds + 8 * KB;
  unsigned char* ltb = lds + 24 * KB;
  const int odd = w & 1;
  const int ks0 = (w >> 1) * 7 + odd * 4, ksx = (w >> 1) * 7 + 3;
  v4u W[42], Wx[8];
#pragma unroll
  for (int lp = 0; lp < 7; ++lp) {
    const int gp = odd ? 6 - lp : lp;
    const unsigned b0 = WG_OFF + (unsigned)(h * 14 + 7 * a + gp) * 28 * KB + lane * 16;
#pragma unroll
    for (int t = 0; t < 2; ++t)
#pragma unroll
      for (int k = 0; k < 3; ++k) W[(lp * 2 + t) * 3 + k] = ld16(c.rs, b0 + (t * 14 + ks0 + k) * KB);
    if (lp < 4) {
#pragma unroll
      for (int t = 0; t < 2; ++t) {
        Wx[lp * 2 + t] = ld16(c.rs, b0 + (t * 14 + ksx) * KB);
        if (lp == 3 && t != odd) Wx[lp * 2 + t] = v4u{0u, 0u, 0u, 0u};
      }
    }
  }
#pragma unroll
  for (int f = 0; f < 42; ++f) PIN_A(W[f]);
#pragma unroll
  for (int f = 0; f < 8; ++f) PIN_A(Wx[f]);
  const unsigned sides = get_sides(c);
  unsigned mo[4];
#pragma unroll
  for (int k = 0; k < 4; ++k) mo[k] = rmap(c, L_MEMH + (k < 3 ? ks0 + k : ksx) * KB + lane * 16, sides);
  const unsigned so0 = rmap(c, L_S + ((a * 2 + (q & 1)) * 16 + h) * 256 + i16 * 16, sides);
  const bool sval = (q >> 1) == (i16 >> 3);
  const bool stager = (w >> 1) == a;
  const int gp1 = (w < 3) ? w + 4 : 3;
  v4u X[8];
  auto load_x = [&](int t) {
#pragma unroll
    for (int l = 0; l < 2; ++l) {
      const unsigned xb = rec_in(l, t - l * LAG) + R_XN + (unsigned)(7 * a) * KB + lane * 16;
#pragma unroll
      for (int tau = 0; tau < 2; ++tau) {
        X[(l * 2 + tau) * 2 + 0] = ld16c(c.rs, xb + (tau * 14 + w) * KB);
        X[(l * 2 + tau) * 2 + 1] = ld16c(c.rs, xb + (tau * 14 + gp1) * KB);
      }
    }
  };
  CBAR();
  load_x(0);
  for (int t = 0; t < NT; ++t) {
    const unsigned slot = (unsigned)(t & 3) * 32 * KB;
    STAMP(t, 0);
    v4u m[4];
    CBAR();
#pragma unroll
    for (int k = 0; k < 4; ++k) m[k] = ld16c(c.rs, mo[k] + slot);
    POLL(anysent(mx4(mx4(mx4(mx4(0u, m[0]), m[1]), m[2]), m[3])),
         { _Pragma("unroll") for (int k = 0; k < 4; ++k) m[k] = ld16c(c.rs, mo[k] + slot); }, 0);
    STAMP(t, 1);
    if (stager) {
#pragma unroll
      for (int k = 0; k < 4; ++k) LDS16(lmem + ((k < 3 ? ks0 + k : ksx) - 7 * a) * KB + lane * 16) = m[k];
    }
    SB();
    v4u tb[7], mu[7];
    f4 acc[7][2];
    f4 sacc = Z4;
#pragma unroll
    for (int lp = 0; lp < 7; ++lp) {
#define HPK(e) do { if (lp >= 2) { SB(); tb[lp - 2][e] = pk2(acc[lp - 2][(e) >> 1][((e) & 1) * 2], acc[lp - 2][(e) >> 1][((e) & 1) * 2 + 1]); SB(); } } while (0)
      acc[lp][0] = mfma32(W[(lp * 2 + 0) * 3 + 0], m[0], Z4);
      HPK(0);
      acc[lp][1] = mfma32(W[(lp * 2 + 1) * 3 + 0], m[0], Z4);
      HPK(1);
      acc[lp][0] = mfma32(W[(lp * 2 + 0) * 3 + 1], m[1], acc[lp][0]);
      HPK(2);
      acc[lp][1] = mfma32(W[(lp * 2 + 1) * 3 + 1], m[1], acc[lp][1]);
      HPK(3);
      acc[lp][0] = mfma32(W[(lp * 2 + 0) * 3 + 2], m[2], acc[lp][0]);
      if (lp == 6) { SB(); tb[5][0] = pk2(acc[5][0][0], acc[5][0][1]); SB(); }
      acc[lp][1] = mfma32(W[(lp * 2 + 1) * 3 + 2], m[2], acc[lp][1]);
      if (lp == 6) { SB(); tb[5][1] = pk2(acc[5][0][2], acc[5][0][3]); SB(); }
      if (lp < 4) {
        acc[lp][0] = mfma32(Wx[lp * 2 + 0], m[3], acc[lp][0]);
        acc[lp][1] = mfma32(Wx[lp * 2 + 1], m[3], acc[lp][1]);
      }
      if (lp >= 2) sacc = mfma32(tb[lp - 2], mu[lp - 2], sacc);
      SB();
      if (lp == 0) {
        BAR();
#pragma unroll
        for (int k = 0; k < 7; ++k) mu[k] = LDS16(lmem + (odd ? 6 - k : k) * KB + lane * 16);
        SB();
      }
    }
    tb[5][2] = pk2(acc[5][1][0], acc[5][1][1]);
    tb[5][3] = pk2(acc[5][1][2], acc[5][1][3]);
    sacc = mfma32(tb[5], mu[5], sacc);
    SB();
    tb[6] = pk8(acc[6][0], acc[6][1]);
    sacc = mfma32(tb[6], mu[6], sacc);
    if (w != 0) LDSF4(lred + w * KB + lane * 16) = sacc;
    BAR();
    if (w == 0) {
      const f4 s = (sacc + LDSF4(lred + KB + lane * 16)) + (LDSF4(lred + 2 * KB + lane * 16) + LDSF4(lred + 3 * KB + lane * 16));
      if (sval) {
        st16c(c.rs, so0 + slot, ns4(f2u(s)));
        if (t >= 1) st16c(c.rs, so0 + (unsigned)((t - 1) & 3) * 32 * KB, S4);
      }
    }
    STAMP(t, 2);
#pragma unroll
    for (int lp = 0; lp < 7; ++lp) LDS16(ltb + (w * 7 + (odd ? 6 - lp : lp)) * KB + lane * 16) = tb[lp];
    BAR();
    v4u tp[8];
#pragma unroll
    for (int ww = 0; ww < 4; ++ww) {
      tp[ww * 2 + 0] = LDS16(ltb + (ww * 7 + w) * KB + lane * 16);
      tp[ww * 2 + 1] = LDS16(ltb + (ww * 7 + gp1) * KB + lane * 16);
      if (w == 3) tp[ww * 2 + 1] = v4u{0u, 0u, 0u, 0u};
    }
    POLL(anysent(mx4(mx4(mx4(mx4(mx4(mx4(mx4(mx4(0u, X[0]), X[1]), X[2]), X[3]), X[4]), X[5]), X[6]), X[7])), load_x(t), 1);
    SB();
    {
      f4 s[4] = {Z4, Z4, Z4, Z4};
#pragma unroll
      for (int k = 0; k < 8; ++k)
#pragma unroll
        for (int lt = 0; lt < 4; ++lt) s[lt] = mfma32(tp[k], X[lt * 2 + (k & 1)], s[lt]);
#pragma unroll
      for (int lt = 0; lt < 4; ++lt) LDSF4(lred + (lt * 4 + w) * KB + lane * 16) = s[lt];
    }
    BAR();
    {
      const int l = w >> 1, tau = w & 1, s = t - l * LAG;
      const f4 v = (LDSF4(lred + (w * 4 + 0) * KB + lane * 16) + LDSF4(lred + (w * 4 + 1) * KB + lane * 16)) +
                   (LDSF4(lred + (w * 4 + 2) * KB + lane * 16) + LDSF4(lred + (w * 4 + 3) * KB + lane * 16));
      if ((q >> 1) == l) {
        const unsigned o = R_STM + (unsigned)((((a * 2 + (q & 1)) * 2 + tau) * 16 + h) * 256 + i16 * 16);
        st16c(c.rs, rec_in(l, s) + o, ns4(f2u(v)));
        if (s >= 0) st16c(c.rs, rec_in(l, s + AD) + o, S4);
      }
    }
    CBAR();
    load_x(t + 1);
    STAMP(t, 3);
  }
}

DEVI void role_CV(Ctx& c, int j, int p, unsigned char* lds, const unsigned char* mem0) {
  PMASK(67);
  const int lane = c.lane, w = c.wave;
  const int i16 = lane & 15, q = lane >> 4;
  unsigned char* lmem = lds;
  unsigned char* lred = lds + 16 * KB;
  unsigned char* lXb = lds + 20 * KB;
  unsigned ko[6];
#pragma unroll
  for (int k = 0; k < 4; ++k) ko[k] = (unsigned)((w + k) & 3) * KB + lane * 16;
#pragma unroll
  for (int k = 0; k < 2; ++k) ko[4 + k] = (unsigned)(12 + ((w + k) & 1)) * KB + lane * 16;
#define KSO(i) (((i) < 12) ? ko[(i) / 3] + ((i) % 3) * 4 * KB : ko[(i) - 8])
  v4u W[56];
  {
    const unsigned b0 = WU_OFF + (unsigned)(j * 16 + 4 * w) * 14 * KB;
#pragma unroll
    for (int f = 0; f < 56; ++f) W[f] = ld16(c.rs, b0 + (f / 14) * 14 * KB + KSO(f % 14));
#pragma unroll
    for (int f = 0; f < 56; ++f) PIN_A(W[f]);
  }
  const v2u m0 = ns2(*(const v2u*)(mem0 + (size_t)(i16 & 7) * 896 + (j * 16 + 4 * q) * 2));
  const unsigned sides = get_sides(c);
  const unsigned sC = sides;
  unsigned mo[4], so[4], eo[4];
#pragma unroll
  for (int k = 0; k < 4; ++k) {
    mo[k] = rmap(c, L_MEMC + (k < 3 ? k * 4096 + c.tid * 16 : 12288 + (c.tid & 127) * 16), sC);
    so[k] = rmap(c, L_S + (k * 16 + 4 * w) * 256 + lane * 16, sC);
    eo[k] = rmap(c, L_E + (4 * w + k) * KB + lane * 16, sC);
  }
  const unsigned xo = rmap(c, L_XS + ((4 * w + q) * 2 + (i16 >> 3)) * 128 + (i16 & 7) * 8, sC);
  const unsigned pf = (unsigned)(j >> 1) * KB + (unsigned)((2 * (j & 1) + (q >> 1)) * 16 + i16) * 16 + (q & 1) * 8;
  const unsigned pg = (unsigned)(i16 >> 3) * 8 * KB + (unsigned)(j >> 1) * 512 + (unsigned)(2 * (j & 1) + (q >> 1)) * 128 + (i16 & 7) * 16 + (q & 1) * 8;
  const unsigned pH = rmap(c, L_MEMH + pf, sides), pC = rmap(c, L_MEMC + pf, sides), pG = rmap(c, L_MEMG + pg, sides ^ (unsigned)(p ^ 1));
  const unsigned pR = (w == 1) ? pH : (w == 2) ? pG : (w == 3) ? pC : OOB;
  const unsigned zo = R_ZB + (unsigned)(j * 4 + w) * KB + lane * 16;
  const unsigned uo = (unsigned)(j * 16 + 4 * w) * KB + lane * 16;
  const unsigned fo = (lane == 0) ? F_U + (unsigned)(p * 28 + j) * 64 + w * 4 : OOB;
#define XBAD anysent(mx4(mx4(mx4(mx4(mx4(mx4(mx4(0u, x[0]), x[1]), x[2]), x[3]), x[4]), x[5]), x[6]))
  v4u U[8], x[7];
  auto load_x = [&](int l, int s) {
    const unsigned xb = rec_in(l, s) + R_XN + c.tid * 16;
#pragma unroll
    for (int f = 0; f < 7; ++f) x[f] = ld16c(c.rs, xb + f * 4096);
  };
  auto stage_x = [&](int l) {
#pragma unroll
    for (int f = 0; f < 7; ++f) LDS16(lXb + l * 28 * KB + f * 4096 + c.tid * 16) = x[f];
  };
  unsigned* lhave = (unsigned*)(lds + 15 * KB);
  bool have = false, allhave = false;
  for (int t = p; t < NT; t += 2) {
    const unsigned slot = (unsigned)(t & 3) * 32 * KB, eslot = (unsigned)(t & 7) * 32 * KB, xslot = (unsigned)(t & 7) * 16 * KB;
    const int s0 = t, s1 = t - LAG;
    STAMP(t, 0);
    if (!allhave) {
      if (!have) {
        CBAR();
        load_x(0, s0);
        POLL(XBAD, load_x(0, s0), 1);
        stage_x(0);
      }
      BAR();
    }
    CBAR();
    load_x(1, s1);
    SB();
    f4 acc[8];
    const unsigned ub0 = u_in(0, s0) + uo, za0 = rec_in(0, s0 + AD) + zo, xb0 = rec_in(0, s0 + 2) + R_XN + c.tid * 16;
    auto side_op = [&](int k) {
      if (k < 4) st16c(c.rs, ub0 + k * KB, U[k]);
      else if (k == 4) st16c(c.rs, za0, S4);
      else if (k < 12) x[k - 5] = ld16c(c.rs, xb0 + (k - 5) * 4096);
    };
#pragma unroll
    for (int l = 0; l < 2; ++l) {
      const int s = l ? s1 : s0;
      const bool act = (s >= 0 && s < NSEG);
#pragma unroll
      for (int k = 0; k < 8; ++k) acc[k] = Z4;
      if (act) {
        const unsigned char* xp = lXb + l * 28 * KB;
        v4u xa = LDS16(xp + KSO(0)), xb = LDS16(xp + 14 * KB + KSO(0)), ya = LDS16(xp + KSO(1)), yb = LDS16(xp + 14 * KB + KSO(1));
        SB();
#pragma unroll
        for (int ks = 0; ks < 14; ++ks) {
          const v4u x0 = xa, x1 = xb;
          xa = ya; xb = yb;
          if (ks + 2 < 14) { ya = LDS16(xp + KSO(ks + 2)); yb = LDS16(xp + 14 * KB + KSO(ks + 2)); }
#pragma unroll
          for (int hh = 0; hh < 4; ++hh) {
            acc[hh * 2 + 0] = mfma32(x0, W[hh * 14 + ks], acc[hh * 2 + 0]);
            acc[hh * 2 + 1] = mfma32(x1, W[hh * 14 + ks], acc[hh * 2 + 1]);
            if (l == 1 && hh == 1) { SB(); side_op(ks); SB(); }
          }
          SB();
        }
      }
      else if (l == 1) {
#pragma unroll
        for (int k = 0; k < 12; ++k) side_op(k);
      }
      if (l == 0) {
        const bool bad = XBAD;
#pragma unroll
        for (int f = 0; f < 7; ++f) {
          LDS16(lXb + 28 * KB + f * 4096 + c.tid * 16) = x[f];
          if (f < 4) { U[f] = pk8(acc[f * 2], acc[f * 2 + 1]); PIN_V(U[f]); }
          SB();
        }
        if (bad) {
          POLL2(true, XBAD, load_x(1, s1), 1);
          stage_x(1);
        }
        CBAR();
        BAR();
        SB();
      }
    }
    STAMP(t, 1);
    v4u m[4];
    CBAR();
#pragma unroll
    for (int k = 0; k < 4; ++k) m[k] = ld16c(c.rs, mo[k] + slot);
    SB();
#pragma unroll
    for (int hh = 0; hh < 4; ++hh) { U[4 + hh] = pk8(acc[hh * 2], acc[hh * 2 + 1]); PIN_V(U[4 + hh]); }
    SB();
    have = !XBAD;
    if (lane == 0) lhave[w] = have ? 1u : 0u;
    SB();
    POLL(anysent(mx4(mx4(mx4(mx4(0u, m[0]), m[1]), m[2]), m[3])),
         { _Pragma("unroll") for (int k = 0; k < 4; ++k) m[k] = ld16c(c.rs, mo[k] + slot); }, 0);
#pragma unroll
    for (int k = 0; k < 3; ++k) LDS16(lmem + k * 4096 + c.tid * 16) = m[k];
    LDS16(lmem + 12288 + (c.tid & 127) * 16) = m[3];
    STAMP(t, 2);
    v4u zA[2];
    {
      f4 z[4] = {Z4, Z4, Z4, Z4};
      const unsigned ub = u_in(1, s1) + uo;
      DRAIN();
      st4c(c.rs, fo, (unsigned)min(s0 + 1, NSEG));
      SB();
#pragma unroll
      for (int hh = 0; hh < 4; ++hh) {
        z[hh] = mfma32(m[0], W[hh * 14], z[hh]);
        SB();
        st16c(c.rs, ub + hh * KB, U[4 + hh]);
        SB();
      }
      st16c(c.rs, (s1 >= 0 ? rec_in(1, s1 + AD) : OOB) + zo, S4);
      SB();
      BAR();
      v4u mf[14];
#pragma unroll
      for (int ks = 3; ks < 14; ++ks)
        if (ks != 12) mf[ks] = LDS16(lmem + KSO(ks));
      const v4u hv = LDS16((unsigned char*)lhave);
      mf[1] = m[1]; mf[2] = m[2]; mf[12] = m[3];
      SB();
#pragma unroll
      for (int st = 1; st < 14; ++st) {
        const int ks = (st < 3) ? st : (st == 3) ? 12 : (st < 13) ? st - 1 : 13;
#pragma unroll
        for (int hh = 0; hh < 4; ++hh) {
          z[hh] = mfma32(mf[ks], W[hh * 14 + ks], z[hh]);
          if (st == 1 && hh == 0) {
            SB();
            st8c(c.rs, (t >= 1 ? pR : OOB) + (unsigned)((t - 1) & 3) * 32 * KB, S2);
            SB();
          }
        }
        SB();
      }
      allhave = __builtin_amdgcn_readfirstlane(hv.x & hv.y & hv.z & hv.w) != 0u;
      unsigned zl[4], zh[4];
#pragma unroll
      for (int hh = 0; hh < 4; ++hh) { zl[hh] = pk2(z[hh][0], z[hh][1]); zh[hh] = pk2(z[hh][2], z[hh][3]); }
      swap16(zl[0], zl[1]); swap16(zl[2], zl[3]); swap32(zl[0], zl[2]); swap32(zl[1], zl[3]);
      swap16(zh[0], zh[1]); swap16(zh[2], zh[3]); swap32(zh[0], zh[2]); swap32(zh[1], zh[3]);
      zA[0] = v4u{zl[0], zh[0], zl[1], zh[1]};
      zA[1] = v4u{zl[2], zh[2], zl[3], zh[3]};
    }
    STAMP(t, 3);
    if (t == NT - 1) {
      CBAR();
      st16c(c.rs, rec_in(1, s1) + zo, ns4(zA[1]));
      asm volatile("s_waitcnt vmcnt(1)" ::: "memory");
      st4c(c.rs, fo + 16, (unsigned)NSEG);
      STAMP(t, 6);
      break;
    }
    v4u E[4], sv[4];
    v2u xs;
    auto load_e = [&]() {
#pragma unroll
      for (int hh = 0; hh < 4; ++hh) E[hh] = ld16c(c.rs, eo[hh] + eslot);
      xs = ld8c(c.rs, xo + xslot);
    };
    auto load_s = [&]() {
#pragma unroll
      for (int k = 0; k < 4; ++k) sv[k] = ld16c(c.rs, so[k] + slot);
    };
    CBAR();
    load_e();
    SB();
    stage_x(0);
    SB();
    if (E2) __builtin_amdgcn_s_sleep(E2);
    CBAR();
    load_s();
    POLL(anysent(mx2(mx4(mx4(mx4(mx4(0u, E[0]), E[1]), E[2]), E[3]), xs)), { load_e(); load_s(); }, 0);
    SB();
    f4 ox[4];
#pragma unroll
    for (int hh = 0; hh < 4; ++hh) {
      const f4 o0 = mfma32(U[hh], E[hh], Z4), o1 = mfma32(U[4 + hh], E[hh], Z4);
      ox[hh] = (i16 < 8) ? o0 : o1;
    }
#pragma unroll
    for (int hh = 0; hh < 4; ++hh) PIN_V(ox[hh]);
    STAMP(t, 4);
    SB();
    POLL(anysent(mx4(mx4(mx4(mx4(0u, sv[0]), sv[1]), sv[2]), sv[3])), load_s(), 0);
    STAMP(t, 5);
    DRAIN();
    st4c(c.rs, fo + 16, (unsigned)max(s1 + 1, 0));
    {
      f4 a = u2f(sv[0]) + u2f(sv[2]), b = u2f(sv[1]) + u2f(sv[3]);
      const float mxx = __uint_as_float(xs.x), lx = __uint_as_float(xs.y);
      const float mm = mxf4(b, mxf4(a, mxx));
      const float ml = mm * LOG2E;
#pragma unroll
      for (int r = 0; r < 4; ++r) { a[r] = ex2(a[r] * LOG2E - ml); b[r] = ex2(b[r] * LOG2E - ml); }
      const float ax = ex2(mxx * LOG2E - ml);
      const float den = ((a[0] + a[1]) + (a[2] + a[3])) + ((b[0] + b[1]) + (b[2] + b[3])) + ax * lx;
      const float inv = rcp(den);
      const v4u P = pk8(a * inv, b * inv);
      const f4 d0 = mfma32(zA[0], P, Z4), d1 = mfma32(zA[1], P, Z4);
      float wx[4];
      rbcast4(ax * inv, wx);
      f4 acc = (ox[0] * wx[0] + ox[1] * wx[1]) + (ox[2] * wx[2] + ox[3] * wx[3]);
      acc += (i16 < 8) ? d0 : d1;
      if (w != 0) LDSF4(lred + w * KB + lane * 16) = acc;
      BAR();
      if (w == 0) {
        acc = (acc + LDSF4(lred + KB + lane * 16)) + (LDSF4(lred + 2 * KB + lane * 16) + LDSF4(lred + 3 * KB + lane * 16));
        v2u o = ns2(pk4(acc));
        if (i16 >= 8 && t + 1 <= LAG) o = m0;
        const unsigned nslot = (unsigned)((t + 1) & 3) * 32 * KB;
        st8c(c.rs, pH + nslot, o);
        st8c(c.rs, pG + nslot, o);
        st8c(c.rs, pC + nslot, o);
      }
      STAMP(t, 6);
      st16c(c.rs, rec_in(0, s0) + zo, ns4(zA[0]));
      st16c(c.rs, rec_in(1, s1) + zo, ns4(zA[1]));
    }
  }
}

template <int L>
DEVI void role_GX(Ctx& c, int h, int p, unsigned char* lds) {
  PMASK(5);
  asm volatile("; layer %2" : "+v"(c.tid), "+v"(c.lane) : "n"(L) : "memory");
  const int lane = c.lane, w = c.wave;
  const int i16 = lane & 15, q = lane >> 4;
  unsigned char* lX = lds;
  unsigned char* lG = lds + 28 * KB;
  unsigned char* lred = lds + 60 * KB;
  v4u W[98];
  {
    const unsigned b0 = WG_OFF + (unsigned)(h * 14 + 3 * w) * 28 * KB + lane * 16;
#pragma unroll
    for (int f = 0; f < 84; ++f) W[f] = ld16(c.rs, b0 + f * KB);
    const unsigned b1 = WG_OFF + (unsigned)(h * 14 + 12 + (w >> 1)) * 28 * KB + (w & 1) * 7 * KB + lane * 16;
#pragma unroll
    for (int k = 0; k < 7; ++k) { W[84 + k] = ld16(c.rs, b1 + k * KB); W[91 + k] = ld16(c.rs, b1 + (14 + k) * KB); }
#pragma unroll
    for (int f = 0; f < 56; ++f) PIN_A(W[f]);
  }
  const unsigned sides = get_sides(c);
  const unsigned sC = sides;
  const int kx = 7 * (w & 1);
  unsigned mo[4];
#pragma unroll
  for (int k = 0; k < 4; ++k) {
    const int vu = 4 * w + k, ks = vu < 12 ? vu : 12 + ((vu - 12) >> 1);
    mo[k] = rmap(c, L_MEMG + L * 8 * KB + ks * 512 + q * 128 + (i16 & 7) * 16, sides ^ (unsigned)p);
  }
  const unsigned eo = rmap(c, L_E + h * KB + (q * 16 + L * 8 + (i16 & 7)) * 16 + (i16 >> 3) * 8, sC);
  const unsigned xo = rmap(c, L_XS + (h * 2 + L) * 128 + (i16 & 7) * 8, sC);
  v4u x[7];
  auto load_x = [&](int s) {
    const unsigned xb = rec_in(L, s) + R_XN + c.tid * 16;
#pragma unroll
    for (int f = 0; f < 7; ++f) x[f] = ld16c(c.rs, xb + f * 4096);
  };
  bool have = false;
  for (int t = p; t < NT; t += 2) {
    const int s = t - L * LAG;
    const bool act = (s >= 0 && s < NSEG);
    v4u g[8];
    STAMP(t, 0);
    if (act) {
      if (!have) {
        CBAR();
        load_x(s);
        POLL(anysent(mx4(mx4(mx4(mx4(mx4(mx4(mx4(0u, x[0]), x[1]), x[2]), x[3]), x[4]), x[5]), x[6])), load_x(s), 1);
      }
#pragma unroll
      for (int f = 0; f < 7; ++f) LDS16(lX + f * 4096 + c.tid * 16) = x[f];
      BAR();
      STAMP(t, 1);
#pragma unroll 1
      for (int tau = 0; tau < 2; ++tau) {
        const unsigned char* xp = lX + tau * 14 * KB + lane * 16;
        const unsigned xnext = (tau ? rec_in(L, s + 2) : OOB) + R_XN + c.tid * 16;
        f4 acc[8] = {Z4, Z4, Z4, Z4, Z4, Z4, Z4, Z4};
        v4u xs[7];
#pragma unroll
        for (int k = 0; k < 7; ++k) xs[k] = LDS16(xp + (kx + k) * KB);
        v4u x0 = LDS16(xp), x1 = LDS16(xp + KB);
        SB();
#pragma unroll
        for (int k = 0; k < 7; ++k) {
          acc[6] = mfma32(W[84 + k], xs[k], acc[6]);
          acc[7] = mfma32(W[91 + k], xs[k], acc[7]);
        }
        SB();
        unsigned char* gp = lG + tau * 16 * KB + lane * 16;
#pragma unroll
        for (int ks = 0; ks < 14; ++ks) {
          const v4u xk = x0;
          x0 = x1;
          if (ks + 2 < 14) x1 = LDS16(xp + (ks + 2) * KB);
          if (ks == 2) LDS16(gp + (12 + w) * KB) = pk8(acc[6], acc[7]);
#pragma unroll
          for (int pp = 0; pp < 3; ++pp) {
            acc[pp * 2 + 0] = mfma32(W[pp * 28 + ks], xk, acc[pp * 2 + 0]);
            acc[pp * 2 + 1] = mfma32(W[pp * 28 + 14 + ks], xk, acc[pp * 2 + 1]);
            if (L == 0 && pp == 0 && ks >= 3 && ks < 10) { SB(); x[ks - 3] = ld16c(c.rs, xnext + (ks - 3) * 4096); SB(); }
          }
          SB();
        }
#pragma unroll
        for (int pp = 0; pp < 3; ++pp) LDS16(gp + (3 * w + pp) * KB) = pk8(acc[pp * 2], acc[pp * 2 + 1]);
      }
      BAR();
#pragma unroll
      for (int k = 0; k < 4; ++k) { g[k] = LDS16(lG + (4 * w + k) * KB + lane * 16); g[4 + k] = LDS16(lG + (16 + 4 * w + k) * KB + lane * 16); }
    } else {
#pragma unroll
      for (int k = 0; k < 8; ++k) g[k] = v4u{0u, 0u, 0u, 0u};
    }
    STAMP(t, 2);
    {
      const unsigned slot = (unsigned)(t & 3) * 32 * KB;
      v4u m[4];
      CBAR();
#pragma unroll
      for (int k = 0; k < 4; ++k) m[k] = ld16c(c.rs, mo[k] + slot);
      POLL(anysent(mx4(mx4(mx4(mx4(0u, m[0]), m[1]), m[2]), m[3])),
           { _Pragma("unroll") for (int k = 0; k < 4; ++k) m[k] = ld16c(c.rs, mo[k] + slot); }, 0);
      STAMP(t, 3);
      f4 a0 = Z4, a1 = Z4;
#pragma unroll
      for (int k = 0; k < 4; ++k) { a0 = mfma32(g[k], m[k], a0); a1 = mfma32(g[4 + k], m[k], a1); }
      LDSF4(lred + 4 * KB + w * KB + lane * 16) = (i16 < 8) ? a0 : a1;
      BAR();
      if (w == 0) {
        const unsigned char* rp = lred + 4 * KB + lane * 16;
        f4 v = (LDSF4(rp) + LDSF4(rp + KB)) + (LDSF4(rp + 2 * KB) + LDSF4(rp + 3 * KB));
        float m1 = mxf4(v, NEG);
        m1 = mx3(m1, ror8(m1), NEG);
        m1 = rmax4(m1);
        const float ml = m1 * LOG2E;
#pragma unroll
        for (int r = 0; r < 4; ++r) v[r] = ex2(v[r] * LOG2E - ml);
        const unsigned eslot = (unsigned)(t & 7) * 32 * KB, xslot = (unsigned)(t & 7) * 16 * KB;
        st8c(c.rs, eo + eslot, act ? ns2(pk4(v)) : v2u{0u, 0u});
        float ls = (v[0] + v[1]) + (v[2] + v[3]);
        ls += ror8(ls);
        ls = rsum4(ls);
        if (lane < 8) st8c(c.rs, xo + xslot, act ? ns2(v2u{__float_as_uint(m1), __float_as_uint(ls)}) : v2u{__float_as_uint(-1.0e30f), 0u});
        if (t >= 2) {
          st8c(c.rs, eo + (unsigned)((t - 2) & 7) * 32 * KB, S2);
          if (lane < 8) st8c(c.rs, xo + (unsigned)((t - 2) & 7) * 16 * KB, S2);
        }
      }
      STAMP(t, 4);
    }
    if (act) {
      const int tq = w & 1, kh = w >> 1;
      f4 a0 = Z4, a1 = Z4;
#pragma unroll
      for (int hf = 0; hf < 3; ++hf) {
        v4u ga[3], gb[3], xq[3];
#pragma unroll
        for (int k = 0; k < 3; ++k)
          if (hf * 3 + k < 8) {
            const int vu = kh * 8 + hf * 3 + k, ks = vu < 12 ? vu : 12 + ((vu - 12) >> 1);
            ga[k] = LDS16(lG + vu * KB + lane * 16);
            gb[k] = LDS16(lG + (16 + vu) * KB + lane * 16);
            xq[k] = LDS16(lX + (tq * 14 + ks) * KB + lane * 16);
          }
        SB();
#pragma unroll
        for (int k = 0; k < 3; ++k)
          if (hf * 3 + k < 8) { a0 = mfma32(ga[k], xq[k], a0); a1 = mfma32(gb[k], xq[k], a1); }
        SB();
      }
      if (!kh) { LDSF4(lred + (tq * 2 + 0) * KB + lane * 16) = a0; LDSF4(lred + (tq * 2 + 1) * KB + lane * 16) = a1; }
      BAR();
      if (L == 0) have = !anysent(mx4(mx4(mx4(mx4(mx4(mx4(mx4(0u, x[0]), x[1]), x[2]), x[3]), x[4]), x[5]), x[6]));
      if (kh) {
        a0 += LDSF4(lred + (tq * 2 + 0) * KB + lane * 16);
        a1 += LDSF4(lred + (tq * 2 + 1) * KB + lane * 16);
        const int lim = tok_of(tq, i16) - 8 * q;
#pragma unroll
        for (int r = 0; r < 4; ++r) {
          a0[r] = (r <= lim) ? a0[r] : NEG;
          a1[r] = (4 + r <= lim) ? a1[r] : NEG;
        }
        const float m = rmax4(mxf4(a1, mxf4(a0, NEG)));
        const float ml = m * LOG2E;
        float lsum = 0.f;
#pragma unroll
        for (int r = 0; r < 4; ++r) {
          a0[r] = (r <= lim) ? ex2(a0[r] * LOG2E - ml) : 0.f;
          a1[r] = (4 + r <= lim) ? ex2(a1[r] * LOG2E - ml) : 0.f;
          lsum += a0[r] + a1[r];
        }
        lsum = rsum4(lsum);
        const unsigned o1 = R_ETT + (unsigned)(h * 2 + tq) * KB + lane * 16, o2 = R_ETS + (unsigned)((tq * 16 + h) * 16 + i16) * 8;
        st16c(c.rs, rec_off(L, s) + o1, ns4(pk8(a0, a1)));
        if (q == 0) st8c(c.rs, rec_off(L, s) + o2, ns2(v2u{__float_as_uint(m), __float_as_uint(lsum)}));
        st16c(c.rs, rec_in(L, s + AD) + o1, S4);
        if (q == 0) st8c(c.rs, rec_in(L, s + AD) + o2, S2);
      }
    }
    STAMP(t, 5);
  }
}

template <int L>
DEVI void role_O(Ctx& c, int tau, int cg, unsigned char* lds, __hip_bfloat16* out) {
  PMASK(8);
  const int lane = c.lane, w = c.wave;
  const int i16 = lane & 15, q = lane >> 4;
  const int ct = 4 * cg + w;
  const unsigned po = (unsigned)(tau * 14 + (ct >> 1)) * KB + (unsigned)((2 * (ct & 1) + (q >> 1)) * 16 + i16) * 16 + (q & 1) * 8;
  v4u U[16], e[4], sv[4], z[4];
  v2u es, xr;
  auto load_u = [&](int s) {
    const unsigned ub = u_in(L, s) + (unsigned)(4 * cg * 16 + 4 * w) * KB + lane * 16;
#pragma unroll
    for (int k = 0; k < 4; ++k)
#pragma unroll
      for (int hh = 0; hh < 4; ++hh) U[k * 4 + hh] = ld16c(c.rs, ub + (k * 16 + hh) * KB);
    xr = ld8c(c.rs, rec_in(L, s) + R_XR + po);
  };
  auto load_e = [&](int s) {
    const unsigned rb = rec_off(L, s);
#pragma unroll
    for (int hh = 0; hh < 4; ++hh) e[hh] = ld16c(c.rs, rb + R_ETT + (unsigned)((4 * w + hh) * 2 + tau) * KB + lane * 16);
    es = ld8c(c.rs, rb + R_ETS + (unsigned)((tau * 16 + 4 * w) * 16 + lane) * 8);
  };
  auto load_z = [&](int s) {
#pragma unroll
    for (int k = 0; k < 4; ++k) z[k] = ld16c(c.rs, rec_off(L, s) + R_ZB + (unsigned)((4 * cg + k) * 4 + w) * KB + lane * 16);
  };
  auto load_s = [&](int s) {
    const unsigned rb = rec_off(L, s);
#pragma unroll
    for (int k = 0; k < 4; ++k) sv[k] = ld16c(c.rs, rb + R_STM + (unsigned)((k * 2 + tau) * 16 + 4 * w) * 256 + lane * 16);
    load_z(s);
  };
  const int fph = (lane >> 2) & 3;
  const unsigned fo = (lane < 4 * NPH) ? F_U + (unsigned)(fph * 28 + 4 * cg + (lane & 3)) * 64 + L * 16 + w * 4
                      : (lane >= 16 && lane < 24) ? F_XR + (unsigned)((L * 2 + tau) * 2 + fph) * 64 + (lane & 3) * 4 : OOB;
  int fl = 0;
  for (int s = 0; s < NSEG; ++s) {
    unsigned char* lred = lds + (s & 1) * 16 * KB;
    STAMP(s, 0);
    {
      const int need = (lane < 24 && fph == (lane < 16 ? (s + L * LAG) % NPH : (s & 1))) ? s + 1 : 0;
      POLL(__builtin_amdgcn_ballot_w64(fl < need) != 0ull, fl = (int)ld4c(c.rs, fo), 1);
      CBAR();
      load_u(s);
    }
    STAMP(s, 1);
    CBAR();
    load_e(s);
    load_s(s);
    fl = (int)ld4c(c.rs, fo);
    POLL(anysent(mx4(mx4(mx4(mx4(mx2(mx4(mx4(mx4(mx4(0u, e[0]), e[1]), e[2]), e[3]), es), sv[0]), sv[1]), sv[2]), sv[3])),
         { load_e(s); load_s(s); fl = (int)ld4c(c.rs, fo); }, PO);
    SB();
    {
      f4 a = u2f(sv[0]) + u2f(sv[2]), b = u2f(sv[1]) + u2f(sv[3]);
      const float mt = __uint_as_float(es.x), lt = __uint_as_float(es.y);
      const float mm = mxf4(b, mxf4(a, mt));
      const float ml = mm * LOG2E;
#pragma unroll
      for (int r = 0; r < 4; ++r) { a[r] = ex2(a[r] * LOG2E - ml); b[r] = ex2(b[r] * LOG2E - ml); }
      const float at = ex2(mt * LOG2E - ml);
      const float den = ((a[0] + a[1]) + (a[2] + a[3])) + ((b[0] + b[1]) + (b[2] + b[3])) + at * lt;
      const float inv = rcp(den);
      const v4u P = pk8(a * inv, b * inv);
      float wt[4];
      rbcast4(at * inv, wt);
      f4 acc[4];
#pragma unroll
      for (int hh = 0; hh < 4; ++hh) {
        v4u en;
#pragma unroll
        for (int r = 0; r < 4; ++r) en[r] = pk2(bflo(e[hh][r]) * wt[hh], bfhi(e[hh][r]) * wt[hh]);
#pragma unroll
        for (int k = 0; k < 4; ++k) acc[k] = mfma32(U[k * 4 + hh], en, hh ? acc[k] : Z4);
      }
      SB();
      POLL(anysent(mx4(mx4(mx4(mx4(0u, z[0]), z[1]), z[2]), z[3])), { load_z(s); fl = (int)ld4c(c.rs, fo); }, PZ);
      TOUCH(fl);
      STAMP(s, 2);
#pragma unroll
      for (int k = 0; k < 4; ++k) acc[k] = mfma32(z[k], P, acc[k]);
#pragma unroll
      for (int k = 0; k < 4; ++k) LDSF4(lred + (k * 4 + w) * KB + lane * 16) = acc[k];
    }
    BAR();
    {
      const unsigned char* rp = lred + w * 4 * KB + lane * 16;
      const f4 acc = (LDSF4(rp) + LDSF4(rp + KB)) + (LDSF4(rp + 2 * KB) + LDSF4(rp + 3 * KB));
      const v2u y = pk4(acc);
      const v2u o = v2u{pk2(bflo(xr.x) + bflo(y.x), bfhi(xr.x) + bfhi(y.x)), pk2(bflo(xr.y) + bflo(y.y), bfhi(xr.y) + bfhi(y.y))};
      if (L == 0) {
        st8c(c.rs, rec_off(0, s) + R_X2 + po, ns2(o));
        st8c(c.rs, rec_in(0, s + AD) + R_X2 + po, S2);
      } else {
        *(v2u*)((unsigned char*)out + (size_t)(s * 32 + tok_of(tau, i16)) * 896 + (ct * 16 + 4 * q) * 2) = o;
      }
    }
    STAMP(s, 3);
  }
}

template <int L>
DEVI void role_AH(Ctx& c, int a, unsigned char* lds) {
  PMASK(9);
  asm volatile("; layer %2" : "+v"(c.tid), "+v"(c.lane) : "n"(L) : "memory");
  const int lane = c.lane, w = c.wave;
  const int i16 = lane & 15, q = lane >> 4;
  unsigned char* lK = lds;
  unsigned char* lV = lds + 18 * KB;
  unsigned char* lQ = lds + 36 * KB;
  unsigned char* lmg = lds + 38 * KB;
  unsigned char* lms = lds + 42 * KB;
  const int tau = w & 1, g = w >> 1;
  v4u x[14];
  auto load_x = [&](int s) {
    const unsigned xb = (L == 0 ? X0_OFF + (unsigned)s * SEGX : rec_off(0, s) + R_X2) + (unsigned)(tau * 14) * KB + lane * 16;
#pragma unroll
    for (int f = 0; f < 14; ++f) x[f] = (L == 0) ? ld16(c.rs, xb + f * KB) : ld16c(c.rs, xb + f * KB);
  };
  auto xbad = [&]() {
    unsigned m = 0u;
#pragma unroll
    for (int f = 0; f < 14; ++f) m = mx4(m, x[f]);
    return anysent(m);
  };
  load_x(0);
  v4u W[42];
#pragma unroll
  for (int tl = 0; tl < 3; ++tl) {
    const unsigned b0 = WQ_OFF + (unsigned)(a * 6 + (tl < 2 ? 2 * g + tl : 4 + g)) * 14 * KB + lane * 16;
#pragma unroll
    for (int k = 0; k < 14; ++k) W[tl * 14 + k] = ld16(c.rs, b0 + k * KB);
  }
  for (int o = c.tid * 16; o < 36 * (int)KB; o += 4096) LDS16(lK + o) = v4u{0u, 0u, (o < 18 * (int)KB && q == 3) ? 0xFF7Fu : 0u, 0u};
  f4 cs0 = Z4, cs1 = Z4, cs2 = Z4;
  {
    const v4u one = {0x3F803F80u, 0x3F803F80u, 0x3F803F80u, 0x3F803F80u};
#pragma unroll
    for (int k = 0; k < 14; ++k) { cs0 = mfma32(W[k], one, cs0); cs1 = mfma32(W[14 + k], one, cs1); cs2 = mfma32(one, W[28 + k], cs2); }
  }
#pragma unroll
  for (int f = 0; f < 42; ++f) PIN_A(W[f]);
  const float csv = cs2[0];
  const float vone = (g == 1 && i16 == 12) ? 1.0f : 0.0f;
  const float qone = (g == 0 && q == 3) ? 1.0f : 0.0f;
  const float sc = 0.18898223650461362f * LOG2E;
  const int lim = tok_of(tau, i16) - 8 * q;
  v4u dm;
#pragma unroll
  for (int e = 0; e < 4; ++e) dm[e] = ((2 * e <= lim) ? 0xFFFFu : 0u) | ((2 * e + 1 <= lim) ? 0xFFFF0000u : 0u);
  const int c0 = 28 * a + 4 * q, c1 = c0 + 16;
  const unsigned oa = (g == 0) ? R_OA + (unsigned)tau * 14 * KB + (c0 >> 5) * KB + (((c0 >> 3) & 3) * 16 + i16) * 16 + (c0 & 4) * 2 : OOB;
  const unsigned ob = (g == 0 && q < 3) ? R_OA + (unsigned)tau * 14 * KB + (c1 >> 5) * KB + (((c1 >> 3) & 3) * 16 + i16) * 16 + (c1 & 4) * 2 : OOB;
  CBAR();
#pragma unroll
  for (int f = 0; f < 14; ++f) TOUCH(x[f]);
  BAR();
  int slot = 0;
  for (int s = 0; s < NSEG; ++s) {
    STAMP(s, 0);
    v4u kf[10], vf[10];
    int sl[4];
#pragma unroll
    for (int b = 0; b < 4; ++b) {
      int k = slot - ((b == 0 && g == 0) ? 8 : 4 * g + b);
      k += (k < 0) ? 9 : 0;
      sl[b] = k;
      kf[2 * b] = LDS16(lK + (k * 2) * KB + lane * 16); kf[2 * b + 1] = LDS16(lK + (k * 2 + 1) * KB + lane * 16);
    }
    if (L == 1) POLL(xbad(), load_x(s), 2);
    STAMP(s, 1);
    SB();
    {
      f4 a0 = Z4, a1 = Z4, a2 = Z4, gm = Z4;
#pragma unroll
      for (int f = 0; f < 14; ++f) {
        gm = mfma32(x[f], x[f], gm);
        a1 = mfma32(W[14 + f], x[f], a1);
        SB();
      }
      float mean, rstd, mr;
      f4 rs, ms;
      float sum, sq;
      f4 su, sv;
#pragma unroll
      for (int f = 0; f < 14; ++f) {
        a0 = mfma32(W[f], x[f], a0);
        a2 = mfma32(x[f], W[28 + f], a2);
        if (f == 1) {
          const float d = (i16 & 2) ? ((i16 & 1) ? gm[3] : gm[2]) : ((i16 & 1) ? gm[1] : gm[0]);
          const int di = __float_as_int(d), si = __float_as_int(a1[1]);
          sq = __int_as_float(__builtin_amdgcn_ds_bpermute(((i16 >> 2) * 16 + i16) * 4, di));
          sum = __int_as_float(__builtin_amdgcn_ds_bpermute((48 + i16) * 4, si));
#pragma unroll
          for (int r = 0; r < 4; ++r) {
            sv[r] = __int_as_float(__builtin_amdgcn_ds_bpermute((20 * q + r) * 4, di));
            su[r] = __int_as_float(__builtin_amdgcn_ds_bpermute((48 + 4 * q + r) * 4, si));
          }
        }
        if (f == 7) {
          mean = sum * (1.0f / 448.0f);
          rstd = rsq(fmaxf(sq * (1.0f / 448.0f) - mean * mean, 0.f) + 1e-5f);
          mr = mean * rstd;
        }
        if (f >= 8 && f < 12) {
          const int r = f - 8;
          const float mn = su[r] * (1.0f / 448.0f);
          rs[r] = rsq(fmaxf(sv[r] * (1.0f / 448.0f) - mn * mn, 0.f) + 1e-5f);
          ms[r] = mn * rs[r];
        }
        SB();
      }
      a0 = a0 * rstd - cs0 * mr;
      a1 = a1 * rstd - cs1 * mr;
      a1[0] += qone;
      a1[1] = (q == 3) ? 0.f : a1[1];
      LDS16((g == 0 ? lQ + tau * KB : lK + (slot * 2 + tau) * KB) + lane * 16) = pk8(a0, a1);
      a2 = a2 * rs - ms * csv + vone;
      *(v2u*)(lV + (slot * 2 + g) * KB + lane * 16 + tau * 8) = pk4(a2);
    }
    BAR();
    STAMP(s, 2);
    {
      const v4u qf = LDS16(lQ + tau * KB + lane * 16);
      if (g == 0) { kf[8] = LDS16(lK + (slot * 2) * KB + lane * 16); kf[9] = LDS16(lK + (slot * 2 + 1) * KB + lane * 16); }
      SB();
      f4 scv[4][2];
#pragma unroll
      for (int b = 0; b < 4; ++b) {
        scv[b][0] = mfma32(kf[2 * b], qf, Z4);
        scv[b][1] = mfma32(kf[2 * b + 1], qf, Z4);
      }
      f4 e0 = Z4, e1 = Z4;
      if (g == 0) { e0 = mfma32(kf[8], qf, Z4); e1 = mfma32(kf[9], qf, Z4); }
      SB();
#pragma unroll
      for (int b = 0; b < 4; ++b) { vf[2 * b] = LDS16(lV + (sl[b] * 2) * KB + lane * 16); vf[2 * b + 1] = LDS16(lV + (sl[b] * 2 + 1) * KB + lane * 16); }
      if (g == 0) { vf[8] = LDS16(lV + (slot * 2) * KB + lane * 16); vf[9] = LDS16(lV + (slot * 2 + 1) * KB + lane * 16); }
      SB();
#pragma unroll
      for (int b = 0; b < 4; ++b) { PIN_V(scv[b][0]); PIN_V(scv[b][1]); }
      if (g == 0) {
        PIN_V(e0); PIN_V(e1);
#pragma unroll
        for (int r = 0; r < 4; ++r) {
          scv[0][0][r] = (r <= lim) ? e0[r] : scv[0][0][r];
          scv[0][1][r] = (4 + r <= lim) ? e1[r] : scv[0][1][r];
        }
      }
      float m = -1.0e30f;
#pragma unroll
      for (int b = 0; b < 4; ++b) m = mxf4(scv[b][1], mxf4(scv[b][0], m));
      m = rmax4(m);
      const float ml = m * sc;
      f4 o0 = Z4, o1 = Z4;
      const unsigned xn = (L == 0 ? X0_OFF + (unsigned)min(s + 1, NSEG - 1) * SEGX : rec_off(0, min(s + 1, NSEG - 1)) + R_X2) + (unsigned)(tau * 14) * KB + lane * 16;
#pragma unroll
      for (int bb = 0; bb < 4; ++bb) {
        const int b = 3 - bb;
#pragma unroll
        for (int r = 0; r < 4; ++r) {
          scv[b][0][r] = ex2(scv[b][0][r] * sc - ml);
          scv[b][1][r] = ex2(scv[b][1][r] * sc - ml);
          if (bb * 4 + r < 14) {
            SB();
            x[bb * 4 + r] = (L == 0) ? ld16(c.rs, xn + (bb * 4 + r) * KB) : ld16c(c.rs, xn + (bb * 4 + r) * KB);
            SB();
          }
        }
        v4u p = pk8(scv[b][0], scv[b][1]);
        if (b == 0 && g == 0) {
          const v4u pd = p & dm;
          p = p & ~dm;
          o0 = mfma32(vf[8], pd, o0);
          o1 = mfma32(vf[9], pd, o1);
        }
        o0 = mfma32(vf[2 * b], p, o0);
        o1 = mfma32(vf[2 * b + 1], p, o1);
      }
      if (g == 1) {
        LDSF4(lmg + (tau * 2 + 0) * KB + lane * 16) = o0;
        LDSF4(lmg + (tau * 2 + 1) * KB + lane * 16) = o1;
        *(float*)(lms + (tau * 64 + lane) * 4) = m;
      }
      BAR();
      v2u y0 = S2, y1 = S2;
      if (g == 0) {
        const float mo = *(const float*)(lms + (tau * 64 + lane) * 4);
        const float mt = mx3(m, mo, NEG);
        const float fa = ex2((m - mt) * sc), fb = ex2((mo - mt) * sc);
        const f4 r0 = o0 * fa + LDSF4(lmg + (tau * 2 + 0) * KB + lane * 16) * fb;
        const f4 r1 = o1 * fa + LDSF4(lmg + (tau * 2 + 1) * KB + lane * 16) * fb;
        float dn[4];
        rbcast4(r1[0], dn);
        const float inv = rcp(dn[3]);
        y0 = ns2(pk4(r0 * inv));
        y1 = ns2(pk4(r1 * inv));
      }
      st8c(c.rs, rec_off(L, s) + oa, y0);
      st8c(c.rs, rec_off(L, s) + ob, y1);
      st8c(c.rs, (s + AD < NSEG) ? rec_off(L, s + AD) + oa : OOB, S2);
      st8c(c.rs, (s + AD < NSEG) ? rec_off(L, s + AD) + ob : OOB, S2);
      STAMP(s, 3);
    }
    slot = (slot == 8) ? 0 : slot + 1;
  }
}

template <int l>
DEVI void role_AO(Ctx& c, int tau, int par, unsigned char* lds) {
  PMASK(9);
  asm volatile("; layer %2" : "+v"(c.tid), "+v"(c.lane) : "n"(l) : "memory");
  const int lane = c.lane, w = c.wave;
  const int i16 = lane & 15, q = lane >> 4;
  unsigned char* lO = lds;
  unsigned char* lx = lds + 14 * KB;
  unsigned char* lst = lds + 18 * KB;
  v4u W[98];
  {
    const unsigned b0 = WO_OFF + (unsigned)(3 * w) * 28 * KB + lane * 16;
#pragma unroll
    for (int f = 0; f < 84; ++f) W[f] = ld16(c.rs, b0 + f * KB);
    const unsigned b1 = WO_OFF + (unsigned)(12 + (w >> 1)) * 28 * KB + (w & 1) * 7 * KB + lane * 16;
#pragma unroll
    for (int k = 0; k < 7; ++k) { W[84 + k] = ld16(c.rs, b1 + k * KB); W[91 + k] = ld16(c.rs, b1 + (14 + k) * KB); }
#pragma unroll
    for (int f = 0; f < 56; ++f) PIN_A(W[f]);
  }
  const int ux = 12 + (w >> 1), kx = 7 * (w & 1);
  const bool own4 = !(w & 1);
  const unsigned fo = (lane == 0) ? F_XR + (unsigned)((l * 2 + tau) * 2 + par) * 64 + w * 4 : OOB;
  for (int s = par; s < NSEG; s += 2) {
    const unsigned rb = rec_off(l, s);
    STAMP(s, 0);
    {
      const unsigned ob = rb + R_OA + (unsigned)tau * 14 * KB;
      v4u v[4];
      auto ldo = [&]() {
        v[0] = ld16c(c.rs, ob + c.tid * 16); v[1] = ld16c(c.rs, ob + 4096 + c.tid * 16); v[2] = ld16c(c.rs, ob + 8192 + c.tid * 16);
        v[3] = ld16c(c.rs, ob + 12288 + (c.tid & 127) * 16);
      };
      CBAR();
      ldo();
      DRAIN();
      st4c(c.rs, fo, (unsigned)max(s - 1, 0));
      POLL(anysent(mx4(mx4(mx4(mx4(0u, v[0]), v[1]), v[2]), v[3])), ldo(), 1);
      LDS16(lO + c.tid * 16) = v[0];
      LDS16(lO + 4096 + c.tid * 16) = v[1];
      LDS16(lO + 8192 + c.tid * 16) = v[2];
      LDS16(lO + 12288 + (c.tid & 127) * 16) = v[3];
    }
    STAMP(s, 1);
    v4u xr[4];
    auto ldr = [&]() {
#pragma unroll
      for (int p = 0; p < 4; ++p) {
        const int u = (p < 3) ? 3 * w + p : ux;
        if (l == 0) xr[p] = ld16(c.rs, X0_OFF + (unsigned)s * SEGX + (unsigned)(tau * 14 + u) * KB + lane * 16);
        else xr[p] = ld16c(c.rs, rec_off(0, s) + R_X2 + (unsigned)(tau * 14 + u) * KB + lane * 16);
      }
    };
    BAR();
    f4 acc[8] = {Z4, Z4, Z4, Z4, Z4, Z4, Z4, Z4};
    {
      const unsigned char* xp = lO + lane * 16;
      v4u xs[7];
#pragma unroll
      for (int k = 0; k < 7; ++k) xs[k] = LDS16(xp + (kx + k) * KB);
      v4u y0 = LDS16(xp), y1 = LDS16(xp + KB);
      SB();
#pragma unroll
      for (int k = 0; k < 7; ++k) {
        acc[6] = mfma32(W[84 + k], xs[k], acc[6]);
        acc[7] = mfma32(W[91 + k], xs[k], acc[7]);
      }
      SB();
      ldr();
      SB();
#pragma unroll
      for (int ks = 0; ks < 14; ++ks) {
        const v4u x = y0;
        y0 = y1;
        if (ks + 2 < 14) y1 = LDS16(xp + (ks + 2) * KB);
        if (ks == 2 && !own4) { LDSF4(lx + ((w >> 1) * 2 + 0) * KB + lane * 16) = acc[6]; LDSF4(lx + ((w >> 1) * 2 + 1) * KB + lane * 16) = acc[7]; }
#pragma unroll
        for (int pp = 0; pp < 3; ++pp) {
          acc[pp * 2 + 0] = mfma32(W[pp * 28 + ks], x, acc[pp * 2 + 0]);
          acc[pp * 2 + 1] = mfma32(W[pp * 28 + 14 + ks], x, acc[pp * 2 + 1]);
        }
        SB();
      }
    }
    if (l == 1) POLL(anysent(mx4(mx4(mx4(mx4(0u, xr[0]), xr[1]), xr[2]), xr[3])), ldr(), 1);
    BAR();
    STAMP(s, 2);
    v4u xb[4];
    if (own4) {
      acc[6] += LDSF4(lx + ((w >> 1) * 2 + 0) * KB + lane * 16);
      acc[7] += LDSF4(lx + ((w >> 1) * 2 + 1) * KB + lane * 16);
    }
    float sum = 0.f, sq = 0.f;
#pragma unroll
    for (int p = 0; p < 4; ++p) {
      xb[p] = resid8(xr[p], acc[2 * p], acc[2 * p + 1]);
      if (p == 3 && !own4) xb[p] = v4u{0u, 0u, 0u, 0u};
#pragma unroll
      for (int e = 0; e < 4; ++e) { sum = dot2(xb[p][e], 0x3F803F80u, sum); sq = dot2(xb[p][e], xb[p][e], sq); }
    }
    sum = rsum4(sum);
    sq = rsum4(sq);
    if (q == 0) *(f2*)(lst + (w * 16 + i16) * 8) = f2{sum, sq};
    BAR();
    const f2 st = (*(const f2*)(lst + i16 * 8) + *(const f2*)(lst + (16 + i16) * 8)) + (*(const f2*)(lst + (32 + i16) * 8) + *(const f2*)(lst + (48 + i16) * 8));
    const float mean = st[0] * (1.0f / 448.0f);
    const float rstd = rsq(fmaxf(st[1] * (1.0f / 448.0f) - mean * mean, 0.f) + 1e-5f);
    const float mr = mean * rstd;
#pragma unroll
    for (int p = 0; p < 4; ++p)
      if (p < 3 || own4) {
        const int u = (p < 3) ? 3 * w + p : ux;
        const unsigned fo = (unsigned)(tau * 14 + u) * KB + lane * 16;
        v4u o;
#pragma unroll
        for (int e = 0; e < 4; ++e) o[e] = pk2(bflo(xb[p][e]) * rstd - mr, bfhi(xb[p][e]) * rstd - mr);
        st16c(c.rs, rb + R_XN + fo, ns4(o));
      }
#pragma unroll
    for (int p = 0; p < 4; ++p)
      if (p < 3 || own4) {
        const int u = (p < 3) ? 3 * w + p : ux;
        const unsigned fo = (unsigned)(tau * 14 + u) * KB + lane * 16;
        st16c(c.rs, rb + R_XR + fo, xb[p]);
        st16c(c.rs, rec_in(l, s + AD) + R_XN + fo, S4);
      }
    STAMP(s, 3);
  }
  DRAIN();
  st4c(c.rs, fo, (unsigned)NSEG);
}

DEVI unsigned ring_offset(const unsigned char* ws) {
  const unsigned lo = (unsigned)((size_t)ws & (2u * MB - 1u));
  return ((lo + FLAG_BYTES + 2u * MB - 1u) & ~(2u * MB - 1u)) - lo;
}

__global__ void __launch_bounds__(256, 1) mega_kernel(unsigned char* ws, const unsigned char* mem0, __hip_bfloat16* out) {
  extern __shared__ __attribute__((aligned(16))) unsigned char lds[];
  Ctx c;
  c.ws = ws;
  c.rs = __builtin_amdgcn_make_buffer_rsrc(ws, 0, WS_END, 0x00020000);
  c.ring = ring_offset(ws);
  c.tid = threadIdx.x;
  c.lane = threadIdx.x & 63;
  c.wave = __builtin_amdgcn_readfirstlane(threadIdx.x >> 6);
  c.dead = false;
  const int b = blockIdx.x, x = b & 7, r = b >> 3;
  PMASK(1);
  STAMP(127, 0);
  {
    const unsigned char* pc = (const unsigned char*)(__builtin_amdgcn_s_getpc() & ~(unsigned long long)255);
    const unsigned o = (unsigned)(r * 256 + threadIdx.x) * 16;
    if (o < CPF * KB) { const v4u v = *(const v4u*)(pc + o); TOUCH(v); }
  }
  if (r >= 28 && (x == 1 || x == 2 || x == 5 || x == 7)) {
    touch_pages(c, r - 28);
  } else if (x == 0) {
    role_H(c, r >> 1, r & 1, lds);
  } else if (x < 3) {
    role_CV(c, r, x - 1, lds, mem0);
  } else if (x < 5) {
    if (r & 1) role_GX<1>(c, r >> 1, x - 3, lds); else role_GX<0>(c, r >> 1, x - 3, lds);
  } else if (x == 5) {
    if (r < 14) role_O<0>(c, r / 7, r % 7, lds, out); else role_O<1>(c, (r - 14) / 7, (r - 14) % 7, lds, out);
  } else if (x == 6) {
    if (r >> 4) role_AH<1>(c, r & 15, lds); else role_AH<0>(c, r & 15, lds);
  } else {
    if (r < 4) role_AO<0>(c, r & 1, r >> 1, lds); else if (r < 8) role_AO<1>(c, r & 1, (r >> 1) & 1, lds); else if (r < 16) probe_side(c, r - 8);
  }
}

#define PAUX 16
#define PST(off, v) __builtin_amdgcn_raw_buffer_store_b128(v, rs, off, 0, PAUX)
__global__ void __launch_bounds__(512) prologue_kernel(unsigned char* ws, const unsigned short* x0, const unsigned short* wqkv, const unsigned short* wo,
                                                       const unsigned short* mem0, const unsigned short* Wq, const unsigned short* Wk,
                                                       const unsigned short* Wv, const unsigned short* Wo) {
  extern __shared__ __attribute__((aligned(16))) unsigned char lb[];
  const int b = blockIdx.x, tid = threadIdx.x, lane = tid & 63, w = __builtin_amdgcn_readfirstlane(tid >> 6);
  const int i16 = lane & 15, q = lane >> 4;
  const rsrc_t rs = __builtin_amdgcn_make_buffer_rsrc(ws, 0, WS_END, 0x00020000);
  const bool prod = b < 224;
  const int kk = (b >> 3) / 7, cg = (b >> 3) % 7;
  const bool isG = kk < 2;
  const int h = (2 * (b & 7) + kk) & 15;
  auto stage_b = [&](const v4u* bv) {
    if (isG) {
#pragma unroll
      for (int k = 0; k < 4; ++k) LDS16(lb + (16 * k + (tid >> 5)) * 528 + (tid & 31) * 16) = bv[k];
    } else {
      unsigned char* p = lb + 8 * (tid & 7) * 528 + (tid >> 3) * 8;
#pragma unroll
      for (int k = 0; k < 4; ++k) {
        *(v2u*)(p + (2 * k) * 528) = v2u{__builtin_amdgcn_perm(bv[1][k], bv[0][k], 0x05040100u), __builtin_amdgcn_perm(bv[3][k], bv[2][k], 0x05040100u)};
        *(v2u*)(p + (2 * k + 1) * 528) = v2u{__builtin_amdgcn_perm(bv[1][k], bv[0][k], 0x07060302u), __builtin_amdgcn_perm(bv[3][k], bv[2][k], 0x07060302u)};
      }
    }
    BAR();
  };
  v4u bv[4];
  if (prod) {
    const unsigned short* bp = isG ? Wq + ((size_t)(64 * cg + (tid >> 5)) * 16 + h) * 256 + (tid & 31) * 8
                                   : Wo + ((size_t)h * 256 + 4 * (tid >> 3)) * 448 + 64 * cg + 8 * (tid & 7);
    const unsigned bs = isG ? 16 * 4096 : 448;
#pragma unroll
    for (int k = 0; k < 4; ++k) bv[k] = *(const v4u*)(bp + (size_t)k * bs);
  }
  if (prod && w < 7) {
    const unsigned short* Wa = isG ? Wk : Wv;
    v4u A[4][8];
#pragma unroll
    for (int mt = 0; mt < 4; ++mt)
#pragma unroll
      for (int k = 0; k < 8; ++k) {
        const int n = 2 * k + (lane >> 5), d = 64 * w + 32 * (mt >> 1) + 8 * (n >> 2) + 4 * (mt & 1) + (n & 3);
        A[mt][k] = *(const v4u*)(Wa + ((size_t)d * 16 + h) * 256 + (lane & 31) * 8);
      }
    stage_b(bv);
    unsigned char* la = lb + (64 + 16 * w) * 528;
    const float sc = isG ? 0.0625f : 1.0f;
#pragma unroll
    for (int mt = 0; mt < 4; ++mt) {
#pragma unroll
      for (int k = 0; k < 8; ++k) LDS16(la + (2 * k + (lane >> 5)) * 528 + (lane & 31) * 16) = A[mt][k];
      CBAR();
#pragma unroll
      for (int es = 0; es < 8; ++es) A[mt][es] = LDS16(la + i16 * 528 + es * 64 + q * 16);
      CBAR();
    }
#pragma unroll
    for (int nt = 0; nt < 4; ++nt) {
      const int row = isG ? 32 * (nt >> 1) + 8 * (i16 >> 2) + 4 * (nt & 1) + (i16 & 3) : 16 * nt + i16;
      f4 a[4] = {Z4, Z4, Z4, Z4};
#pragma unroll
      for (int es = 0; es < 8; ++es) {
        const v4u bf = LDS16(lb + row * 528 + es * 64 + q * 16);
#pragma unroll
        for (int mt = 0; mt < 4; ++mt) a[mt] = mfma32(A[mt][es], bf, a[mt]);
      }
      const unsigned off = isG ? WG_OFF + (unsigned)(((h * 14 + 2 * cg + (nt >> 1)) * 2 + (nt & 1)) * 14 + 2 * w) * KB
                               : WU_OFF + (unsigned)(((4 * cg + nt) * 16 + h) * 14 + 2 * w) * KB;
      PST(off + lane * 16, pk8(a[0] * sc, a[1] * sc));
      PST(off + KB + lane * 16, pk8(a[2] * sc, a[3] * sc));
    }
  } else {
    constexpr int NW = 224 + 32 * 8;
    const int wid = prod ? b : 224 + (b - 224) * 8 + w;
    unsigned short gv[4][8];
    v4u xv[4];
#pragma unroll
    for (int it = 0; it < 4; ++it) {
      const int f = min(wid + it * NW, 1735), ks = f % 14;
      const unsigned short* src;
      unsigned stride;
      bool ok = true;
      unsigned short pad = 0;
      if (f < 1344) {
        const int tl = (f / 14) % 6, a = f / 84;
        const int dh = (tl < 4) ? 8 * (i16 >> 2) + 4 * (tl & 1) + (i16 & 3) : 16 * (tl - 4) + i16;
        ok = dh < 28;
        pad = (tl < 4 && dh == 29) ? 0x3F80 : 0;
        src = wqkv + ((tl < 4) ? (tl >> 1) * 448 : 896) + 28 * a + min(dh, 27);
        stride = 1344;
      } else {
        const int g = f - 1344;
        src = wo + 32 * (g / 28) + 8 * (i16 >> 2) + 4 * ((g / 14) & 1) + (i16 & 3);
        stride = 448;
      }
      src += (size_t)(32 * ks + 8 * q) * stride;
#pragma unroll
      for (int jj = 0; jj < 8; ++jj) gv[it][jj] = src[(size_t)jj * stride];
      if (!ok) {
#pragma unroll
        for (int jj = 0; jj < 8; ++jj) gv[it][jj] = pad;
      }
    }
#pragma unroll
    for (int it = 0; it < 4; ++it) {
      const int f = min(wid + it * NW, 1791), sg = f / 28, tau = (f / 14) & 1, ks = f % 14;
      xv[it] = *(const v4u*)(x0 + (size_t)(sg * 32 + tok_of(tau, i16)) * 448 + 32 * ks + 8 * q);
    }
    SB();
    if (prod) stage_b(bv);
    const unsigned gtid = wid * 64 + lane, gstride = NW * 64;
    for (unsigned idx = gtid; idx < FLAG_BYTES / 16; idx += gstride) PST(idx * 16, (v4u{0u, 0u, 0u, 0u}));
    for (unsigned idx = gtid; idx < 2 * AD * REC / 16; idx += gstride) {
      const unsigned l = idx / (AD * REC / 16), r = idx % (AD * REC / 16);
      PST(REC_OFF + l * NSEG * REC + r * 16, S4);
    }
#pragma unroll
    for (int it = 0; it < 4; ++it) {
      const int f = wid + it * NW;
      if (f < 1736) {
        v4u o;
#pragma unroll
        for (int jj = 0; jj < 4; ++jj) o[jj] = (unsigned)gv[it][2 * jj] | ((unsigned)gv[it][2 * jj + 1] << 16);
        PST(WQ_OFF + (unsigned)f * KB + lane * 16, o);
      }
      if (f < 1792) PST(X0_OFF + (unsigned)f * KB + lane * 16, xv[it]);
    }
    const unsigned ring = ring_offset(ws);
    for (unsigned idx = gstride - 1 - gtid; idx < RING_BYTES / 16; idx += gstride) {
      const unsigned po = idx * 16, lo = ((po >> 14) << 13) | (po & 8191u);
      v4u o = S4;
      if (lo < L_MEMG && (lo & (64 * KB - 1)) < 14 * KB) {
        const unsigned r = lo & (64 * KB - 1), ln = (r >> 4) & 63, ks = r >> 10;
        o = ns4(*(const v4u*)(mem0 + (size_t)(ln & 7) * 448 + 32 * ks + 8 * (ln >> 4)));
      } else if (lo >= L_MEMG && lo < L_MEMG + 16 * KB && (lo & 8191u) < 7 * KB) {
        const unsigned r = lo & 8191u, ks = r >> 9, qq = (r >> 7) & 3, row = (r >> 4) & 7;
        o = ns4(*(const v4u*)(mem0 + (size_t)row * 448 + 32 * ks + 8 * qq));
      }
      PST(ring + po, o);
    }
  }
}

extern "C" void kernel_launch(void* const* d_in, const int* in_sizes, int n_in, void* d_out, int out_size, void* d_ws, size_t ws_size,
                              hipStream_t stream) {
  constexpr int LDS_BYTES = 100 * 1024;
  (void)hipFuncSetAttribute((const void*)mega_kernel, hipFuncAttributeMaxDynamicSharedMemorySize, LDS_BYTES);
  unsigned char* ws = (unsigned char*)d_ws;
  constexpr int PRO_LDS = (64 + 7 * 16) * 528;
  (void)hipFuncSetAttribute((const void*)prologue_kernel, hipFuncAttributeMaxDynamicSharedMemorySize, PRO_LDS);
  prologue_kernel<<<256, 512, PRO_LDS, stream>>>(ws, (const unsigned short*)d_in[0], (const unsigned short*)d_in[1], (const unsigned short*)d_in[2], (const unsigned short*)d_in[4],
                                          (const unsigned short*)d_in[5], (const unsigned short*)d_in[6], (const unsigned short*)d_in[7],
                                          (const unsigned short*)d_in[8]);
  mega_kernel<<<256, 256, LDS_BYTES, stream>>>(ws, (const unsigned char*)d_in[4], (__hip_bfloat16*)d_out);
}
```

```cpp
#include <hip/hip_runtime.h>
#include <hip/hip_bf16.h>

typedef unsigned v4u __attribute__((ext_vector_type(4)));
typedef unsigned v2u __attribute__((ext_vector_type(2)));
typedef float f4 __attribute__((ext_vector_type(4)));
typedef float f2 __attribute__((ext_vector_type(2)));
typedef short s8v __attribute__((ext_vector_type(8)));
typedef __bf16 b2v __attribute__((ext_vector_type(2)));
typedef __amdgpu_buffer_rsrc_t rsrc_t;
#define DEVI __device__ __forceinline__
#define RLX_AGENT __ATOMIC_RELAXED, __HIP_MEMORY_SCOPE_AGENT

#define LAG 6
#define GXS 1
#define PD 7
#define PP 0
#define CPF 71
#define D1 4
#define D2 2
#define NAP 110
#define DX 8
#define PX 6
#define PO 4
#define PZ 1
#define E2 2
#define USTREAM 1
constexpr int NSEG = 64, NT = NSEG + LAG, AD = 4, NPH = 2;
constexpr unsigned SENT = 0xFFFFFFFFu;
constexpr unsigned SPINCAP = 1u << 20;
constexpr float LOG2E = 1.4426950408889634f;
constexpr float NEG = -3.0e38f;

constexpr unsigned KB = 1024u, MB = 1024u * 1024u;
constexpr unsigned FLAG_BYTES = 8 * KB;
constexpr unsigned F_U = 1024, F_XR = 7168;
constexpr unsigned OOB = 0x80000000u;
constexpr unsigned F_ABORT = 0, F_SIDE = 128;
constexpr unsigned RING_BYTES = 1 * MB;
constexpr unsigned L_MEMH = 0, L_MEMC = 64 * KB, L_MEMG = 128 * KB, L_S = 192 * KB, L_E = 256 * KB, L_XS = 384 * KB, L_PROBE = 504 * KB;
constexpr unsigned WG_OFF = 4 * MB;
constexpr unsigned WG_BYTES = 16 * 14 * 2 * 14 * KB;
constexpr unsigned WU_OFF = WG_OFF + WG_BYTES;
constexpr unsigned WQ_OFF = WU_OFF + WG_BYTES;
constexpr unsigned WO_OFF = WQ_OFF + 16 * 6 * 14 * KB;
constexpr unsigned SEGX = 28 * KB;
constexpr unsigned R_XN = 0, R_XR = SEGX, R_OA = 2 * SEGX, R_X2 = 3 * SEGX, R_ETT = 4 * SEGX, R_ETS = R_ETT + 32 * KB, R_STM = R_ETS + 4 * KB,
                   R_ZB = R_STM + 32 * KB, REC = R_ZB + 112 * KB;
constexpr unsigned X0_OFF = WO_OFF + 392 * KB;
constexpr unsigned REC_OFF = X0_OFF + NSEG * SEGX;
constexpr unsigned SEGG = 448 * KB;
constexpr unsigned U_OFF = REC_OFF + 2 * NSEG * REC;
constexpr unsigned WS_END = U_OFF + 2 * NSEG * SEGG;
static_assert(WS_END < 200u * MB, "workspace");

DEVI f4 mfma32(v4u a, v4u b, f4 c) {
  return __builtin_amdgcn_mfma_f32_16x16x32_bf16(__builtin_bit_cast(s8v, a), __builtin_bit_cast(s8v, b), c, 0, 0, 0);
}
DEVI unsigned pk2(float a, float b) {
  f2 v = {a, b};
  return __builtin_bit_cast(unsigned, __builtin_convertvector(v, b2v));
}
DEVI float bflo(unsigned u) { return __uint_as_float(u << 16); }
DEVI float bfhi(unsigned u) { return __uint_as_float(u & 0xFFFF0000u); }
DEVI float rbf(float a) { return bflo(pk2(a, 0.f)); }
DEVI v4u pk8(f4 a, f4 b) { return v4u{pk2(a[0], a[1]), pk2(a[2], a[3]), pk2(b[0], b[1]), pk2(b[2], b[3])}; }
DEVI v2u pk4(f4 a) { return v2u{pk2(a[0], a[1]), pk2(a[2], a[3])}; }
DEVI v4u resid8(v4u x, f4 a, f4 b) {
  const v4u y = pk8(a, b);
  return v4u{pk2(bflo(x.x) + bflo(y.x), bfhi(x.x) + bfhi(y.x)), pk2(bflo(x.y) + bflo(y.y), bfhi(x.y) + bfhi(y.y)),
             pk2(bflo(x.z) + bflo(y.z), bfhi(x.z) + bfhi(y.z)), pk2(bflo(x.w) + bflo(y.w), bfhi(x.w) + bfhi(y.w))};
}
DEVI unsigned ns(unsigned x) { return min(x, 0xFFFFFFFEu); }
DEVI v4u ns4(v4u x) { return v4u{ns(x.x), ns(x.y), ns(x.z), ns(x.w)}; }
DEVI v2u ns2(v2u x) { return v2u{ns(x.x), ns(x.y)}; }
DEVI v4u f2u(f4 x) { return v4u{__float_as_uint(x[0]), __float_as_uint(x[1]), __float_as_uint(x[2]), __float_as_uint(x[3])}; }
DEVI f4 u2f(v4u x) { return f4{__uint_as_float(x.x), __uint_as_float(x.y), __uint_as_float(x.z), __uint_as_float(x.w)}; }
DEVI unsigned mx4(unsigned m, v4u v) { return max(max(max(max(m, v.x), v.y), v.z), v.w); }
DEVI unsigned mx2(unsigned m, v2u v) { return max(max(m, v.x), v.y); }
DEVI bool anysent(unsigned m) { return __builtin_amdgcn_ballot_w64(m == SENT) != 0ull; }
DEVI float ex2(float x) { return __builtin_amdgcn_exp2f(x); }
DEVI float rcp(float x) { return __builtin_amdgcn_rcpf(x); }
DEVI float rsq(float x) { return __builtin_amdgcn_rsqf(x); }
DEVI float mx3(float a, float b, float c) { return __builtin_fmaxf(__builtin_fmaxf(a, b), c); }
DEVI float mxf4(f4 a, float m) { return mx3(a[2], a[3], mx3(a[0], a[1], m)); }
DEVI float dot2(unsigned a, unsigned b, float c) {
  return __builtin_amdgcn_fdot2_f32_bf16(__builtin_bit_cast(b2v, a), __builtin_bit_cast(b2v, b), c, false);
}
DEVI void swap16(unsigned& a, unsigned& b) { auto r = __builtin_amdgcn_permlane16_swap(a, b, false, false); a = r[0]; b = r[1]; }
DEVI void swap32(unsigned& a, unsigned& b) { auto r = __builtin_amdgcn_permlane32_swap(a, b, false, false); a = r[0]; b = r[1]; }
DEVI float rmax4(float x) {
  unsigned a = __float_as_uint(x), b = a;
  swap16(a, b);
  a = b = __float_as_uint(mx3(__uint_as_float(a), __uint_as_float(b), NEG));
  swap32(a, b);
  return mx3(__uint_as_float(a), __uint_as_float(b), NEG);
}
DEVI float rsum4(float x) {
  unsigned a = __float_as_uint(x), b = a;
  swap16(a, b);
  a = b = __float_as_uint(__uint_as_float(a) + __uint_as_float(b));
  swap32(a, b);
  return __uint_as_float(a) + __uint_as_float(b);
}
DEVI float ror8(float x) { return __uint_as_float(__builtin_amdgcn_update_dpp(0u, __float_as_uint(x), 0x128, 0xF, 0xF, false)); }
DEVI void rbcast4(float x, float* o) {
  unsigned a = __float_as_uint(x), b = a;
  swap16(a, b);
  unsigned a2 = a, b2 = b;
  swap32(a, a2);
  swap32(b, b2);
  o[0] = __uint_as_float(a); o[1] = __uint_as_float(b); o[2] = __uint_as_float(a2); o[3] = __uint_as_float(b2);
}

DEVI v4u ld16(rsrc_t r, unsigned off) { return __builtin_amdgcn_raw_buffer_load_b128(r, off, 0, 0); }
DEVI v4u ld16c(rsrc_t r, unsigned off) { return __builtin_amdgcn_raw_buffer_load_b128(r, off, 0, 16); }
DEVI v2u ld8c(rsrc_t r, unsigned off) { return __builtin_amdgcn_raw_buffer_load_b64(r, off, 0, 16); }
DEVI unsigned ld4c(rsrc_t r, unsigned off) { return __builtin_amdgcn_raw_buffer_load_b32(r, off, 0, 16); }
DEVI void st16c(rsrc_t r, unsigned off, v4u v) { __builtin_amdgcn_raw_buffer_store_b128(v, r, off, 0, 16); }
DEVI void st8c(rsrc_t r, unsigned off, v2u v) { __builtin_amdgcn_raw_buffer_store_b64(v, r, off, 0, 16); }
DEVI void st4c(rsrc_t r, unsigned off, unsigned v) { __builtin_amdgcn_raw_buffer_store_b32(v, r, off, 0, 16); }
#define PIN_A(x) asm volatile("" : "+a"(x))
#define TOUCH(x) asm volatile("" :: "v"(x))
#define PIN_V(x) asm volatile("" : "+v"(x))
#define DRAIN() asm volatile("s_waitcnt vmcnt(0)" ::: "memory")
#define CBAR() asm volatile("" ::: "memory")
#define SB() __builtin_amdgcn_sched_barrier(0)
#define BAR() do { asm volatile("s_waitcnt lgkmcnt(0)" ::: "memory"); __builtin_amdgcn_s_barrier(); asm volatile("" ::: "memory"); } while (0)
#define LDS16(p) (*(v4u*)(p))
#define LDSF4(p) (*(f4*)(p))
#define S4 (v4u{SENT, SENT, SENT, SENT})
#define S2 (v2u{SENT, SENT})
#define Z4 (f4{0.f, 0.f, 0.f, 0.f})

#define STAMP(it, e) do { } while (0)
#define PMASK(m) do { } while (0)
#define STAMPC(it, e) do { } while (0)
struct Ctx {
  unsigned char* ws;
  rsrc_t rs;
  unsigned ring;
  int tid, lane, wave;
  bool dead;
};
DEVI bool give_up(Ctx& c, unsigned& spins) {
  if (c.dead) return true;
  ++spins;
  bool d = false;
  if (spins > SPINCAP) { __hip_atomic_store((unsigned*)(c.ws + F_ABORT), 1u, RLX_AGENT); d = true; }
  else if ((spins & 255u) == 0u) d = __builtin_amdgcn_readfirstlane(__hip_atomic_load((unsigned*)(c.ws + F_ABORT), RLX_AGENT)) != 0u;
  c.dead = d;
  return d;
}
#define POLL2(FIRST, BADEXPR, RELOAD, SLEEP) do { if (FIRST) { unsigned spins_ = 0; do { if (give_up(c, spins_)) break; if (SLEEP) __builtin_amdgcn_s_sleep(SLEEP); CBAR(); RELOAD; } while (BADEXPR); } } while (0)
#define POLL(BADEXPR, RELOAD, SLEEP) do { unsigned spins_ = 0; while (BADEXPR) { if (give_up(c, spins_)) break; if (SLEEP) __builtin_amdgcn_s_sleep(SLEEP); CBAR(); RELOAD; } } while (0)

template <class LD>
DEVI void poll2(Ctx& c, v4u* r, LD ld) {
  v4u a[4] = {r[0], r[1], r[2], r[3]}, b[4];
  unsigned spins = 0;
  __builtin_amdgcn_s_sleep(PD);
  while (true) {
    CBAR();
    ld(b);
    SB();
    if (!anysent(mx4(mx4(mx4(mx4(0u, a[0]), a[1]), a[2]), a[3])) || give_up(c, spins)) {
#pragma unroll
      for (int k = 0; k < 4; ++k) r[k] = a[k];
      break;
    }
    CBAR();
    ld(a);
    SB();
    if (!anysent(mx4(mx4(mx4(mx4(0u, b[0]), b[1]), b[2]), b[3]))) {
#pragma unroll
      for (int k = 0; k < 4; ++k) r[k] = b[k];
      break;
    }
  }
}

DEVI unsigned rmap(const Ctx& c, unsigned lo, unsigned side) { return c.ring + ((lo >> 13) << 14) + (side << 13) + (lo & 8191u); }
DEVI unsigned rec_off(int l, int s) { return REC_OFF + (unsigned)(l * NSEG + s) * REC; }
DEVI unsigned rec_in(int l, int s) { return (s >= 0 && s < NSEG) ? rec_off(l, s) : OOB; }
DEVI unsigned u_in(int l, int s) { return (s >= 0 && s < NSEG) ? U_OFF + (unsigned)(l * NSEG + s) * SEGG : OOB; }
DEVI int tok_of(int tau, int m) { return 8 * (m >> 2) + 4 * tau + (m & 3); }

DEVI void probe_side(Ctx& c, int k) {
  if (c.wave != 0) return;
  unsigned best[2] = {0xFFFFFFFFu, 0xFFFFFFFFu};
#pragma unroll 1
  for (int it = 0; it < 4; ++it) {
#pragma unroll
    for (int sd = 0; sd < 2; ++sd) {
      DRAIN();
      const unsigned t0 = (unsigned)__builtin_amdgcn_s_memtime();
      if (c.lane < 8) st16c(c.rs, rmap(c, L_PROBE + k * 256 + c.lane * 16, sd), S4);
      DRAIN();
      const unsigned t1 = (unsigned)__builtin_amdgcn_s_memtime();
      best[sd] = min(best[sd], t1 - t0);
    }
  }
  if (c.lane == 0) st4c(c.rs, F_SIDE + k * 64, best[1] < best[0] ? 2u : 1u);
}
DEVI unsigned get_sides(Ctx& c) {
  const unsigned o = F_SIDE + (c.lane & 7) * 64;
  unsigned v = ld4c(c.rs, o);
  POLL(__builtin_amdgcn_ballot_w64(v == 0u) != 0ull, v = ld4c(c.rs, o), 2);
  return (__builtin_popcountll(__builtin_amdgcn_ballot_w64(v == 2u)) > 32) ? 0u : 1u;
}

DEVI void role_H(Ctx& c, int h, int a, unsigned char* lds) {
  PMASK(1);
  const int lane = c.lane, w = c.wave;
  const int i16 = lane & 15, q = lane >> 4;
  unsigned char* lmem = lds;
  unsigned char* lred = lds + 8 * KB;
  unsigned char* ltb = lds + 24 * KB;
  const int odd = w & 1;
  const int ks0 = (w >> 1) * 7 + odd * 4, ksx = (w >> 1) * 7 + 3;
  v4u W[42], Wx[8];
#pragma unroll
  for (int lp = 0; lp < 7; ++lp) {
    const int gp = odd ? 6 - lp : lp;
    const unsigned b0 = WG_OFF + (unsigned)(h * 14 + 7 * a + gp) * 28 * KB + lane * 16;
#pragma unroll
    for (int t = 0; t < 2; ++t)
#pragma unroll
      for (int k = 0; k < 3; ++k) W[(lp * 2 + t) * 3 + k] = ld16(c.rs, b0 + (t * 14 + ks0 + k) * KB);
    if (lp < 4) {
#pragma unroll
      for (int t = 0; t < 2; ++t) {
        Wx[lp * 2 + t] = ld16(c.rs, b0 + (t * 14 + ksx) * KB);
        if (lp == 3 && t != odd) Wx[lp * 2 + t] = v4u{0u, 0u, 0u, 0u};
      }
    }
  }
#pragma unroll
  for (int f = 0; f < 42; ++f) PIN_A(W[f]);
#pragma unroll
  for (int f = 0; f < 8; ++f) PIN_A(Wx[f]);
  const unsigned sides = get_sides(c);
  unsigned mo[4];
#pragma unroll
  for (int k = 0; k < 4; ++k) mo[k] = rmap(c, L_MEMH + (k < 3 ? ks0 + k : ksx) * KB + lane * 16, sides);
  const unsigned so0 = rmap(c, L_S + ((a * 2 + (q & 1)) * 16 + h) * 256 + i16 * 16, sides);
  const bool sval = (q >> 1) == (i16 >> 3);
  const bool stager = (w >> 1) == a;
  const int gp1 = (w < 3) ? w + 4 : 3;
  v4u X[8];
  auto load_x = [&](int t) {
#pragma unroll
    for (int l = 0; l < 2; ++l) {
      const unsigned xb = rec_in(l, t - l * LAG) + R_XN + (unsigned)(7 * a) * KB + lane * 16;
#pragma unroll
      for (int tau = 0; tau < 2; ++tau) {
        X[(l * 2 + tau) * 2 + 0] = ld16c(c.rs, xb + (tau * 14 + w) * KB);
        X[(l * 2 + tau) * 2 + 1] = ld16c(c.rs, xb + (tau * 14 + gp1) * KB);
      }
    }
  };
  CBAR();
  load_x(0);
  for (int t = 0; t < NT; ++t) {
    const unsigned slot = (unsigned)(t & 3) * 32 * KB;
    STAMP(t, 0);
    v4u m[4];
    CBAR();
#pragma unroll
    for (int k = 0; k < 4; ++k) m[k] = ld16c(c.rs, mo[k] + slot);
    POLL(anysent(mx4(mx4(mx4(mx4(0u, m[0]), m[1]), m[2]), m[3])),
         { _Pragma("unroll") for (int k = 0; k < 4; ++k) m[k] = ld16c(c.rs, mo[k] + slot); }, 0);
    STAMP(t, 1);
    if (stager) {
#pragma unroll
      for (int k = 0; k < 4; ++k) LDS16(lmem + ((k < 3 ? ks0 + k : ksx) - 7 * a) * KB + lane * 16) = m[k];
    }
    SB();
    v4u tb[7], mu[7];
    f4 acc[7][2];
    f4 sacc = Z4;
#pragma unroll
    for (int lp = 0; lp < 7; ++lp) {
#define HPK(e) do { if (lp >= 2) { SB(); tb[lp - 2][e] = pk2(acc[lp - 2][(e) >> 1][((e) & 1) * 2], acc[lp - 2][(e) >> 1][((e) & 1) * 2 + 1]); SB(); } } while (0)
      acc[lp][0] = mfma32(W[(lp * 2 + 0) * 3 + 0], m[0], Z4);
      HPK(0);
      acc[lp][1] = mfma32(W[(lp * 2 + 1) * 3 + 0], m[0], Z4);
      HPK(1);
      acc[lp][0] = mfma32(W[(lp * 2 + 0) * 3 + 1], m[1], acc[lp][0]);
      HPK(2);
      acc[lp][1] = mfma32(W[(lp * 2 + 1) * 3 + 1], m[1], acc[lp][1]);
      HPK(3);
      acc[lp][0] = mfma32(W[(lp * 2 + 0) * 3 + 2], m[2], acc[lp][0]);
      if (lp == 6) { SB(); tb[5][0] = pk2(acc[5][0][0], acc[5][0][1]); SB(); }
      acc[lp][1] = mfma32(W[(lp * 2 + 1) * 3 + 2], m[2], acc[lp][1]);
      if (lp == 6) { SB(); tb[5][1] = pk2(acc[5][0][2], acc[5][0][3]); SB(); }
      if (lp < 4) {
        acc[lp][0] = mfma32(Wx[lp * 2 + 0], m[3], acc[lp][0]);
        acc[lp][1] = mfma32(Wx[lp * 2 + 1], m[3], acc[lp][1]);
      }
      if (lp >= 2) sacc = mfma32(tb[lp - 2], mu[lp - 2], sacc);
      SB();
      if (lp == 0) {
        BAR();
#pragma unroll
        for (int k = 0; k < 7; ++k) mu[k] = LDS16(lmem + (odd ? 6 - k : k) * KB + lane * 16);
        SB();
      }
    }
    tb[5][2] = pk2(acc[5][1][0], acc[5][1][1]);
    tb[5][3] = pk2(acc[5][1][2], acc[5][1][3]);
    sacc = mfma32(tb[5], mu[5], sacc);
    SB();
    tb[6] = pk8(acc[6][0], acc[6][1]);
    sacc = mfma32(tb[6], mu[6], sacc);
    if (w != 0) LDSF4(lred + w * KB + lane * 16) = sacc;
    BAR();
    if (w == 0) {
      const f4 s = (sacc + LDSF4(lred + KB + lane * 16)) + (LDSF4(lred + 2 * KB + lane * 16) + LDSF4(lred + 3 * KB + lane * 16));
      if (sval) {
        st16c(c.rs, so0 + slot, ns4(f2u(s)));
        if (t >= 1) st16c(c.rs, so0 + (unsigned)((t - 1) & 3) * 32 * KB, S4);
      }
    }
    STAMP(t, 2);
#pragma unroll
    for (int lp = 0; lp < 7; ++lp) LDS16(ltb + (w * 7 + (odd ? 6 - lp : lp)) * KB + lane * 16) = tb[lp];
    BAR();
    v4u tp[8];
#pragma unroll
    for (int ww = 0; ww < 4; ++ww) {
      tp[ww * 2 + 0] = LDS16(ltb + (ww * 7 + w) * KB + lane * 16);
      tp[ww * 2 + 1] = LDS16(ltb + (ww * 7 + gp1) * KB + lane * 16);
      if (w == 3) tp[ww * 2 + 1] = v4u{0u, 0u, 0u, 0u};
    }
    POLL(anysent(mx4(mx4(mx4(mx4(mx4(mx4(mx4(mx4(0u, X[0]), X[1]), X[2]), X[3]), X[4]), X[5]), X[6]), X[7])), load_x(t), 1);
    SB();
    {
      f4 s[4] = {Z4, Z4, Z4, Z4};
#pragma unroll
      for (int k = 0; k < 8; ++k)
#pragma unroll
        for (int lt = 0; lt < 4; ++lt) s[lt] = mfma32(tp[k], X[lt * 2 + (k & 1)], s[lt]);
#pragma unroll
      for (int lt = 0; lt < 4; ++lt) LDSF4(lred + (lt * 4 + w) * KB + lane * 16) = s[lt];
    }
    BAR();
    {
      const int l = w >> 1, tau = w & 1, s = t - l * LAG;
      const f4 v = (LDSF4(lred + (w * 4 + 0) * KB + lane * 16) + LDSF4(lred + (w * 4 + 1) * KB + lane * 16)) +
                   (LDSF4(lred + (w * 4 + 2) * KB + lane * 16) + LDSF4(lred + (w * 4 + 3) * KB + lane * 16));
      if ((q >> 1) == l) {
        const unsigned o = R_STM + (unsigned)((((a * 2 + (q & 1)) * 2 + tau) * 16 + h) * 256 + i16 * 16);
        st16c(c.rs, rec_in(l, s) + o, ns4(f2u(v)));
        if (s >= 0) st16c(c.rs, rec_in(l, s + AD) + o, S4);
      }
    }
    CBAR();
    load_x(t + 1);
    STAMP(t, 3);
  }
}

DEVI void role_CV(Ctx& c, int j, int p, unsigned char* lds, const unsigned char* mem0) {
  PMASK(67);
  const int lane = c.lane, w = c.wave;
  const int i16 = lane & 15, q = lane >> 4;
  unsigned char* lmem = lds;
  unsigned char* lred = lds + 16 * KB;
  unsigned char* lXb = lds + 20 * KB;
  unsigned ko[6];
#pragma unroll
  for (int k = 0; k < 4; ++k) ko[k] = (unsigned)((w + k) & 3) * KB + lane * 16;
#pragma unroll
  for (int k = 0; k < 2; ++k) ko[4 + k] = (unsigned)(12 + ((w + k) & 1)) * KB + lane * 16;
#define KSO(i) (((i) < 12) ? ko[(i) / 3] + ((i) % 3) * 4 * KB : ko[(i) - 8])
  v4u W[56];
  {
    const unsigned b0 = WU_OFF + (unsigned)(j * 16 + 4 * w) * 14 * KB;
#pragma unroll
    for (int f = 0; f < 56; ++f) W[f] = ld16(c.rs, b0 + (f / 14) * 14 * KB + KSO(f % 14));
#pragma unroll
    for (int f = 0; f < 56; ++f) PIN_A(W[f]);
  }
  const v2u m0 = ns2(*(const v2u*)(mem0 + (size_t)(i16 & 7) * 896 + (j * 16 + 4 * q) * 2));
  const unsigned sides = get_sides(c);
  const unsigned sC = sides;
  unsigned mo[4], so[4], eo[4];
#pragma unroll
  for (int k = 0; k < 4; ++k) {
    mo[k] = rmap(c, L_MEMC + (k < 3 ? k * 4096 + c.tid * 16 : 12288 + (c.tid & 127) * 16), sC);
    so[k] = rmap(c, L_S + (k * 16 + 4 * w) * 256 + lane * 16, sC);
    eo[k] = rmap(c, L_E + (4 * w + k) * KB + lane * 16, sC);
  }
  const unsigned xo = rmap(c, L_XS + ((4 * w + q) * 2 + (i16 >> 3)) * 128 + (i16 & 7) * 8, sC);
  const unsigned pf = (unsigned)(j >> 1) * KB + (unsigned)((2 * (j & 1) + (q >> 1)) * 16 + i16) * 16 + (q & 1) * 8;
  const unsigned pg = (unsigned)(i16 >> 3) * 8 * KB + (unsigned)(j >> 1) * 512 + (unsigned)(2 * (j & 1) + (q >> 1)) * 128 + (i16 & 7) * 16 + (q & 1) * 8;
  const unsigned pH = rmap(c, L_MEMH + pf, sides), pC = rmap(c, L_MEMC + pf, sides), pG = rmap(c, L_MEMG + pg, sides ^ (unsigned)(p ^ 1));
  const unsigned pR = (w == 1) ? pH : (w == 2) ? pG : (w == 3) ? pC : OOB;
  const unsigned zo = R_ZB + (unsigned)(j * 4 + w) * KB + lane * 16;
  const unsigned uo = (unsigned)(j * 16 + 4 * w) * KB + lane * 16;
  const unsigned fo = (lane == 0) ? F_U + (unsigned)(p * 28 + j) * 64 + w * 4 : OOB;
#define XBAD anysent(mx4(mx4(mx4(mx4(mx4(mx4(mx4(0u, x[0]), x[1]), x[2]), x[3]), x[4]), x[5]), x[6]))
  v4u U[8], x[7];
  auto load_x = [&](int l, int s) {
    const unsigned xb = rec_in(l, s) + R_XN + c.tid * 16;
#pragma unroll
    for (int f = 0; f < 7; ++f) x[f] = ld16c(c.rs, xb + f * 4096);
  };
  auto stage_x = [&](int l) {
#pragma unroll
    for (int f = 0; f < 7; ++f) LDS16(lXb + l * 28 * KB + f * 4096 + c.tid * 16) = x[f];
  };
  unsigned* lhave = (unsigned*)(lds + 15 * KB);
  bool have = false, allhave = false;
  for (int t = p; t < NT; t += 2) {
    const unsigned slot = (unsigned)(t & 3) * 32 * KB, eslot = (unsigned)(t & 7) * 32 * KB, xslot = (unsigned)(t & 7) * 16 * KB;
    const int s0 = t, s1 = t - LAG;
    STAMP(t, 0);
    if (!allhave) {
      if (!have) {
        CBAR();
        load_x(0, s0);
        POLL(XBAD, load_x(0, s0), 1);
        stage_x(0);
      }
      BAR();
    }
    CBAR();
    load_x(1, s1);
    SB();
    f4 acc[8];
    const unsigned ub0 = u_in(0, s0) + uo, za0 = rec_in(0, s0 + AD) + zo, xb0 = rec_in(0, s0 + 2) + R_XN + c.tid * 16;
    auto side_op = [&](int k) {
      if (k < 4) st16c(c.rs, ub0 + k * KB, U[k]);
      else if (k == 4) st16c(c.rs, za0, S4);
      else if (k < 12) x[k - 5] = ld16c(c.rs, xb0 + (k - 5) * 4096);
    };
#pragma unroll
    for (int l = 0; l < 2; ++l) {
      const int s = l ? s1 : s0;
      const bool act = (s >= 0 && s < NSEG);
#pragma unroll
      for (int k = 0; k < 8; ++k) acc[k] = Z4;
      if (act) {
        const unsigned char* xp = lXb + l * 28 * KB;
        v4u xa = LDS16(xp + KSO(0)), xb = LDS16(xp + 14 * KB + KSO(0)), ya = LDS16(xp + KSO(1)), yb = LDS16(xp + 14 * KB + KSO(1));
        SB();
#pragma unroll
        for (int ks = 0; ks < 14; ++ks) {
          const v4u x0 = xa, x1 = xb;
          xa = ya; xb = yb;
          if (ks + 2 < 14) { ya = LDS16(xp + KSO(ks + 2)); yb = LDS16(xp + 14 * KB + KSO(ks + 2)); }
#pragma unroll
          for (int hh = 0; hh < 4; ++hh) {
            acc[hh * 2 + 0] = mfma32(x0, W[hh * 14 + ks], acc[hh * 2 + 0]);
            acc[hh * 2 + 1] = mfma32(x1, W[hh * 14 + ks], acc[hh * 2 + 1]);
            if (l == 1 && hh == 1) { SB(); side_op(ks); SB(); }
          }
          SB();
        }
      }
      else if (l == 1) {
#pragma unroll
        for (int k = 0; k < 12; ++k) side_op(k);
      }
      if (l == 0) {
        const bool bad = XBAD;
#pragma unroll
        for (int f = 0; f < 7; ++f) {
          LDS16(lXb + 28 * KB + f * 4096 + c.tid * 16) = x[f];
          if (f < 4) { U[f] = pk8(acc[f * 2], acc[f * 2 + 1]); PIN_V(U[f]); }
          SB();
        }
        if (bad) {
          POLL2(true, XBAD, load_x(1, s1), 1);
          stage_x(1);
        }
        CBAR();
        BAR();
        SB();
      }
    }
    STAMP(t, 1);
    v4u m[4];
    CBAR();
#pragma unroll
    for (int k = 0; k < 4; ++k) m[k] = ld16c(c.rs, mo[k] + slot);
    SB();
#pragma unroll
    for (int hh = 0; hh < 4; ++hh) { U[4 + hh] = pk8(acc[hh * 2], acc[hh * 2 + 1]); PIN_V(U[4 + hh]); }
    SB();
    have = !XBAD;
    if (lane == 0) lhave[w] = have ? 1u : 0u;
    SB();
    POLL(anysent(mx4(mx4(mx4(mx4(0u, m[0]), m[1]), m[2]), m[3])),
         { _Pragma("unroll") for (int k = 0; k < 4; ++k) m[k] = ld16c(c.rs, mo[k] + slot); }, 0);
#pragma unroll
    for (int k = 0; k < 3; ++k) LDS16(lmem + k * 4096 + c.tid * 16) = m[k];
    LDS16(lmem + 12288 + (c.tid & 127) * 16) = m[3];
    STAMP(t, 2);
    v4u zA[2];
    {
      f4 z[4] = {Z4, Z4, Z4, Z4};
      const unsigned ub = u_in(1, s1) + uo;
      DRAIN();
      st4c(c.rs, fo, (unsigned)min(s0 + 1, NSEG));
      SB();
#pragma unroll
      for (int hh = 0; hh < 4; ++hh) {
        z[hh] = mfma32(m[0], W[hh * 14], z[hh]);
        SB();
        st16c(c.rs, ub + hh * KB, U[4 + hh]);
        SB();
      }
      st16c(c.rs, (s1 >= 0 ? rec_in(1, s1 + AD) : OOB) + zo, S4);
      SB();
      BAR();
      v4u mf[14];
#pragma unroll
      for (int ks = 3; ks < 14; ++ks)
        if (ks != 12) mf[ks] = LDS16(lmem + KSO(ks));
      const v4u hv = LDS16((unsigned char*)lhave);
      mf[1] = m[1]; mf[2] = m[2]; mf[12] = m[3];
      SB();
#pragma unroll
      for (int st = 1; st < 14; ++st) {
        const int ks = (st < 3) ? st : (st == 3) ? 12 : (st < 13) ? st - 1 : 13;
#pragma unroll
        for (int hh = 0; hh < 4; ++hh) {
          z[hh] = mfma32(mf[ks], W[hh * 14 + ks], z[hh]);
          if (st == 1 && hh == 0) {
            SB();
            st8c(c.rs, (t >= 1 ? pR : OOB) + (unsigned)((t - 1) & 3) * 32 * KB, S2);
            SB();
          }
        }
        SB();
      }
      allhave = __builtin_amdgcn_readfirstlane(hv.x & hv.y & hv.z & hv.w) != 0u;
      unsigned zl[4], zh[4];
#pragma unroll
      for (int hh = 0; hh < 4; ++hh) { zl[hh] = pk2(z[hh][0], z[hh][1]); zh[hh] = pk2(z[hh][2], z[hh][3]); }
      swap16(zl[0], zl[1]); swap16(zl[2], zl[3]); swap32(zl[0], zl[2]); swap32(zl[1], zl[3]);
      swap16(zh[0], zh[1]); swap16(zh[2], zh[3]); swap32(zh[0], zh[2]); swap32(zh[1], zh[3]);
      zA[0] = v4u{zl[0], zh[0], zl[1], zh[1]};
      zA[1] = v4u{zl[2], zh[2], zl[3], zh[3]};
    }
    STAMP(t, 3);
    if (t == NT - 1) {
      CBAR();
      st16c(c.rs, rec_in(1, s1) + zo, ns4(zA[1]));
      asm volatile("s_waitcnt vmcnt(1)" ::: "memory");
      st4c(c.rs, fo + 16, (unsigned)NSEG);
      STAMP(t, 6);
      break;
    }
    v4u E[4], sv[4];
    v2u xs;
    auto load_e = [&]() {
#pragma unroll
      for (int hh = 0; hh < 4; ++hh) E[hh] = ld16c(c.rs, eo[hh] + eslot);
      xs = ld8c(c.rs, xo + xslot);
    };
    auto load_s = [&]() {
#pragma unroll
      for (int k = 0; k < 4; ++k) sv[k] = ld16c(c.rs, so[k] + slot);
    };
    CBAR();
    load_e();
    SB();
    stage_x(0);
    SB();
    if (E2) __builtin_amdgcn_s_sleep(E2);
    CBAR();
    load_s();
    POLL(anysent(mx2(mx4(mx4(mx4(mx4(0u, E[0]), E[1]), E[2]), E[3]), xs)), { load_e(); load_s(); }, 0);
    SB();
    f4 ox[4];
#pragma unroll
    for (int hh = 0; hh < 4; ++hh) {
      const f4 o0 = mfma32(U[hh], E[hh], Z4), o1 = mfma32(U[4 + hh], E[hh], Z4);
      ox[hh] = (i16 < 8) ? o0 : o1;
    }
#pragma unroll
    for (int hh = 0; hh < 4; ++hh) PIN_V(ox[hh]);
    STAMP(t, 4);
    SB();
    POLL(anysent(mx4(mx4(mx4(mx4(0u, sv[0]), sv[1]), sv[2]), sv[3])), load_s(), 0);
    STAMP(t, 5);
    DRAIN();
    st4c(c.rs, fo + 16, (unsigned)max(s1 + 1, 0));
    {
      f4 a = u2f(sv[0]) + u2f(sv[2]), b = u2f(sv[1]) + u2f(sv[3]);
      const float mxx = __uint_as_float(xs.x), lx = __uint_as_float(xs.y);
      const float mm = mxf4(b, mxf4(a, mxx));
      const float ml = mm * LOG2E;
#pragma unroll
      for (int r = 0; r < 4; ++r) { a[r] = ex2(a[r] * LOG2E - ml); b[r] = ex2(b[r] * LOG2E - ml); }
      const float ax = ex2(mxx * LOG2E - ml);
      const float den = ((a[0] + a[1]) + (a[2] + a[3])) + ((b[0] + b[1]) + (b[2] + b[3])) + ax * lx;
      const float inv = rcp(den);
      const v4u P = pk8(a * inv, b * inv);
      const f4 d0 = mfma32(zA[0], P, Z4), d1 = mfma32(zA[1], P, Z4);
      float wx[4];
      rbcast4(ax * inv, wx);
      f4 acc = (ox[0] * wx[0] + ox[1] * wx[1]) + (ox[2] * wx[2] + ox[3] * wx[3]);
      acc += (i16 < 8) ? d0 : d1;
      if (w != 0) LDSF4(lred + w * KB + lane * 16) = acc;
      BAR();
      if (w == 0) {
        acc = (acc + LDSF4(lred + KB + lane * 16)) + (LDSF4(lred + 2 * KB + lane * 16) + LDSF4(lred + 3 * KB + lane * 16));
        v2u o = ns2(pk4(acc));
        if (i16 >= 8 && t + 1 <= LAG) o = m0;
        const unsigned nslot = (unsigned)((t + 1) & 3) * 32 * KB;
        st8c(c.rs, pH + nslot, o);
        st8c(c.rs, pG + nslot, o);
        st8c(c.rs, pC + nslot, o);
      }
      STAMP(t, 6);
      st16c(c.rs, rec_in(0, s0) + zo, ns4(zA[0]));
      st16c(c.rs, rec_in(1, s1) + zo, ns4(zA[1]));
    }
  }
}

template <int L>
DEVI void role_GX(Ctx& c, int h, int p, unsigned char* lds) {
  PMASK(5);
  asm volatile("; layer %2" : "+v"(c.tid), "+v"(c.lane) : "n"(L) : "memory");
  const int lane = c.lane, w = c.wave;
  const int i16 = lane & 15, q = lane >> 4;
  unsigned char* lX = lds;
  unsigned char* lG = lds + 28 * KB;
  unsigned char* lred = lds + 60 * KB;
  v4u W[98];
  {
    const unsigned b0 = WG_OFF + (unsigned)(h * 14 + 3 * w) * 28 * KB + lane * 16;
#pragma unroll
    for (int f = 0; f < 84; ++f) W[f] = ld16(c.rs, b0 + f * KB);
    const unsigned b1 = WG_OFF + (unsigned)(h * 14 + 12 + (w >> 1)) * 28 * KB + (w & 1) * 7 * KB + lane * 16;
#pragma unroll
    for (int k = 0; k < 7; ++k) { W[84 + k] = ld16(c.rs, b1 + k * KB); W[91 + k] = ld16(c.rs, b1 + (14 + k) * KB); }
#pragma unroll
    for (int f = 0; f < 56; ++f) PIN_A(W[f]);
  }
  const unsigned sides = get_sides(c);
  const unsigned sC = sides;
  const int kx = 7 * (w & 1);
  unsigned mo[4];
#pragma unroll
  for (int k = 0; k < 4; ++k) {
    const int vu = 4 * w + k, ks = vu < 12 ? vu : 12 + ((vu - 12) >> 1);
    mo[k] = rmap(c, L_MEMG + L * 8 * KB + ks * 512 + q * 128 + (i16 & 7) * 16, sides ^ (unsigned)p);
  }
  const unsigned eo = rmap(c, L_E + h * KB + (q * 16 + L * 8 + (i16 & 7)) * 16 + (i16 >> 3) * 8, sC);
  const unsigned xo = rmap(c, L_XS + (h * 2 + L) * 128 + (i16 & 7) * 8, sC);
  v4u x[7];
  auto load_x = [&](int s) {
    const unsigned xb = rec_in(L, s) + R_XN + c.tid * 16;
#pragma unroll
    for (int f = 0; f < 7; ++f) x[f] = ld16c(c.rs, xb + f * 4096);
  };
  bool have = false;
  for (int t = p; t < NT; t += 2) {
    const int s = t - L * LAG;
    const bool act = (s >= 0 && s < NSEG);
    v4u g[8];
    STAMP(t, 0);
    if (act) {
      if (!have) {
        CBAR();
        load_x(s);
        POLL(anysent(mx4(mx4(mx4(mx4(mx4(mx4(mx4(0u, x[0]), x[1]), x[2]), x[3]), x[4]), x[5]), x[6])), load_x(s), 1);
      }
#pragma unroll
      for (int f = 0; f < 7; ++f) LDS16(lX + f * 4096 + c.tid * 16) = x[f];
      BAR();
      STAMP(t, 1);
#pragma unroll 1
      for (int tau = 0; tau < 2; ++tau) {
        const unsigned char* xp = lX + tau * 14 * KB + lane * 16;
        const unsigned xnext = (tau ? rec_in(L, s + 2) : OOB) + R_XN + c.tid * 16;
        f4 acc[8] = {Z4, Z4, Z4, Z4, Z4, Z4, Z4, Z4};
        v4u xs[7];
#pragma unroll
        for (int k = 0; k < 7; ++k) xs[k] = LDS16(xp + (kx + k) * KB);
        v4u x0 = LDS16(xp), x1 = LDS16(xp + KB);
        SB();
#pragma unroll
        for (int k = 0; k < 7; ++k) {
          acc[6] = mfma32(W[84 + k], xs[k], acc[6]);
          acc[7] = mfma32(W[91 + k], xs[k], acc[7]);
        }
        SB();
        unsigned char* gp = lG + tau * 16 * KB + lane * 16;
#pragma unroll
        for (int ks = 0; ks < 14; ++ks) {
          const v4u xk = x0;
          x0 = x1;
          if (ks + 2 < 14) x1 = LDS16(xp + (ks + 2) * KB);
          if (ks == 2) LDS16(gp + (12 + w) * KB) = pk8(acc[6], acc[7]);
#pragma unroll
          for (int pp = 0; pp < 3; ++pp) {
            acc[pp * 2 + 0] = mfma32(W[pp * 28 + ks], xk, acc[pp * 2 + 0]);
            acc[pp * 2 + 1] = mfma32(W[pp * 28 + 14 + ks], xk, acc[pp * 2 + 1]);
            if (L == 0 && pp == 0 && ks >= 3 && ks < 10) { SB(); x[ks - 3] = ld16c(c.rs, xnext + (ks - 3) * 4096); SB(); }
          }
          SB();
        }
#pragma unroll
        for (int pp = 0; pp < 3; ++pp) LDS16(gp + (3 * w + pp) * KB) = pk8(acc[pp * 2], acc[pp * 2 + 1]);
      }
      BAR();
#pragma unroll
      for (int k = 0; k < 4; ++k) { g[k] = LDS16(lG + (4 * w + k) * KB + lane * 16); g[4 + k] = LDS16(lG + (16 + 4 * w + k) * KB + lane * 16); }
    } else {
#pragma unroll
      for (int k = 0; k < 8; ++k) g[k] = v4u{0u, 0u, 0u, 0u};
    }
    STAMP(t, 2);
    {
      const unsigned slot = (unsigned)(t & 3) * 32 * KB;
      v4u m[4];
      CBAR();
#pragma unroll
      for (int k = 0; k < 4; ++k) m[k] = ld16c(c.rs, mo[k] + slot);
      POLL(anysent(mx4(mx4(mx4(mx4(0u, m[0]), m[1]), m[2]), m[3])),
           { _Pragma("unroll") for (int k = 0; k < 4; ++k) m[k] = ld16c(c.rs, mo[k] + slot); }, 0);
      STAMP(t, 3);
      f4 a0 = Z4, a1 = Z4;
#pragma unroll
      for (int k = 0; k < 4; ++k) { a0 = mfma32(g[k], m[k], a0); a1 = mfma32(g[4 + k], m[k], a1); }
      LDSF4(lred + 4 * KB + w * KB + lane * 16) = (i16 < 8) ? a0 : a1;
      BAR();
      if (w == 0) {
        const unsigned char* rp = lred + 4 * KB + lane * 16;
        f4 v = (LDSF4(rp) + LDSF4(rp + KB)) + (LDSF4(rp + 2 * KB) + LDSF4(rp + 3 * KB));
        float m1 = mxf4(v, NEG);
        m1 = mx3(m1, ror8(m1), NEG);
        m1 = rmax4(m1);
        const float ml = m1 * LOG2E;
#pragma unroll
        for (int r = 0; r < 4; ++r) v[r] = ex2(v[r] * LOG2E - ml);
        const unsigned eslot = (unsigned)(t & 7) * 32 * KB, xslot = (unsigned)(t & 7) * 16 * KB;
        st8c(c.rs, eo + eslot, act ? ns2(pk4(v)) : v2u{0u, 0u});
        float ls = (v[0] + v[1]) + (v[2] + v[3]);
        ls += ror8(ls);
        ls = rsum4(ls);
        if (lane < 8) st8c(c.rs, xo + xslot, act ? ns2(v2u{__float_as_uint(m1), __float_as_uint(ls)}) : v2u{__float_as_uint(-1.0e30f), 0u});
        if (t >= 2) {
          st8c(c.rs, eo + (unsigned)((t - 2) & 7) * 32 * KB, S2);
          if (lane < 8) st8c(c.rs, xo + (unsigned)((t - 2) & 7) * 16 * KB, S2);
        }
      }
      STAMP(t, 4);
    }
    if (act) {
      const int tq = w & 1, kh = w >> 1;
      f4 a0 = Z4, a1 = Z4;
#pragma unroll
      for (int hf = 0; hf < 3; ++hf) {
        v4u ga[3], gb[3], xq[3];
#pragma unroll
        for (int k = 0; k < 3; ++k)
          if (hf * 3 + k < 8) {
            const int vu = kh * 8 + hf * 3 + k, ks = vu < 12 ? vu : 12 + ((vu - 12) >> 1);
            ga[k] = LDS16(lG + vu * KB + lane * 16);
            gb[k] = LDS16(lG + (16 + vu) * KB + lane * 16);
            xq[k] = LDS16(lX + (tq * 14 + ks) * KB + lane * 16);
          }
        SB();
#pragma unroll
        for (int k = 0; k < 3; ++k)
          if (hf * 3 + k < 8) { a0 = mfma32(ga[k], xq[k], a0); a1 = mfma32(gb[k], xq[k], a1); }
        SB();
      }
      if (!kh) { LDSF4(lred + (tq * 2 + 0) * KB + lane * 16) = a0; LDSF4(lred + (tq * 2 + 1) * KB + lane * 16) = a1; }
      BAR();
      if (L == 0) have = !anysent(mx4(mx4(mx4(mx4(mx4(mx4(mx4(0u, x[0]), x[1]), x[2]), x[3]), x[4]), x[5]), x[6]));
      if (kh) {
        a0 += LDSF4(lred + (tq * 2 + 0) * KB + lane * 16);
        a1 += LDSF4(lred + (tq * 2 + 1) * KB + lane * 16);
        const int lim = tok_of(tq, i16) - 8 * q;
#pragma unroll
        for (int r = 0; r < 4; ++r) {
          a0[r] = (r <= lim) ? a0[r] : NEG;
          a1[r] = (4 + r <= lim) ? a1[r] : NEG;
        }
        const float m = rmax4(mxf4(a1, mxf4(a0, NEG)));
        const float ml = m * LOG2E;
        float lsum = 0.f;
#pragma unroll
        for (int r = 0; r < 4; ++r) {
          a0[r] = (r <= lim) ? ex2(a0[r] * LOG2E - ml) : 0.f;
          a1[r] = (4 + r <= lim) ? ex2(a1[r] * LOG2E - ml) : 0.f;
          lsum += a0[r] + a1[r];
        }
        lsum = rsum4(lsum);
        const unsigned o1 = R_ETT + (unsigned)(h * 2 + tq) * KB + lane * 16, o2 = R_ETS + (unsigned)((tq * 16 + h) * 16 + i16) * 8;
        st16c(c.rs, rec_off(L, s) + o1, ns4(pk8(a0, a1)));
        if (q == 0) st8c(c.rs, rec_off(L, s) + o2, ns2(v2u{__float_as_uint(m), __float_as_uint(lsum)}));
        st16c(c.rs, rec_in(L, s + AD) + o1, S4);
        if (q == 0) st8c(c.rs, rec_in(L, s + AD) + o2, S2);
      }
    }
    STAMP(t, 5);
  }
}

template <int L>
DEVI void role_O(Ctx& c, int tau, int cg, unsigned char* lds, __hip_bfloat16* out) {
  PMASK(8);
  const int lane = c.lane, w = c.wave;
  const int i16 = lane & 15, q = lane >> 4;
  const int ct = 4 * cg + w;
  const unsigned po = (unsigned)(tau * 14 + (ct >> 1)) * KB + (unsigned)((2 * (ct & 1) + (q >> 1)) * 16 + i16) * 16 + (q & 1) * 8;
  v4u U[16], e[4], sv[4], z[4];
  v2u es, xr;
  auto load_u = [&](int s) {
    const unsigned ub = u_in(L, s) + (unsigned)(4 * cg * 16 + 4 * w) * KB + lane * 16;
#pragma unroll
    for (int k = 0; k < 4; ++k)
#pragma unroll
      for (int hh = 0; hh < 4; ++hh) U[k * 4 + hh] = ld16c(c.rs, ub + (k * 16 + hh) * KB);
    xr = ld8c(c.rs, rec_in(L, s) + R_XR + po);
  };
  auto load_e = [&](int s) {
    const unsigned rb = rec_off(L, s);
#pragma unroll
    for (int hh = 0; hh < 4; ++hh) e[hh] = ld16c(c.rs, rb + R_ETT + (unsigned)((4 * w + hh) * 2 + tau) * KB + lane * 16);
    es = ld8c(c.rs, rb + R_ETS + (unsigned)((tau * 16 + 4 * w) * 16 + lane) * 8);
  };
  auto load_z = [&](int s) {
#pragma unroll
    for (int k = 0; k < 4; ++k) z[k] = ld16c(c.rs, rec_off(L, s) + R_ZB + (unsigned)((4 * cg + k) * 4 + w) * KB + lane * 16);
  };
  auto load_s = [&](int s) {
    const unsigned rb = rec_off(L, s);
#pragma unroll
    for (int k = 0; k < 4; ++k) sv[k] = ld16c(c.rs, rb + R_STM + (unsigned)((k * 2 + tau) * 16 + 4 * w) * 256 + lane * 16);
    load_z(s);
  };
  const int fph = (lane >> 2) & 3;
  const unsigned fo = (lane < 4 * NPH) ? F_U + (unsigned)(fph * 28 + 4 * cg + (lane & 3)) * 64 + L * 16 + w * 4
                      : (lane >= 16 && lane < 24) ? F_XR + (unsigned)((L * 2 + tau) * 2 + fph) * 64 + (lane & 3) * 4 : OOB;
  int fl = 0;
  for (int s = 0; s < NSEG; ++s) {
    unsigned char* lred = lds + (s & 1) * 16 * KB;
    STAMP(s, 0);
    {
      const int need = (lane < 24 && fph == (lane < 16 ? (s + L * LAG) % NPH : (s & 1))) ? s + 1 : 0;
      POLL(__builtin_amdgcn_ballot_w64(fl < need) != 0ull, fl = (int)ld4c(c.rs, fo), 1);
      CBAR();
      load_u(s);
    }
    STAMP(s, 1);
    CBAR();
    load_e(s);
    load_s(s);
    fl = (int)ld4c(c.rs, fo);
    POLL(anysent(mx4(mx4(mx4(mx4(mx2(mx4(mx4(mx4(mx4(0u, e[0]), e[1]), e[2]), e[3]), es), sv[0]), sv[1]), sv[2]), sv[3])),
         { load_e(s); load_s(s); fl = (int)ld4c(c.rs, fo); }, PO);
    SB();
    {
      f4 a = u2f(sv[0]) + u2f(sv[2]), b = u2f(sv[1]) + u2f(sv[3]);
      const float mt = __uint_as_float(es.x), lt = __uint_as_float(es.y);
      const float mm = mxf4(b, mxf4(a, mt));
      const float ml = mm * LOG2E;
#pragma unroll
      for (int r = 0; r < 4; ++r) { a[r] = ex2(a[r] * LOG2E - ml); b[r] = ex2(b[r] * LOG2E - ml); }
      const float at = ex2(mt * LOG2E - ml);
      const float den = ((a[0] + a[1]) + (a[2] + a[3])) + ((b[0] + b[1]) + (b[2] + b[3])) + at * lt;
      const float inv = rcp(den);
      const v4u P = pk8(a * inv, b * inv);
      float wt[4];
      rbcast4(at * inv, wt);
      f4 acc[4];
#pragma unroll
      for (int hh = 0; hh < 4; ++hh) {
        v4u en;
#pragma unroll
        for (int r = 0; r < 4; ++r) en[r] = pk2(bflo(e[hh][r]) * wt[hh], bfhi(e[hh][r]) * wt[hh]);
#pragma unroll
        for (int k = 0; k < 4; ++k) acc[k] = mfma32(U[k * 4 + hh], en, hh ? acc[k] : Z4);
      }
      SB();
      POLL(anysent(mx4(mx4(mx4(mx4(0u, z[0]), z[1]), z[2]), z[3])), { load_z(s); fl = (int)ld4c(c.rs, fo); }, PZ);
      TOUCH(fl);
      STAMP(s, 2);
#pragma unroll
      for (int k = 0; k < 4; ++k) acc[k] = mfma32(z[k], P, acc[k]);
#pragma unroll
      for (int k = 0; k < 4; ++k) LDSF4(lred + (k * 4 + w) * KB + lane * 16) = acc[k];
    }
    BAR();
    {
      const unsigned char* rp = lred + w * 4 * KB + lane * 16;
      const f4 acc = (LDSF4(rp) + LDSF4(rp + KB)) + (LDSF4(rp + 2 * KB) + LDSF4(rp + 3 * KB));
      const v2u y = pk4(acc);
      const v2u o = v2u{pk2(bflo(xr.x) + bflo(y.x), bfhi(xr.x) + bfhi(y.x)), pk2(bflo(xr.y) + bflo(y.y), bfhi(xr.y) + bfhi(y.y))};
      if (L == 0) {
        st8c(c.rs, rec_off(0, s) + R_X2 + po, ns2(o));
        st8c(c.rs, rec_in(0, s + AD) + R_X2 + po, S2);
      } else {
        *(v2u*)((unsigned char*)out + (size_t)(s * 32 + tok_of(tau, i16)) * 896 + (ct * 16 + 4 * q) * 2) = o;
      }
    }
    STAMP(s, 3);
  }
}

template <int L>
DEVI void role_AH(Ctx& c, int a, unsigned char* lds) {
  PMASK(9);
  asm volatile("; layer %2" : "+v"(c.tid), "+v"(c.lane) : "n"(L) : "memory");
  const int lane = c.lane, w = c.wave;
  const int i16 = lane & 15, q = lane >> 4;
  unsigned char* lK = lds;
  unsigned char* lV = lds + 18 * KB;
  unsigned char* lQ = lds + 36 * KB;
  unsigned char* lmg = lds + 38 * KB;
  unsigned char* lms = lds + 42 * KB;
  const int tau = w & 1, g = w >> 1;
  v4u x[14];
  auto load_x = [&](int s) {
    const unsigned xb = (L == 0 ? X0_OFF + (unsigned)s * SEGX : rec_off(0, s) + R_X2) + (unsigned)(tau * 14) * KB + lane * 16;
#pragma unroll
    for (int f = 0; f < 14; ++f) x[f] = (L == 0) ? ld16(c.rs, xb + f * KB) : ld16c(c.rs, xb + f * KB);
  };
  auto xbad = [&]() {
    unsigned m = 0u;
#pragma unroll
    for (int f = 0; f < 14; ++f) m = mx4(m, x[f]);
    return anysent(m);
  };
  load_x(0);
  v4u W[42];
#pragma unroll
  for (int tl = 0; tl < 3; ++tl) {
    const unsigned b0 = WQ_OFF + (unsigned)(a * 6 + (tl < 2 ? 2 * g + tl : 4 + g)) * 14 * KB + lane * 16;
#pragma unroll
    for (int k = 0; k < 14; ++k) W[tl * 14 + k] = ld16(c.rs, b0 + k * KB);
  }
  for (int o = c.tid * 16; o < 36 * (int)KB; o += 4096) LDS16(lK + o) = v4u{0u, 0u, (o < 18 * (int)KB && q == 3) ? 0xFF7Fu : 0u, 0u};
  f4 cs0 = Z4, cs1 = Z4, cs2 = Z4;
  {
    const v4u one = {0x3F803F80u, 0x3F803F80u, 0x3F803F80u, 0x3F803F80u};
#pragma unroll
    for (int k = 0; k < 14; ++k) { cs0 = mfma32(W[k], one, cs0); cs1 = mfma32(W[14 + k], one, cs1); cs2 = mfma32(one, W[28 + k], cs2); }
  }
#pragma unroll
  for (int f = 0; f < 42; ++f) PIN_A(W[f]);
  const float csv = cs2[0];
  const float vone = (g == 1 && i16 == 12) ? 1.0f : 0.0f;
  const float qone = (g == 0 && q == 3) ? 1.0f : 0.0f;
  const float sc = 0.18898223650461362f * LOG2E;
  const int lim = tok_of(tau, i16) - 8 * q;
  v4u dm;
#pragma unroll
  for (int e = 0; e < 4; ++e) dm[e] = ((2 * e <= lim) ? 0xFFFFu : 0u) | ((2 * e + 1 <= lim) ? 0xFFFF0000u : 0u);
  const int c0 = 28 * a + 4 * q, c1 = c0 + 16;
  const unsigned oa = (g == 0) ? R_OA + (unsigned)tau * 14 * KB + (c0 >> 5) * KB + (((c0 >> 3) & 3) * 16 + i16) * 16 + (c0 & 4) * 2 : OOB;
  const unsigned ob = (g == 0 && q < 3) ? R_OA + (unsigned)tau * 14 * KB + (c1 >> 5) * KB + (((c1 >> 3) & 3) * 16 + i16) * 16 + (c1 & 4) * 2 : OOB;
  CBAR();
#pragma unroll
  for (int f = 0; f < 14; ++f) TOUCH(x[f]);
  BAR();
  int slot = 0;
  for (int s = 0; s < NSEG; ++s) {
    STAMP(s, 0);
    v4u kf[10], vf[10];
    int sl[4];
#pragma unroll
    for (int b = 0; b < 4; ++b) {
      int k = slot - ((b == 0 && g == 0) ? 8 : 4 * g + b);
      k += (k < 0) ? 9 : 0;
      sl[b] = k;
      kf[2 * b] = LDS16(lK + (k * 2) * KB + lane * 16); kf[2 * b + 1] = LDS16(lK + (k * 2 + 1) * KB + lane * 16);
    }
    if (L == 1) POLL(xbad(), load_x(s), 2);
    STAMP(s, 1);
    SB();
    {
      f4 a0 = Z4, a1 = Z4, a2 = Z4, gm = Z4;
#pragma unroll
      for (int f = 0; f < 14; ++f) {
        gm = mfma32(x[f], x[f], gm);
        a1 = mfma32(W[14 + f], x[f], a1);
        SB();
      }
      float mean, rstd, mr;
      f4 rs, ms;
      float sum, sq;
      f4 su, sv;
#pragma unroll
      for (int f = 0; f < 14; ++f) {
        a0 = mfma32(W[f], x[f], a0);
        a2 = mfma32(x[f], W[28 + f], a2);
        if (f == 1) {
          const float d = (i16 & 2) ? ((i16 & 1) ? gm[3] : gm[2]) : ((i16 & 1) ? gm[1] : gm[0]);
          const int di = __float_as_int(d), si = __float_as_int(a1[1]);
          sq = __int_as_float(__builtin_amdgcn_ds_bpermute(((i16 >> 2) * 16 + i16) * 4, di));
          sum = __int_as_float(__builtin_amdgcn_ds_bpermute((48 + i16) * 4, si));
#pragma unroll
          for (int r = 0; r < 4; ++r) {
            sv[r] = __int_as_float(__builtin_amdgcn_ds_bpermute((20 * q + r) * 4, di));
            su[r] = __int_as_float(__builtin_amdgcn_ds_bpermute((48 + 4 * q + r) * 4, si));
          }
        }
        if (f == 7) {
          mean = sum * (1.0f / 448.0f);
          rstd = rsq(fmaxf(sq * (1.0f / 448.0f) - mean * mean, 0.f) + 1e-5f);
          mr = mean * rstd;
        }
        if (f >= 8 && f < 12) {
          const int r = f - 8;
          const float mn = su[r] * (1.0f / 448.0f);
          rs[r] = rsq(fmaxf(sv[r] * (1.0f / 448.0f) - mn * mn, 0.f) + 1e-5f);
          ms[r] = mn * rs[r];
        }
        SB();
      }
      a0 = a0 * rstd - cs0 * mr;
      a1 = a1 * rstd - cs1 * mr;
      a1[0] += qone;
      a1[1] = (q == 3) ? 0.f : a1[1];
      LDS16((g == 0 ? lQ + tau * KB : lK + (slot * 2 + tau) * KB) + lane * 16) = pk8(a0, a1);
      a2 = a2 * rs - ms * csv + vone;
      *(v2u*)(lV + (slot * 2 + g) * KB + lane * 16 + tau * 8) = pk4(a2);
    }
    BAR();
    STAMP(s, 2);
    {
      const v4u qf = LDS16(lQ + tau * KB + lane * 16);
      if (g == 0) { kf[8] = LDS16(lK + (slot * 2) * KB + lane * 16); kf[9] = LDS16(lK + (slot * 2 + 1) * KB + lane * 16); }
      SB();
      f4 scv[4][2];
#pragma unroll
      for (int b = 0; b < 4; ++b) {
        scv[b][0] = mfma32(kf[2 * b], qf, Z4);
        scv[b][1] = mfma32(kf[2 * b + 1], qf, Z4);
      }
      f4 e0 = Z4, e1 = Z4;
      if (g == 0) { e0 = mfma32(kf[8], qf, Z4); e1 = mfma32(kf[9], qf, Z4); }
      SB();
#pragma unroll
      for (int b = 0; b < 4; ++b) { vf[2 * b] = LDS16(lV + (sl[b] * 2) * KB + lane * 16); vf[2 * b + 1] = LDS16(lV + (sl[b] * 2 + 1) * KB + lane * 16); }
      if (g == 0) { vf[8] = LDS16(lV + (slot * 2) * KB + lane * 16); vf[9] = LDS16(lV + (slot * 2 + 1) * KB + lane * 16); }
      SB();
#pragma unroll
      for (int b = 0; b < 4; ++b) { PIN_V(scv[b][0]); PIN_V(scv[b][1]); }
      if (g == 0) {
        PIN_V(e0); PIN_V(e1);
#pragma unroll
        for (int r = 0; r < 4; ++r) {
          scv[0][0][r] = (r <= lim) ? e0[r] : scv[0][0][r];
          scv[0][1][r] = (4 + r <= lim) ? e1[r] : scv[0][1][r];
        }
      }
      float m = -1.0e30f;
#pragma unroll
      for (int b = 0; b < 4; ++b) m = mxf4(scv[b][1], mxf4(scv[b][0], m));
      m = rmax4(m);
      const float ml = m * sc;
      f4 o0 = Z4, o1 = Z4;
      const unsigned xn = (L == 0 ? X0_OFF + (unsigned)min(s + 1, NSEG - 1) * SEGX : rec_off(0, min(s + 1, NSEG - 1)) + R_X2) + (unsigned)(tau * 14) * KB + lane * 16;
#pragma unroll
      for (int bb = 0; bb < 4; ++bb) {
        const int b = 3 - bb;
#pragma unroll
        for (int r = 0; r < 4; ++r) {
          scv[b][0][r] = ex2(scv[b][0][r] * sc - ml);
          scv[b][1][r] = ex2(scv[b][1][r] * sc - ml);
          if (bb * 4 + r < 14) {
            SB();
            x[bb * 4 + r] = (L == 0) ? ld16(c.rs, xn + (bb * 4 + r) * KB) : ld16c(c.rs, xn + (bb * 4 + r) * KB);
            SB();
          }
        }
        v4u p = pk8(scv[b][0], scv[b][1]);
        if (b == 0 && g == 0) {
          const v4u pd = p & dm;
          p = p & ~dm;
          o0 = mfma32(vf[8], pd, o0);
          o1 = mfma32(vf[9], pd, o1);
        }
        o0 = mfma32(vf[2 * b], p, o0);
        o1 = mfma32(vf[2 * b + 1], p, o1);
      }
      if (g == 1) {
        LDSF4(lmg + (tau * 2 + 0) * KB + lane * 16) = o0;
        LDSF4(lmg + (tau * 2 + 1) * KB + lane * 16) = o1;
        *(float*)(lms + (tau * 64 + lane) * 4) = m;
      }
      BAR();
      v2u y0 = S2, y1 = S2;
      if (g == 0) {
        const float mo = *(const float*)(lms + (tau * 64 + lane) * 4);
        const float mt = mx3(m, mo, NEG);
        const float fa = ex2((m - mt) * sc), fb = ex2((mo - mt) * sc);
        const f4 r0 = o0 * fa + LDSF4(lmg + (tau * 2 + 0) * KB + lane * 16) * fb;
        const f4 r1 = o1 * fa + LDSF4(lmg + (tau * 2 + 1) * KB + lane * 16) * fb;
        float dn[4];
        rbcast4(r1[0], dn);
        const float inv = rcp(dn[3]);
        y0 = ns2(pk4(r0 * inv));
        y1 = ns2(pk4(r1 * inv));
      }
      st8c(c.rs, rec_off(L, s) + oa, y0);
      st8c(c.rs, rec_off(L, s) + ob, y1);
      st8c(c.rs, (s + AD < NSEG) ? rec_off(L, s + AD) + oa : OOB, S2);
      st8c(c.rs, (s + AD < NSEG) ? rec_off(L, s + AD) + ob : OOB, S2);
      STAMP(s, 3);
    }
    slot = (slot == 8) ? 0 : slot + 1;
  }
}

template <int l>
DEVI void role_AO(Ctx& c, int tau, int par, unsigned char* lds) {
  PMASK(9);
  asm volatile("; layer %2" : "+v"(c.tid), "+v"(c.lane) : "n"(l) : "memory");
  const int lane = c.lane, w = c.wave;
  const int i16 = lane & 15, q = lane >> 4;
  unsigned char* lO = lds;
  unsigned char* lx = lds + 14 * KB;
  unsigned char* lst = lds + 18 * KB;
  v4u W[98];
  {
    const unsigned b0 = WO_OFF + (unsigned)(3 * w) * 28 * KB + lane * 16;
#pragma unroll
    for (int f = 0; f < 84; ++f) W[f] = ld16(c.rs, b0 + f * KB);
    const unsigned b1 = WO_OFF + (unsigned)(12 + (w >> 1)) * 28 * KB + (w & 1) * 7 * KB + lane * 16;
#pragma unroll
    for (int k = 0; k < 7; ++k) { W[84 + k] = ld16(c.rs, b1 + k * KB); W[91 + k] = ld16(c.rs, b1 + (14 + k) * KB); }
#pragma unroll
    for (int f = 0; f < 56; ++f) PIN_A(W[f]);
  }
  const int ux = 12 + (w >> 1), kx = 7 * (w & 1);
  const bool own4 = !(w & 1);
  const unsigned fo = (lane == 0) ? F_XR + (unsigned)((l * 2 + tau) * 2 + par) * 64 + w * 4 : OOB;
  for (int s = par; s < NSEG; s += 2) {
    const unsigned rb = rec_off(l, s);
    STAMP(s, 0);
    {
      const unsigned ob = rb + R_OA + (unsigned)tau * 14 * KB;
      v4u v[4];
      auto ldo = [&]() {
        v[0] = ld16c(c.rs, ob + c.tid * 16); v[1] = ld16c(c.rs, ob + 4096 + c.tid * 16); v[2] = ld16c(c.rs, ob + 8192 + c.tid * 16);
        v[3] = ld16c(c.rs, ob + 12288 + (c.tid & 127) * 16);
      };
      CBAR();
      ldo();
      DRAIN();
      st4c(c.rs, fo, (unsigned)max(s - 1, 0));
      POLL(anysent(mx4(mx4(mx4(mx4(0u, v[0]), v[1]), v[2]), v[3])), ldo(), 1);
      LDS16(lO + c.tid * 16) = v[0];
      LDS16(lO + 4096 + c.tid * 16) = v[1];
      LDS16(lO + 8192 + c.tid * 16) = v[2];
      LDS16(lO + 12288 + (c.tid & 127) * 16) = v[3];
    }
    STAMP(s, 1);
    v4u xr[4];
    auto ldr = [&]() {
#pragma unroll
      for (int p = 0; p < 4; ++p) {
        const int u = (p < 3) ? 3 * w + p : ux;
        if (l == 0) xr[p] = ld16(c.rs, X0_OFF + (unsigned)s * SEGX + (unsigned)(tau * 14 + u) * KB + lane * 16);
        else xr[p] = ld16c(c.rs, rec_off(0, s) + R_X2 + (unsigned)(tau * 14 + u) * KB + lane * 16);
      }
    };
    BAR();
    f4 acc[8] = {Z4, Z4, Z4, Z4, Z4, Z4, Z4, Z4};
    {
      const unsigned char* xp = lO + lane * 16;
      v4u xs[7];
#pragma unroll
      for (int k = 0; k < 7; ++k) xs[k] = LDS16(xp + (kx + k) * KB);
      v4u y0 = LDS16(xp), y1 = LDS16(xp + KB);
      SB();
#pragma unroll
      for (int k = 0; k < 7; ++k) {
        acc[6] = mfma32(W[84 + k], xs[k], acc[6]);
        acc[7] = mfma32(W[91 + k], xs[k], acc[7]);
      }
      SB();
      ldr();
      SB();
#pragma unroll
      for (int ks = 0; ks < 14; ++ks) {
        const v4u x = y0;
        y0 = y1;
        if (ks + 2 < 14) y1 = LDS16(xp + (ks + 2) * KB);
        if (ks == 2 && !own4) { LDSF4(lx + ((w >> 1) * 2 + 0) * KB + lane * 16) = acc[6]; LDSF4(lx + ((w >> 1) * 2 + 1) * KB + lane * 16) = acc[7]; }
#pragma unroll
        for (int pp = 0; pp < 3; ++pp) {
          acc[pp * 2 + 0] = mfma32(W[pp * 28 + ks], x, acc[pp * 2 + 0]);
          acc[pp * 2 + 1] = mfma32(W[pp * 28 + 14 + ks], x, acc[pp * 2 + 1]);
        }
        SB();
      }
    }
    if (l == 1) POLL(anysent(mx4(mx4(mx4(mx4(0u, xr[0]), xr[1]), xr[2]), xr[3])), ldr(), 1);
    BAR();
    STAMP(s, 2);
    v4u xb[4];
    if (own4) {
      acc[6] += LDSF4(lx + ((w >> 1) * 2 + 0) * KB + lane * 16);
      acc[7] += LDSF4(lx + ((w >> 1) * 2 + 1) * KB + lane * 16);
    }
    float sum = 0.f, sq = 0.f;
#pragma unroll
    for (int p = 0; p < 4; ++p) {
      xb[p] = resid8(xr[p], acc[2 * p], acc[2 * p + 1]);
      if (p == 3 && !own4) xb[p] = v4u{0u, 0u, 0u, 0u};
#pragma unroll
      for (int e = 0; e < 4; ++e) { sum = dot2(xb[p][e], 0x3F803F80u, sum); sq = dot2(xb[p][e], xb[p][e], sq); }
    }
    sum = rsum4(sum);
    sq = rsum4(sq);
    if (q == 0) *(f2*)(lst + (w * 16 + i16) * 8) = f2{sum, sq};
    BAR();
    const f2 st = (*(const f2*)(lst + i16 * 8) + *(const f2*)(lst + (16 + i16) * 8)) + (*(const f2*)(lst + (32 + i16) * 8) + *(const f2*)(lst + (48 + i16) * 8));
    const float mean = st[0] * (1.0f / 448.0f);
    const float rstd = rsq(fmaxf(st[1] * (1.0f / 448.0f) - mean * mean, 0.f) + 1e-5f);
    const float mr = mean * rstd;
#pragma unroll
    for (int p = 0; p < 4; ++p)
      if (p < 3 || own4) {
        const int u = (p < 3) ? 3 * w + p : ux;
        const unsigned fo = (unsigned)(tau * 14 + u) * KB + lane * 16;
        v4u o;
#pragma unroll
        for (int e = 0; e < 4; ++e) o[e] = pk2(bflo(xb[p][e]) * rstd - mr, bfhi(xb[p][e]) * rstd - mr);
        st16c(c.rs, rb + R_XN + fo, ns4(o));
      }
#pragma unroll
    for (int p = 0; p < 4; ++p)
      if (p < 3 || own4) {
        const int u = (p < 3) ? 3 * w + p : ux;
        const unsigned fo = (unsigned)(tau * 14 + u) * KB + lane * 16;
        st16c(c.rs, rb + R_XR + fo, xb[p]);
        st16c(c.rs, rec_in(l, s + AD) + R_XN + fo, S4);
      }
    STAMP(s, 3);
  }
  DRAIN();
  st4c(c.rs, fo, (unsigned)NSEG);
}

DEVI unsigned ring_offset(const unsigned char* ws) {
  const unsigned lo = (unsigned)((size_t)ws & (2u * MB - 1u));
  return ((lo + FLAG_BYTES + 2u * MB - 1u) & ~(2u * MB - 1u)) - lo;
}

__global__ void __launch_bounds__(256, 1) mega_kernel(unsigned char* ws, const unsigned char* mem0, __hip_bfloat16* out) {
  extern __shared__ __attribute__((aligned(16))) unsigned char lds[];
  Ctx c;
  c.ws = ws;
  c.rs = __builtin_amdgcn_make_buffer_rsrc(ws, 0, WS_END, 0x00020000);
  c.ring = ring_offset(ws);
  c.tid = threadIdx.x;
  c.lane = threadIdx.x & 63;
  c.wave = __builtin_amdgcn_readfirstlane(threadIdx.x >> 6);
  c.dead = false;
  const int b = blockIdx.x, x = b & 7, r = b >> 3;
  PMASK(1);
  STAMP(127, 0);
  {
    const unsigned char* pc = (const unsigned char*)(__builtin_amdgcn_s_getpc() & ~(unsigned long long)255);
    const unsigned o = (unsigned)(r * 256 + threadIdx.x) * 16;
    if (o < CPF * KB) { const v4u v = *(const v4u*)(pc + o); TOUCH(v); }
  }
  if (x == 0) {
    role_H(c, r >> 1, r & 1, lds);
  } else if (x < 3) {
    if (r < 28) role_CV(c, r, x - 1, lds, mem0);
  } else if (x < 5) {
    if (r & 1) role_GX<1>(c, r >> 1, x - 3, lds); else role_GX<0>(c, r >> 1, x - 3, lds);
  } else if (x == 5) {
    if (r < 14) role_O<0>(c, r / 7, r % 7, lds, out); else if (r < 28) role_O<1>(c, (r - 14) / 7, (r - 14) % 7, lds, out);
  } else if (x == 6) {
    if (r >> 4) role_AH<1>(c, r & 15, lds); else role_AH<0>(c, r & 15, lds);
  } else {
    if (r < 4) role_AO<0>(c, r & 1, r >> 1, lds); else if (r < 8) role_AO<1>(c, r & 1, (r >> 1) & 1, lds); else if (r < 16) probe_side(c, r - 8);
  }
}

#define PAUX 16
#define PST(off, v) __builtin_amdgcn_raw_buffer_store_b128(v, rs, off, 0, PAUX)
__global__ void __launch_bounds__(512) prologue_kernel(unsigned char* ws, const unsigned short* x0, const unsigned short* wqkv, const unsigned short* wo,
                                                       const unsigned short* mem0, const unsigned short* Wq, const unsigned short* Wk,
                                                       const unsigned short* Wv, const unsigned short* Wo) {
  extern __shared__ __attribute__((aligned(16))) unsigned char lb[];
  const int b = blockIdx.x, tid = threadIdx.x, lane = tid & 63, w = __builtin_amdgcn_readfirstlane(tid >> 6);
  const int i16 = lane & 15, q = lane >> 4;
  const rsrc_t rs = __builtin_amdgcn_make_buffer_rsrc(ws, 0, WS_END, 0x00020000);
  const bool prod = b < 224;
  const int kk = (b >> 3) / 7, cg = (b >> 3) % 7;
  const bool isG = kk < 2;
  const int h = (2 * (b & 7) + kk) & 15;
  auto stage_b = [&](const v4u* bv) {
    if (isG) {
#pragma unroll
      for (int k = 0; k < 4; ++k) LDS16(lb + (16 * k + (tid >> 5)) * 528 + (tid & 31) * 16) = bv[k];
    } else {
      unsigned char* p = lb + 8 * (tid & 7) * 528 + (tid >> 3) * 8;
#pragma unroll
      for (int k = 0; k < 4; ++k) {
        *(v2u*)(p + (2 * k) * 528) = v2u{__builtin_amdgcn_perm(bv[1][k], bv[0][k], 0x05040100u), __builtin_amdgcn_perm(bv[3][k], bv[2][k], 0x05040100u)};
        *(v2u*)(p + (2 * k + 1) * 528) = v2u{__builtin_amdgcn_perm(bv[1][k], bv[0][k], 0x07060302u), __builtin_amdgcn_perm(bv[3][k], bv[2][k], 0x07060302u)};
      }
    }
    BAR();
  };
  v4u bv[4];
  if (prod) {
    const unsigned short* bp = isG ? Wq + ((size_t)(64 * cg + (tid >> 5)) * 16 + h) * 256 + (tid & 31) * 8
                                   : Wo + ((size_t)h * 256 + 4 * (tid >> 3)) * 448 + 64 * cg + 8 * (tid & 7);
    const unsigned bs = isG ? 16 * 4096 : 448;
#pragma unroll
    for (int k = 0; k < 4; ++k) bv[k] = *(const v4u*)(bp + (size_t)k * bs);
  }
  if (prod && w < 7) {
    const unsigned short* Wa = isG ? Wk : Wv;
    v4u A[4][8];
#pragma unroll
    for (int mt = 0; mt < 4; ++mt)
#pragma unroll
      for (int k = 0; k < 8; ++k) {
        const int n = 2 * k + (lane >> 5), d = 64 * w + 32 * (mt >> 1) + 8 * (n >> 2) + 4 * (mt & 1) + (n & 3);
        A[mt][k] = *(const v4u*)(Wa + ((size_t)d * 16 + h) * 256 + (lane & 31) * 8);
      }
    stage_b(bv);
    unsigned char* la = lb + (64 + 16 * w) * 528;
    const float sc = isG ? 0.0625f : 1.0f;
#pragma unroll
    for (int mt = 0; mt < 4; ++mt) {
#pragma unroll
      for (int k = 0; k < 8; ++k) LDS16(la + (2 * k + (lane >> 5)) * 528 + (lane & 31) * 16) = A[mt][k];
      CBAR();
#pragma unroll
      for (int es = 0; es < 8; ++es) A[mt][es] = LDS16(la + i16 * 528 + es * 64 + q * 16);
      CBAR();
    }
#pragma unroll
    for (int nt = 0; nt < 4; ++nt) {
      const int row = isG ? 32 * (nt >> 1) + 8 * (i16 >> 2) + 4 * (nt & 1) + (i16 & 3) : 16 * nt + i16;
      f4 a[4] = {Z4, Z4, Z4, Z4};
#pragma unroll
      for (int es = 0; es < 8; ++es) {
        const v4u bf = LDS16(lb + row * 528 + es * 64 + q * 16);
#pragma unroll
        for (int mt = 0; mt < 4; ++mt) a[mt] = mfma32(A[mt][es], bf, a[mt]);
      }
      const unsigned off = isG ? WG_OFF + (unsigned)(((h * 14 + 2 * cg + (nt >> 1)) * 2 + (nt & 1)) * 14 + 2 * w) * KB
                               : WU_OFF + (unsigned)(((4 * cg + nt) * 16 + h) * 14 + 2 * w) * KB;
      PST(off + lane * 16, pk8(a[0] * sc, a[1] * sc));
      PST(off + KB + lane * 16, pk8(a[2] * sc, a[3] * sc));
    }
  } else {
    constexpr int NW = 224 + 32 * 8;
    const int wid = prod ? b : 224 + (b - 224) * 8 + w;
    unsigned short gv[4][8];
    v4u xv[4];
#pragma unroll
    for (int it = 0; it < 4; ++it) {
      const int f = min(wid + it * NW, 1735), ks = f % 14;
      const unsigned short* src;
      unsigned stride;
      bool ok = true;
      unsigned short pad = 0;
      if (f < 1344) {
        const int tl = (f / 14) % 6, a = f / 84;
        const int dh = (tl < 4) ? 8 * (i16 >> 2) + 4 * (tl & 1) + (i16 & 3) : 16 * (tl - 4) + i16;
        ok = dh < 28;
        pad = (tl < 4 && dh == 29) ? 0x3F80 : 0;
        src = wqkv + ((tl < 4) ? (tl >> 1) * 448 : 896) + 28 * a + min(dh, 27);
        stride = 1344;
      } else {
        const int g = f - 1344;
        src = wo + 32 * (g / 28) + 8 * (i16 >> 2) + 4 * ((g / 14) & 1) + (i16 & 3);
        stride = 448;
      }
      src += (size_t)(32 * ks + 8 * q) * stride;
#pragma unroll
      for (int jj = 0; jj < 8; ++jj) gv[it][jj] = src[(size_t)jj * stride];
      if (!ok) {
#pragma unroll
        for (int jj = 0; jj < 8; ++jj) gv[it][jj] = pad;
      }
    }
#pragma unroll
    for (int it = 0; it < 4; ++it) {
      const int f = min(wid + it * NW, 1791), sg = f / 28, tau = (f / 14) & 1, ks = f % 14;
      xv[it] = *(const v4u*)(x0 + (size_t)(sg * 32 + tok_of(tau, i16)) * 448 + 32 * ks + 8 * q);
    }
    SB();
    if (prod) stage_b(bv);
    const unsigned gtid = wid * 64 + lane, gstride = NW * 64;
    for (unsigned idx = gtid; idx < FLAG_BYTES / 16; idx += gstride) PST(idx * 16, (v4u{0u, 0u, 0u, 0u}));
    for (unsigned idx = gtid; idx < 2 * AD * REC / 16; idx += gstride) {
      const unsigned l = idx / (AD * REC / 16), r = idx % (AD * REC / 16);
      PST(REC_OFF + l * NSEG * REC + r * 16, S4);
    }
#pragma unroll
    for (int it = 0; it < 4; ++it) {
      const int f = wid + it * NW;
      if (f < 1736) {
        v4u o;
#pragma unroll
        for (int jj = 0; jj < 4; ++jj) o[jj] = (unsigned)gv[it][2 * jj] | ((unsigned)gv[it][2 * jj + 1] << 16);
        PST(WQ_OFF + (unsigned)f * KB + lane * 16, o);
      }
      if (f < 1792) PST(X0_OFF + (unsigned)f * KB + lane * 16, xv[it]);
    }
    const unsigned ring = ring_offset(ws);
    for (unsigned idx = gstride - 1 - gtid; idx < RING_BYTES / 16; idx += gstride) {
      const unsigned po = idx * 16, lo = ((po >> 14) << 13) | (po & 8191u);
      v4u o = S4;
      if (lo < L_MEMG && (lo & (64 * KB - 1)) < 14 * KB) {
        const unsigned r = lo & (64 * KB - 1), ln = (r >> 4) & 63, ks = r >> 10;
        o = ns4(*(const v4u*)(mem0 + (size_t)(ln & 7) * 448 + 32 * ks + 8 * (ln >> 4)));
      } else if (lo >= L_MEMG && lo < L_MEMG + 16 * KB && (lo & 8191u) < 7 * KB) {
        const unsigned r = lo & 8191u, ks = r >> 9, qq = (r >> 7) & 3, row = (r >> 4) & 7;
        o = ns4(*(const v4u*)(mem0 + (size_t)row * 448 + 32 * ks + 8 * qq));
      }
      PST(ring + po, o);
    }
  }
}

extern "C" void kernel_launch(void* const* d_in, const int* in_sizes, int n_in, void* d_out, int out_size, void* d_ws, size_t ws_size,
                              hipStream_t stream) {
  constexpr int LDS_BYTES = 100 * 1024;
  (void)hipFuncSetAttribute((const void*)mega_kernel, hipFuncAttributeMaxDynamicSharedMemorySize, LDS_BYTES);
  unsigned char* ws = (unsigned char*)d_ws;
  constexpr int PRO_LDS = (64 + 7 * 16) * 528;
  (void)hipFuncSetAttribute((const void*)prologue_kernel, hipFuncAttributeMaxDynamicSharedMemorySize, PRO_LDS);
  prologue_kernel<<<256, 512, PRO_LDS, stream>>>(ws, (const unsigned short*)d_in[0], (const unsigned short*)d_in[1], (const unsigned short*)d_in[2], (const unsigned short*)d_in[4],
                                          (const unsigned short*)d_in[5], (const unsigned short*)d_in[6], (const unsigned short*)d_in[7],
                                          (const unsigned short*)d_in[8]);
  mega_kernel<<<256, 256, LDS_BYTES, stream>>>(ws, (const unsigned char*)d_in[4], (__hip_bfloat16*)d_out);
}
```
